```python
import torch

def _softmax(x, axis=-1):
    return torch.softmax(x, dim=axis)
def _gelu(x):
    return torch.nn.functional.gelu(x, approximate="tanh")
def _silu(x):
    return torch.nn.functional.silu(x)
def _sigmoid(x):
    return torch.sigmoid(x)
def _layernorm(x, axis=-1, eps=1e-5):
    m = x.mean(dim=axis, keepdim=True)
    v = x.var(dim=axis, keepdim=True, unbiased=False)
    return (x - m) / torch.sqrt(v + eps)
def _scatter_add(tgt, idx, src, axis=0):
    return tgt.clone().index_add_(axis, idx, src)
def _segment_sum(src, seg, n):
    out = torch.zeros((n,) + src.shape[1:], dtype=src.dtype)
    return out.index_add_(0, seg, src)
def _pad(x, pw):
    flat = tuple(v for pair in reversed(pw) for v in pair)
    return torch.nn.functional.pad(x, flat)

kernel_name = 'parallel_sum__grouped_kv_mqa__glu_gated_reglu__prompt'

def setup_inputs():

    _w = (lambda a: (lambda t: t.to(torch.bfloat16) if t.is_floating_point() else t)(torch.as_tensor(a)))
    def _A_setup():
        B, S, D, H = 1, 2048, 1024, 32
        Dh = D // H
        return {
            "x": _w((lambda *s: torch.randn(*s, dtype=torch.bfloat16) * 0.05)(B, S, D)),
            "wq": _w((lambda *s: torch.randn(*s, dtype=torch.bfloat16) * 0.05)(D, H * Dh)),
            "wk": _w((lambda *s: torch.randn(*s, dtype=torch.bfloat16) * 0.05)(D, Dh)),
            "wv": _w((lambda *s: torch.randn(*s, dtype=torch.bfloat16) * 0.05)(D, Dh)),
            "wo": _w((lambda *s: torch.randn(*s, dtype=torch.bfloat16) * 0.05)(H * Dh, D)),
        }
    _A_in = _A_setup()

    def _B_setup():
        B, S, D, F = 1, 2048, 1024, 3584
        return {
            "x": _w((lambda *s: torch.randn(*s, dtype=torch.bfloat16) * 0.05)(B, S, D)),
            "W_gate": _w((lambda *s: torch.randn(*s, dtype=torch.bfloat16) * 0.8)(D, F) * (2.0 / D) ** 0.5),
            "W_up": _w((lambda *s: torch.randn(*s, dtype=torch.bfloat16) * 0.8)(D, F) * (2.0 / D) ** 0.5),
            "W_down": _w((lambda *s: torch.randn(*s, dtype=torch.bfloat16) * 0.8)(F, D) * (1.0 / F) ** 0.5),
        }
    _B_in = _B_setup()

    return {
        "A_x": _A_in["x"],
        "A_wq": _A_in["wq"],
        "A_wk": _A_in["wk"],
        "A_wv": _A_in["wv"],
        "A_wo": _A_in["wo"],
        "B_x": _B_in["x"],
        "B_W_gate": _B_in["W_gate"],
        "B_W_up": _B_in["W_up"],
        "B_W_down": _B_in["W_down"],
    }

def reference(A_x, A_wq, A_wk, A_wv, A_wo, B_x, B_W_gate, B_W_up, B_W_down):
    def _A_ref(x, wq, wk, wv, wo):
        B, S, D = x.shape
        H = 32
        Dh = D // H
        q = (lambda x, *d: x.permute(*d))((x @ wq).reshape(B, S, H, Dh), 0, 2, 1, 3)
        k = x @ wk
        v = x @ wv
        scores = torch.einsum("bhqd,bkd->bhqk", q, k) * (Dh ** -0.5)
        p = _softmax(scores, axis=-1)
        o = torch.einsum("bhqk,bkd->bhqd", p, v)
        return (lambda x, *d: x.permute(*d))(o, 0, 2, 1, 3).reshape(B, S, H * Dh) @ wo

    def _B_ref(x, W_gate, W_up, W_down):
        gate = torch.einsum("bsd,df->bsf", x, W_gate)
        up = torch.einsum("bsd,df->bsf", x, W_up)
        gate = gate * (lambda x, dt: x.to(dt))(gate > 0, gate.dtype)
        h = gate * up
        return torch.einsum("bsf,fd->bsd", h, W_down)

    x = A_x
    xn = _layernorm(x, axis=-1)
    return x + _A_ref(xn, A_wq, A_wk, A_wv, A_wo) + _B_ref(xn, B_W_gate, B_W_up, B_W_down)
```

```cpp
#include <hip/hip_runtime.h>
#include <hip/hip_bf16.h>
#define LAS __attribute__((address_space(3)))
typedef unsigned u32x4 __attribute__((ext_vector_type(4)));
typedef unsigned u32x2 __attribute__((ext_vector_type(2)));
typedef unsigned long long u64;
#define DI __device__ __forceinline__
DI u32x4 mk_srd(u64 p, unsigned bytes) { u32x4 r; r[0] = (unsigned)p; r[1] = (unsigned)(p >> 32) & 0xffffu; r[2] = bytes; r[3] = 0x20000u; return r; }
template <class T> DI T sel(bool c, T a, T b) { return c ? a : b; }
DI int fresh_lane() { int l; asm volatile("v_mbcnt_lo_u32_b32 %0, -1, 0\n\tv_mbcnt_hi_u32_b32 %0, -1, %0" : "=v"(l)); return l; }

__global__ __launch_bounds__(512, 2) void fused_block(const char* __restrict__ x, char* ws, const char* __restrict__ wq, const char* __restrict__ wk, const char* __restrict__ wv,
                                                      const char* __restrict__ wo, const char* __restrict__ wg, const char* __restrict__ wu, const char* __restrict__ wd,
                                                      char* __restrict__ out) {
  __shared__ __attribute__((aligned(1024))) char lds[163840];

  int tid = threadIdx.x; unsigned b = blockIdx.x;
  unsigned pfc = tid * 128u;
  {
    pfc += sel(tid < 32, 0u, (b >> 3) * 1920u);
    u64 pc;
    asm volatile("s_getpc_b64 %1\n.Lfb_pc:\n\tv_min_u32 %0, .Lfb_end-.Lfb_pc, %0\n\tv_cmp_gt_u32 vcc, 47, %2\n\ts_nop 1\n\tv_cndmask_b32 %0, 0, %0, vcc\n\tglobal_load_dword %0, %0, %1"
                 : "+v"(pfc), "=&s"(pc) : "v"(tid) : "vcc", "memory");
  }
  u32x4 x0, x1; u32x2 ev;
  asm volatile("global_load_dwordx4 %0, %4, %5 \n\tglobal_load_dwordx4 %1, %4, %5 offset:1024 \n\tglobal_load_dwordx2 %2, %6, %7 sc1"
               : "=&v"(x0), "=&v"(x1), "=&v"(ev), "+v"(tid)
               : "v"((((b & 2) * 2 + ((b >> 3) & 3)) * 256 + (((b & 1) * 2 + ((b >> 2) & 1)) * 8 + (b >> 5)) * 8) * 2048u + (tid >> 6) * 2048u + (tid & 63) * 16u),
                 "s"(x), "v"(9441280u), "s"(ws), "s"((LAS char*)lds) : "memory");
  asm volatile("" : "+s"(b));
  const u64 aq = (u64)wq, ak = (u64)wk, av = (u64)wv, ao = (u64)wo, ag = (u64)wg, au = (u64)wu, ad = (u64)wd;
  const u32x4 srdWS = mk_srd((u64)ws, 33554432u);
  unsigned taglo_, taghi_, vKVsrc; u64 wsrc, vWst, pfa; u32x4 srdKW;
  {
    const unsigned lane = tid & 63, w = tid >> 6, xcd = b & 7, jj = b >> 3, pm = (xcd & 2) * 2 + (jj & 3), xp2 = (xcd & 1) * 2 + (xcd >> 2), pn = xp2 * 8 + (jj >> 2);
    const unsigned row = w * 64 + (xcd & 2) * 16 + jj, l5 = lane & 31;
    const unsigned og = row * 7168u + xp2 * 1792u + l5 * 128u, ou = og - 14u * 128u, oq = row * 2048u + xp2 * 512u + (l5 - 28u) * 128u;
    pfa = sel(l5 < 14, ag, sel(l5 < 28, au, aq)) + sel(l5 < 14, og, sel(l5 < 28, ou, oq));
    const unsigned vSw = 65536u + ((lane >> 2) * 4 + (lane & 1) * 2) * 144u + w * 16 + ((lane >> 1) & 1) * 8, vSr = 65536u + (tid >> 3) * 144u + (tid & 7) * 16u;
    const unsigned vSo = (pm * 8 + (pn >> 2)) * 65536u + (tid >> 3) * 512u + ((pn & 3) * 8 + (tid & 7)) * 16u;
    const unsigned par = (pn & 1), ob = (pn >> 1) * 448u, w10 = __builtin_amdgcn_readfirstlane(w) << 10;
    const unsigned wrow = w * 2 + (lane >> 5), gi_l = (lane >> 4) & 1, cs = ((lane >> 1) & 7) ^ (wrow & 7), hf = lane & 1;
    const unsigned wo_q = wrow * 2048u + pn * 64 + gi_l * 32 + hf * 16, wo_s = 192u + hf * 32 + par * 16, wo_f = par * 256u + cs * 32 + hf * 16;
    const unsigned wo_g = wrow * 7168u + ob + sel(cs == 6, wo_s, wo_f);
    vWst = sel(cs == 7, 16u * 2048u, 16u * 7168u);
    wsrc = sel(cs == 7, aq, sel(gi_l != 0, au, ag)) + sel(cs == 7, wo_q, wo_g) - vWst;
    srdKW = mk_srd(sel((jj & 4) != 0, av, ak), ((xcd & 1) ^ 1) << 16);
    vKVsrc = (lane >> 2) * 64u + (((lane & 3) ^ ((lane >> 3) & 2)) * 16);

  asm volatile(
      "s_waitcnt vmcnt(0)\n\t"
      "s_cmp_gt_u32 %[w10], 0\n\t"
      "s_cbranch_scc1 P0_NOP1_%=\n\t"
      "global_load_dword %[pf], %[pfa], off sc1\n\t"
      "P0_NOP1_%=:\n\t"
      "v_readfirstlane_b32 s56, %[x0]\n\t"
      "v_readfirstlane_b32 s50, %[e0]\n\t"
      "v_readfirstlane_b32 s51, %[e1]\n\t"
      "v_lshlrev_b32 v64, 16, %[x0]\n\t"
      "v_and_b32 v65, 0xffff0000, %[x0]\n\t"
      "v_lshlrev_b32 v66, 16, %[x1]\n\t"
      "v_and_b32 v67, 0xffff0000, %[x1]\n\t"
      "v_lshlrev_b32 v68, 16, %[x2]\n\t"
      "v_and_b32 v69, 0xffff0000, %[x2]\n\t"
      "v_lshlrev_b32 v70, 16, %[x3]\n\t"
      "v_and_b32 v71, 0xffff0000, %[x3]\n\t"
      "v_lshlrev_b32 v72, 16, %[x4]\n\t"
      "v_and_b32 v73, 0xffff0000, %[x4]\n\t"
      "v_lshlrev_b32 v74, 16, %[x5]\n\t"
      "v_and_b32 v75, 0xffff0000, %[x5]\n\t"
      "v_lshlrev_b32 v76, 16, %[x6]\n\t"
      "v_and_b32 v77, 0xffff0000, %[x6]\n\t"
      "v_lshlrev_b32 v78, 16, %[x7]\n\t"
      "v_and_b32 v79, 0xffff0000, %[x7]\n\t"
      "s_lshl_b32 s56, s56, 16\n\t"
      "s_mov_b32 s57, s56\n\t"
      "v_pk_add_f32 v[64:65], v[64:65], s[56:57] neg_lo:[0,1] neg_hi:[0,1]\n\t"
      "v_pk_add_f32 v[66:67], v[66:67], s[56:57] neg_lo:[0,1] neg_hi:[0,1]\n\t"
      "v_pk_add_f32 v[68:69], v[68:69], s[56:57] neg_lo:[0,1] neg_hi:[0,1]\n\t"
      "v_pk_add_f32 v[70:71], v[70:71], s[56:57] neg_lo:[0,1] neg_hi:[0,1]\n\t"
      "v_pk_add_f32 v[72:73], v[72:73], s[56:57] neg_lo:[0,1] neg_hi:[0,1]\n\t"
      "v_pk_add_f32 v[74:75], v[74:75], s[56:57] neg_lo:[0,1] neg_hi:[0,1]\n\t"
      "v_pk_add_f32 v[76:77], v[76:77], s[56:57] neg_lo:[0,1] neg_hi:[0,1]\n\t"
      "v_pk_add_f32 v[78:79], v[78:79], s[56:57] neg_lo:[0,1] neg_hi:[0,1]\n\t"
      "v_pk_mul_f32 v[104:105], v[64:65], v[64:65]\n\t"
      "v_pk_mul_f32 v[106:107], v[68:69], v[68:69]\n\t"
      "v_pk_mul_f32 v[108:109], v[72:73], v[72:73]\n\t"
      "v_pk_mul_f32 v[110:111], v[76:77], v[76:77]\n\t"
      "v_pk_add_f32 v[96:97], v[64:65], v[66:67]\n\t"
      "v_pk_add_f32 v[98:99], v[68:69], v[70:71]\n\t"
      "v_pk_add_f32 v[100:101], v[72:73], v[74:75]\n\t"
      "v_pk_add_f32 v[102:103], v[76:77], v[78:79]\n\t"
      "v_pk_fma_f32 v[104:105], v[66:67], v[66:67], v[104:105]\n\t"
      "v_pk_fma_f32 v[106:107], v[70:71], v[70:71], v[106:107]\n\t"
      "v_pk_fma_f32 v[108:109], v[74:75], v[74:75], v[108:109]\n\t"
      "v_pk_fma_f32 v[110:111], v[78:79], v[78:79], v[110:111]\n\t"
      "v_pk_add_f32 v[96:97], v[96:97], v[100:101]\n\t"
      "v_pk_add_f32 v[98:99], v[98:99], v[102:103]\n\t"
      "v_pk_add_f32 v[104:105], v[104:105], v[108:109]\n\t"
      "v_pk_add_f32 v[106:107], v[106:107], v[110:111]\n\t"
      "v_pk_add_f32 v[96:97], v[96:97], v[98:99]\n\t"
      "v_pk_add_f32 v[104:105], v[104:105], v[106:107]\n\t"
      "v_add_f32 v96, v96, v97\n\t"
      "v_add_f32 v104, v104, v105\n\t"
      "s_mul_i32 s50, s50, 0x9E3779B1\n\t"
      "v_add_f32_dpp v96, v96, v96 quad_perm:[1,0,3,2] row_mask:0xf bank_mask:0xf\n\t"
      "v_add_f32_dpp v104, v104, v104 quad_perm:[1,0,3,2] row_mask:0xf bank_mask:0xf\n\t"
      "s_add_u32 s50, s50, 0x7F4A7C15\n\t"
      "v_add_f32_dpp v96, v96, v96 quad_perm:[2,3,0,1] row_mask:0xf bank_mask:0xf\n\t"
      "v_add_f32_dpp v104, v104, v104 quad_perm:[2,3,0,1] row_mask:0xf bank_mask:0xf\n\t"
      "s_mul_i32 s51, s51, 0x85EBCA75\n\t"
      "v_add_f32_dpp v96, v96, v96 row_half_mirror row_mask:0xf bank_mask:0xf\n\t"
      "v_add_f32_dpp v104, v104, v104 row_half_mirror row_mask:0xf bank_mask:0xf\n\t"
      "s_add_u32 s51, s51, 0x165667B1\n\t"
      "v_add_f32_dpp v96, v96, v96 row_mirror row_mask:0xf bank_mask:0xf\n\t"
      "v_add_f32_dpp v104, v104, v104 row_mirror row_mask:0xf bank_mask:0xf\n\t"
      "v_mov_b32 v97, v96\n\t"
      "v_mov_b32 v105, v104\n\t"
      "s_mov_b32 %[taglo], s50\n\t"
      "v_permlane16_swap_b32 v96, v97\n\t"
      "v_permlane16_swap_b32 v104, v105\n\t"
      "s_nop 0\n\t"
      "v_add_f32 v96, v96, v97\n\t"
      "v_add_f32 v104, v104, v105\n\t"
      "v_mov_b32 v97, v96\n\t"
      "v_mov_b32 v105, v104\n\t"
      "s_mov_b32 %[taghi], s51\n\t"
      "v_permlane32_swap_b32 v96, v97\n\t"
      "v_permlane32_swap_b32 v104, v105\n\t"
      "s_nop 0\n\t"
      "v_add_f32 v96, v96, v97\n\t"
      "v_add_f32 v104, v104, v105\n\t"
      "v_mul_f32 v96, 0x3a800000, v96\n\t"
      "v_mul_f32 v104, 0x3a800000, v104\n\t"
      "v_fma_f32 v104, -v96, v96, v104\n\t"
      "v_add_f32 v96, s56, v96\n\t"
      "v_max_f32 v104, 0, v104\n\t"
      "v_cvt_pk_bf16_f32 v96, v96, v96\n\t"
      "v_lshlrev_b32 v96, 16, v96\n\t"
      "v_sub_f32 v96, s56, v96\n\t"
      "v_pk_add_f32 v[64:65], v[64:65], v[96:97] op_sel_hi:[1,0]\n\t"
      "v_pk_add_f32 v[66:67], v[66:67], v[96:97] op_sel_hi:[1,0]\n\t"
      "v_pk_add_f32 v[68:69], v[68:69], v[96:97] op_sel_hi:[1,0]\n\t"
      "v_pk_add_f32 v[70:71], v[70:71], v[96:97] op_sel_hi:[1,0]\n\t"
      "v_pk_add_f32 v[72:73], v[72:73], v[96:97] op_sel_hi:[1,0]\n\t"
      "v_pk_add_f32 v[74:75], v[74:75], v[96:97] op_sel_hi:[1,0]\n\t"
      "v_pk_add_f32 v[76:77], v[76:77], v[96:97] op_sel_hi:[1,0]\n\t"
      "v_pk_add_f32 v[78:79], v[78:79], v[96:97] op_sel_hi:[1,0]\n\t"
      "v_cvt_pk_bf16_f32 v104, v104, v104\n\t"
      "v_lshlrev_b32 v104, 16, v104\n\t"
      "v_add_f32 v104, 0x3727c5ac, v104\n\t"
      "v_cvt_pk_bf16_f32 v104, v104, v104\n\t"
      "v_lshlrev_b32 v104, 16, v104\n\t"
      "v_sqrt_f32 v104, v104\n\t"
      "v_cvt_pk_bf16_f32 v112, v64, v65\n\t"
      "v_cvt_pk_bf16_f32 v113, v66, v67\n\t"
      "v_cvt_pk_bf16_f32 v114, v68, v69\n\t"
      "v_cvt_pk_bf16_f32 v115, v70, v71\n\t"
      "v_cvt_pk_bf16_f32 v116, v72, v73\n\t"
      "v_cvt_pk_bf16_f32 v117, v74, v75\n\t"
      "v_cvt_pk_bf16_f32 v118, v76, v77\n\t"
      "v_cvt_pk_bf16_f32 v119, v78, v79\n\t"
      "v_cvt_pk_bf16_f32 v104, v104, v104\n\t"
      "v_lshlrev_b32 v104, 16, v104\n\t"
      "v_rcp_f32 v104, v104\n\t"
      "v_lshlrev_b32 v64, 16, v112\n\t"
      "v_and_b32 v65, 0xffff0000, v112\n\t"
      "v_lshlrev_b32 v66, 16, v113\n\t"
      "v_and_b32 v67, 0xffff0000, v113\n\t"
      "v_lshlrev_b32 v68, 16, v114\n\t"
      "v_and_b32 v69, 0xffff0000, v114\n\t"
      "v_lshlrev_b32 v70, 16, v115\n\t"
      "v_and_b32 v71, 0xffff0000, v115\n\t"
      "v_lshlrev_b32 v72, 16, v116\n\t"
      "v_and_b32 v73, 0xffff0000, v116\n\t"
      "v_lshlrev_b32 v74, 16, v117\n\t"
      "v_and_b32 v75, 0xffff0000, v117\n\t"
      "v_lshlrev_b32 v76, 16, v118\n\t"
      "v_and_b32 v77, 0xffff0000, v118\n\t"
      "v_lshlrev_b32 v78, 16, v119\n\t"
      "v_and_b32 v79, 0xffff0000, v119\n\t"
      "v_pk_mul_f32 v[64:65], v[64:65], v[104:105] op_sel_hi:[1,0]\n\t"
      "v_pk_mul_f32 v[66:67], v[66:67], v[104:105] op_sel_hi:[1,0]\n\t"
      "v_pk_mul_f32 v[68:69], v[68:69], v[104:105] op_sel_hi:[1,0]\n\t"
      "v_pk_mul_f32 v[70:71], v[70:71], v[104:105] op_sel_hi:[1,0]\n\t"
      "v_pk_mul_f32 v[72:73], v[72:73], v[104:105] op_sel_hi:[1,0]\n\t"
      "v_pk_mul_f32 v[74:75], v[74:75], v[104:105] op_sel_hi:[1,0]\n\t"
      "v_pk_mul_f32 v[76:77], v[76:77], v[104:105] op_sel_hi:[1,0]\n\t"
      "v_pk_mul_f32 v[78:79], v[78:79], v[104:105] op_sel_hi:[1,0]\n\t"
      "v_cvt_pk_bf16_f32 v112, v64, v65\n\t"
      "v_cvt_pk_bf16_f32 v113, v66, v67\n\t"
      "v_cvt_pk_bf16_f32 v114, v68, v69\n\t"
      "v_cvt_pk_bf16_f32 v115, v70, v71\n\t"
      "v_cvt_pk_bf16_f32 v116, v72, v73\n\t"
      "v_cvt_pk_bf16_f32 v117, v74, v75\n\t"
      "v_cvt_pk_bf16_f32 v118, v76, v77\n\t"
      "v_cvt_pk_bf16_f32 v119, v78, v79\n\t"
      "ds_write_b64 %[vSw], v[112:113] offset:0\n\t"
      "ds_write_b64 %[vSw], v[114:115] offset:144\n\t"
      "ds_write_b64 %[vSw], v[116:117] offset:9216\n\t"
      "ds_write_b64 %[vSw], v[118:119] offset:9360\n\t"
      "s_waitcnt lgkmcnt(0)\n\t"
      "s_barrier\n\t"
      "ds_read_b128 v[64:67], %[vSr]\n\t"
      "ds_read_b128 v[68:71], %[vSr] offset:9216\n\t"
      "s_mov_b32 s48, 32768\n\t"
      "s_waitcnt lgkmcnt(1)\n\t"
      "buffer_store_dwordx4 v[64:67], %[vSo], %[srdWS], 0 offen sc1\n\t"
      "s_waitcnt lgkmcnt(0)\n\t"
      "buffer_store_dwordx4 v[68:71], %[vSo], %[srdWS], s48 offen sc1\n\t"
      "s_nop 1\n\t"
      : [taglo] "=&s"(taglo_), [taghi] "=&s"(taghi_), [vA] "+v"(wsrc)
      : [srdWS] "s"(srdWS), [srdKW] "s"(srdKW), [w10] "s"(w10), [vWst] "v"(vWst), [vKVsrc] "v"(vKVsrc), [vSw] "v"(vSw), [vSr] "v"(vSr), [vSo] "v"(vSo), [pfa] "v"(pfa), [pf] "v"(pfc), [e0] "v"(ev[0]), [e1] "v"(ev[1]),
        [x0] "v"(x0[0]), [x1] "v"(x0[1]), [x2] "v"(x0[2]), [x3] "v"(x0[3]), [x4] "v"(x1[0]), [x5] "v"(x1[1]), [x6] "v"(x1[2]), [x7] "v"(x1[3])
      : "v64","v65","v66","v67","v68","v69","v70","v71","v72","v73","v74","v75","v76","v77","v78","v79","v80","v81","v82","v83","v84","v85","v86","v87","v88","v89","v90","v91","v92","v93","v94","v95","v96","v97","v98","v99","v100","v101","v102","v103","v104","v105","v106","v107","v108","v109","v110","v111","v112","v113","v114","v115","v116","v117","v118","v119","v120","v121","v122","v123","v124","v125","v126","v127","s40","s41","s42","s43","s44","s45","s46","s47","s48","s49","s50","s51","s52","s53","s54","s55","s56","s57","s58","s59","vcc","scc","memory");

  }
  asm volatile("" : "+s"(b)); asm volatile("" : "+v"(tid));
  const unsigned taglo = __builtin_amdgcn_readfirstlane(taglo_), taghi = __builtin_amdgcn_readfirstlane(taghi_);
  const int lane = tid & 63;
  unsigned w = __builtin_amdgcn_readfirstlane(tid >> 6);
  unsigned vL16, vWr0, vWr1, vOut, vXr, vKVr, vXkv, vKVw, vKVo, vKi, vPF;
#define BLOCK_IDS const unsigned xcd = b & 7, jj = b >> 3, pm = (xcd & 2) * 2 + (jj & 3), c8 = jj >> 2, xp2 = (xcd & 1) * 2 + (xcd >> 2), pn = xp2 * 8 + c8, par = (pn & 1), prow = (xcd & 2) * 16 + jj; \
  const unsigned wn = ((w >> 1) + 3) & 3, wt = w & 1; const bool w3 = wn == 3;      \
  const unsigned isV = pn & 1, ktb = (pn >> 1) & 7; \
  const unsigned kks = w & 1, kct = (w >> 1) & 1, ktt = w >> 2;      \
  const bool pgu = (w & 2) == 0, pd = (w & 3) == 2, pq = (w & 3) == 3;      \
  const unsigned pld = sel(pgu, 7168u, 2048u), sPFs = sel(pgu, 64u * 7168u, sel(pd, 2048u, 64u * 2048u)), sMW = w * 1024u;
#define LANE_PF const unsigned pnl = sel(pgu, 14u, sel(pd, 16u, 4u)), pfar = (64u + prow) * pld; \
  \
  vPF = sel((unsigned)lane < pnl, lane * 128u + sel(pd, 0u, pfar), 0x80000000u);
  {
    BLOCK_IDS
    const unsigned g = lane >> 4, q = (lane >> 2) & 3, p = lane & 3, rho = (g & 1) * 4 + q, r16 = lane & 15, sF0 = 9502720u + (pm * 32 + pn) * 128u;
    vL16 = lane * 16;
    vWr0 = (g * 4 + q) * 512u + (((2 * wn) ^ rho) * 32) + sel(w3, (p & 1) * 16 + (p >> 1) * 8, p * 8);
    vWr1 = (g * 4 + q) * 512u + (((2 * wn + 1) ^ rho) * 32) + p * 8;
    vOut = g * 512u + r16 * 16u; vXr = 65536u + wt * 16384u + vOut;
    vKVr = 131072u + kks * 2048u + (g * 4 + q) * 64u + ((kct ^ (g & 1)) * 32) + p * 8;
    vXkv = 65536u + ktb * 4096u + kks * 2048u + ktt * 256u + vOut;
    vKVw = 155648u + kks * 4096u + sel(isV != 0, ktt * 2048u + kct * 1024u + ((r16 >> 2) * 16 + 4 * g) * 16u + (r16 & 3) * 4u, kct * 2048u + ktt * 1024u + vL16);
    vKVo = 8388608u + ((w >> 1) << 30) + (pm * 8 + ktb) * 4096u + isV * 2048u + g * 512u + ((r16 >> 3) * 16 + (w & 1) * 8 + (r16 & 7)) * 16u;
    vKi = ((((lane >> 1) & 15) * 4 + (w & 3)) * 4 + (lane >> 5) * 2 + (lane & 1)) * 32u;

    pfa = sel(w <= 2, pfa, sel((lane & 2) != 0, av, ak) + (b * 256u + (lane & 1) * 128u));

  asm volatile(
      "v_mbcnt_lo_u32_b32 v96, -1, 0\n\t"
      "v_lshlrev_b32 v96, 3, v96\n\t"
      "s_waitcnt vmcnt(0)\n\t"
      "s_barrier\n\t"
      "s_cmp_lg_u32 %[w10], 0\n\t"
      "s_cbranch_scc1 P0_NOF0_%=\n\t"
      "v_mov_b32 v104, %[taglo]\n\t"
      "v_mov_b32 v105, %[taghi]\n\t"
      "s_mov_b64 exec, 0xffff\n\t"
      "buffer_store_dwordx2 v[104:105], v96, %[srdWS], %[sF0] offen sc1\n\t"
      "s_mov_b64 exec, -1\n\t"
      "P0_NOF0_%=:\n\t"
      "s_add_u32 m0, %[w10], 0\n\t"
      "v_lshl_add_u64 %[vA], %[vWst], 0, %[vA]\n\t"
      "global_load_lds_dwordx4 %[vA], off\n\t"
      "s_add_u32 m0, %[w10], 8192\n\t"
      "v_lshl_add_u64 %[vA], %[vWst], 0, %[vA]\n\t"
      "global_load_lds_dwordx4 %[vA], off\n\t"
      "s_add_u32 m0, %[w10], 16384\n\t"
      "v_lshl_add_u64 %[vA], %[vWst], 0, %[vA]\n\t"
      "global_load_lds_dwordx4 %[vA], off\n\t"
      "s_add_u32 m0, %[w10], 24576\n\t"
      "v_lshl_add_u64 %[vA], %[vWst], 0, %[vA]\n\t"
      "global_load_lds_dwordx4 %[vA], off\n\t"
      "s_add_u32 m0, %[w10], 131072\n\t"
      "s_nop 0\n\t"
      "buffer_load_dwordx4 %[vKVsrc], %[srdKW], %[w10] offen lds\n\t"
      "s_cmp_le_u32 %[w10], 0\n\t"
      "s_cbranch_scc1 P0_NOP2_%=\n\t"
      "global_load_dword %[pf], %[pfa], off sc1\n\t"
      "P0_NOP2_%=:\n\t"
      : [vA] "+v"(wsrc)
      : [srdWS] "s"(srdWS), [srdKW] "s"(srdKW), [w10] "s"(sMW), [sF0] "s"(sF0), [taglo] "s"(taglo), [taghi] "s"(taghi), [vWst] "v"(vWst), [vKVsrc] "v"(vKVsrc), [pfa] "v"(pfa), [pf] "v"(pfc)
        , "v"(vL16), "v"(vWr0), "v"(vWr1), "v"(vOut), "v"(vXr), "v"(vKVr), "v"(vXkv), "v"(vKVw), "v"(vKVo), "v"(vKi)
      : "v96","v97","v98","v99","v100","v101","v102","v103","v104","v105","v106","v107","v108","v109","v110","v111","v112","v113","v114","v115","v116","v117","v118","v119","v120","v121","v122","v123","v124","v125","v126","v127","s40","s41","s42","s43","s44","s45","s46","s47","s48","s49","s50","s51","s52","s53","s54","s55","s56","s57","s58","s59","scc","memory");

  }
  asm volatile("" : "+s"(b)); asm volatile("" : "+s"(w));
  BLOCK_IDS
  LANE_PF
  const u32x4 srdWO = mk_srd(ao, sel(w >= 6, 1024u * 2048u, 0u));
  const unsigned sX0 = (pm * 8 + w) * 65536u - 4096u, sMX = 65536u + w * 4096u;
  const unsigned sO = 10485760u + (pm * 8 + wt * 4) * (288u * 1024u) + (pn >> 1) * 7u * 2048u + sel(w3, 6144u + par * 1024u, (par * 4 + wn) * 2048u);
  const unsigned vWlo = (unsigned)wsrc, vWhi = (unsigned)(wsrc >> 32);
  const unsigned sXd = (pm * 8 + ktb) * 65536u + 12u * 4096u + kks * 2048u + ktt * 256u, sKVst = 4u + isV * 12u;
  const unsigned kvno = w >> 1, sKVi = (pm * 8 + ktb) * 4 + isV * 2 + (w & 1);
  const unsigned sPkv = 9633792u + xcd * 8192u, sP0 = 9502720u + (pm * 32 + w * 4) * 128u;
  const unsigned sOa = 10485760u + (pm * 8 + 2 * w) * (288u * 1024u) + (112u + pn) * 2048u, sFO = 9830400u + ((pm * 4 + w) * 32 + pn) * 128u;
  const unsigned krot = (jj & 15) * 16384u;
  const u32x4 srdP = mk_srd(sel(pgu, sel((w & 1) == 0, ag, au), sel(pd, ad, aq)), sel(pq, 1024u * 2048u, 1024u * 7168u));
  const unsigned sPFo = (b + (w & 1)) * 8192u, sPF0 = sel(pd, b * 224u * 128u, sel(pgu, xp2 * 1792u, xp2 * 512u) + 128u * pld);

  asm volatile(
      "s_mov_b32 s40, %[sX0]\n\t"
      "s_mov_b32 s41, %[sMX]\n\t"
      "s_mov_b32 s42, %[sMW]\n\t"
      "s_mov_b32 s44, %[sO]\n\t"
      "s_mov_b32 s47, %[sPF0]\n\t"
      "s_add_u32 s45, %[sMW], 8192\n\t"
      "s_add_u32 s46, s45, 131072\n\t"
      "v_mov_b32 v50, %[vKVr]\n\t"
      "v_mov_b32 v48, %[vWlo]\n\t"
      "v_mov_b32 v49, %[vWhi]\n\t"
      "v_mov_b32 v60, 0\n\t"
      "v_mov_b32 v61, 0\n\t"
      "v_mov_b32 v62, 0\n\t"
      "v_mov_b32 v63, 0\n\t"
      "v_and_b32 v52, 48, %[vL16]\n\t"
      "v_lshlrev_b32 v52, 3, v52\n\t"
      "v_mfma_f32_32x32x16_bf16 v[128:143], v[60:63], v[60:63], 0\n\t"
      "v_mfma_f32_32x32x16_bf16 v[144:159], v[60:63], v[60:63], 0\n\t"
      "v_mfma_f32_32x32x16_bf16 v[160:175], v[60:63], v[60:63], 0\n\t"
      "v_mfma_f32_32x32x16_bf16 v[176:191], v[60:63], v[60:63], 0\n\t"
      "v_mfma_f32_32x32x16_bf16 v[192:207], v[60:63], v[60:63], 0\n\t"
      "v_mfma_f32_32x32x16_bf16 v[208:223], v[60:63], v[60:63], 0\n\t"
      "v_mfma_f32_32x32x16_bf16 v[224:239], v[60:63], v[60:63], 0\n\t"
      "v_mfma_f32_32x32x16_bf16 v[240:255], v[60:63], v[60:63], 0\n\t"
      "s_mov_b32 s50, %[taglo]\n\t"
      "s_mov_b32 s51, %[taghi]\n\t"
      "s_mov_b32 s49, 0\n\t"
      "P1_PL_%=:\n\t"
      "buffer_load_dwordx2 v[30:31], v52, %[srdWS], %[sP0] offen sc1\n\t"
      "s_waitcnt vmcnt(0)\n\t"
      "v_cmp_eq_u64 vcc, s[50:51], v[30:31]\n\t"
      "s_add_u32 s49, s49, 1\n\t"
      "s_cmp_eq_u64 vcc, -1\n\t"
      "s_cbranch_scc1 P1_PL_OK_%=\n\t"
      "s_cmp_lt_u32 s49, 0x40000\n\t"
      "s_cbranch_scc1 P1_PL_%=\n\t"
      "P1_PL_OK_%=:\n\t"
      "s_add_u32 m0, s41, 0\n\t"
      "s_add_u32 s40, s40, 4096\n\t"
      "buffer_load_dwordx4 %[vL16], %[srdWS], s40 offen offset:0 sc1 lds\n\t"
      "buffer_load_dwordx4 %[vL16], %[srdWS], s40 offen offset:1024 sc1 lds\n\t"
      "buffer_load_dwordx4 %[vL16], %[srdWS], s40 offen offset:2048 sc1 lds\n\t"
      "buffer_load_dwordx4 %[vL16], %[srdWS], s40 offen offset:3072 sc1 lds\n\t"
      "s_cmp_ge_u32 %[w], 4\n\t"
      "s_cbranch_scc1 P1_YOUNG_%=\n\t"
      "s_add_u32 m0, s42, 32768\n\t"
      "v_lshl_add_u64 v[48:49], %[vWst], 0, v[48:49]\n\t"
      "global_load_lds_dwordx4 v[48:49], off\n\t"
      "s_add_u32 m0, s42, 40960\n\t"
      "v_lshl_add_u64 v[48:49], %[vWst], 0, v[48:49]\n\t"
      "global_load_lds_dwordx4 v[48:49], off\n\t"
      "s_add_u32 m0, s42, 49152\n\t"
      "v_lshl_add_u64 v[48:49], %[vWst], 0, v[48:49]\n\t"
      "global_load_lds_dwordx4 v[48:49], off\n\t"
      "s_add_u32 m0, s42, 57344\n\t"
      "v_lshl_add_u64 v[48:49], %[vWst], 0, v[48:49]\n\t"
      "global_load_lds_dwordx4 v[48:49], off\n\t"
      "s_waitcnt vmcnt(4)\n\t"
      "s_barrier\n\t"
      "ds_read_b128 v[64:67], %[vXr] offset:0\n\t"
      "ds_read_b128 v[68:71], %[vXr] offset:256\n\t"
      "ds_read_b128 v[72:75], %[vXr] offset:4096\n\t"
      "ds_read_b64_tr_b16 v[96:97], %[vWr0] offset:0\n\t"
      "ds_read_b64_tr_b16 v[98:99], %[vWr0] offset:8192\n\t"
      "ds_read_b128 v[76:79], %[vXr] offset:4352\n\t"
      "ds_read_b64_tr_b16 v[100:101], %[vWr1] offset:0\n\t"
      "ds_read_b64_tr_b16 v[102:103], %[vWr1] offset:8192\n\t"
      "ds_read_b128 v[80:83], %[vXr] offset:8192\n\t"
      "ds_read_b64_tr_b16 v[104:105], %[vWr0] offset:256\n\t"
      "ds_read_b64_tr_b16 v[106:107], %[vWr0] offset:8448\n\t"
      "ds_read_b128 v[84:87], %[vXr] offset:8448\n\t"
      "ds_read_b64_tr_b16 v[108:109], %[vWr1] offset:256\n\t"
      "ds_read_b64_tr_b16 v[110:111], %[vWr1] offset:8448\n\t"
      "s_waitcnt lgkmcnt(0)\n\t"
      "s_cmp_eq_u32 %[light], 0\n\t"
      "s_cbranch_scc1 P1_HEAVYO_%=\n\t"
      "s_mov_b32 s43, 7\n\t"
      "P1_LIGHTO_%=:\n\t"
      "s_waitcnt lgkmcnt(10)\n\t"
      "v_mfma_f32_16x16x32_bf16 v[128:131], v[96:99], v[64:67], v[128:131]\n\t"
      "ds_read_b128 v[88:91], %[vXr] offset:12288\n\t"
      "s_waitcnt lgkmcnt(8)\n\t"
      "v_mfma_f32_16x16x32_bf16 v[132:135], v[100:103], v[64:67], v[132:135]\n\t"
      "ds_read_b64_tr_b16 v[112:113], %[vWr0] offset:16384\n\t"
      "v_mfma_f32_16x16x32_bf16 v[148:151], v[100:103], v[68:71], v[148:151]\n\t"
      "ds_read_b64_tr_b16 v[114:115], %[vWr0] offset:24576\n\t"
      "v_mfma_f32_16x16x32_bf16 v[144:147], v[96:99], v[68:71], v[144:147]\n\t"
      "s_add_u32 m0, s41, 32768\n\t"
      "s_add_u32 s40, s40, 4096\n\t"
      "buffer_load_dwordx4 %[vL16], %[srdWS], s40 offen offset:0 sc1 lds\n\t"
      "s_waitcnt lgkmcnt(7)\n\t"
      "v_mfma_f32_16x16x32_bf16 v[152:155], v[104:107], v[68:71], v[152:155]\n\t"
      "ds_read_b128 v[92:95], %[vXr] offset:12544\n\t"
      "s_waitcnt lgkmcnt(5)\n\t"
      "v_mfma_f32_16x16x32_bf16 v[156:159], v[108:111], v[68:71], v[156:159]\n\t"
      "ds_read_b64_tr_b16 v[116:117], %[vWr1] offset:16384\n\t"
      "v_mfma_f32_16x16x32_bf16 v[140:143], v[108:111], v[64:67], v[140:143]\n\t"
      "ds_read_b64_tr_b16 v[118:119], %[vWr1] offset:24576\n\t"
      "v_mfma_f32_16x16x32_bf16 v[136:139], v[104:107], v[64:67], v[136:139]\n\t"
      "v_mfma_f32_16x16x32_bf16 v[168:171], v[104:107], v[72:75], v[168:171]\n\t"
      "ds_read_b128 v[64:67], %[vXr] offset:2048\n\t"
      "v_mfma_f32_16x16x32_bf16 v[172:175], v[108:111], v[72:75], v[172:175]\n\t"
      "ds_read_b64_tr_b16 v[120:121], %[vWr0] offset:16640\n\t"
      "v_mfma_f32_16x16x32_bf16 v[188:191], v[108:111], v[76:79], v[188:191]\n\t"
      "ds_read_b64_tr_b16 v[122:123], %[vWr0] offset:24832\n\t"
      "v_mfma_f32_16x16x32_bf16 v[184:187], v[104:107], v[76:79], v[184:187]\n\t"
      "buffer_load_dwordx4 %[vL16], %[srdWS], s40 offen offset:1024 sc1 lds\n\t"
      "v_mfma_f32_16x16x32_bf16 v[176:179], v[96:99], v[76:79], v[176:179]\n\t"
      "ds_read_b128 v[68:71], %[vXr] offset:2304\n\t"
      "v_mfma_f32_16x16x32_bf16 v[180:183], v[100:103], v[76:79], v[180:183]\n\t"
      "ds_read_b64_tr_b16 v[124:125], %[vWr1] offset:16640\n\t"
      "v_mfma_f32_16x16x32_bf16 v[164:167], v[100:103], v[72:75], v[164:167]\n\t"
      "ds_read_b64_tr_b16 v[126:127], %[vWr1] offset:24832\n\t"
      "v_mfma_f32_16x16x32_bf16 v[160:163], v[96:99], v[72:75], v[160:163]\n\t"
      "v_mfma_f32_16x16x32_bf16 v[192:195], v[96:99], v[80:83], v[192:195]\n\t"
      "ds_read_b128 v[72:75], %[vXr] offset:6144\n\t"
      "v_mfma_f32_16x16x32_bf16 v[196:199], v[100:103], v[80:83], v[196:199]\n\t"
      "buffer_load_dwordx4 %[vL16], %[srdWS], s40 offen offset:2048 sc1 lds\n\t"
      "s_waitcnt lgkmcnt(13)\n\t"
      "v_mfma_f32_16x16x32_bf16 v[212:215], v[100:103], v[84:87], v[212:215]\n\t"
      "v_mfma_f32_16x16x32_bf16 v[208:211], v[96:99], v[84:87], v[208:211]\n\t"
      "v_mfma_f32_16x16x32_bf16 v[216:219], v[104:107], v[84:87], v[216:219]\n\t"
      "ds_read_b128 v[76:79], %[vXr] offset:6400\n\t"
      "v_mfma_f32_16x16x32_bf16 v[220:223], v[108:111], v[84:87], v[220:223]\n\t"
      "v_mfma_f32_16x16x32_bf16 v[204:207], v[108:111], v[80:83], v[204:207]\n\t"
      "v_mfma_f32_16x16x32_bf16 v[200:203], v[104:107], v[80:83], v[200:203]\n\t"
      "s_waitcnt lgkmcnt(13)\n\t"
      "v_mfma_f32_16x16x32_bf16 v[232:235], v[104:107], v[88:91], v[232:235]\n\t"
      "ds_read_b128 v[80:83], %[vXr] offset:10240\n\t"
      "v_mfma_f32_16x16x32_bf16 v[236:239], v[108:111], v[88:91], v[236:239]\n\t"
      "buffer_load_dwordx4 %[vL16], %[srdWS], s40 offen offset:3072 sc1 lds\n\t"
      "s_waitcnt lgkmcnt(11)\n\t"
      "v_mfma_f32_16x16x32_bf16 v[252:255], v[108:111], v[92:95], v[252:255]\n\t"
      "v_mfma_f32_16x16x32_bf16 v[248:251], v[104:107], v[92:95], v[248:251]\n\t"
      "v_mfma_f32_16x16x32_bf16 v[240:243], v[96:99], v[92:95], v[240:243]\n\t"
      "ds_read_b128 v[84:87], %[vXr] offset:10496\n\t"
      "v_mfma_f32_16x16x32_bf16 v[244:247], v[100:103], v[92:95], v[244:247]\n\t"
      "v_mfma_f32_16x16x32_bf16 v[228:231], v[100:103], v[88:91], v[228:231]\n\t"
      "v_mfma_f32_16x16x32_bf16 v[224:227], v[96:99], v[88:91], v[224:227]\n\t"
      "s_waitcnt lgkmcnt(9)\n\t"
      "v_mfma_f32_16x16x32_bf16 v[128:131], v[112:115], v[64:67], v[128:131]\n\t"
      "ds_read_b128 v[88:91], %[vXr] offset:14336\n\t"
      "v_mfma_f32_16x16x32_bf16 v[132:135], v[116:119], v[64:67], v[132:135]\n\t"
      "ds_read_b128 v[92:95], %[vXr] offset:14592\n\t"
      "s_waitcnt lgkmcnt(8)\n\t"
      "v_mfma_f32_16x16x32_bf16 v[148:151], v[116:119], v[68:71], v[148:151]\n\t"
      "v_mfma_f32_16x16x32_bf16 v[144:147], v[112:115], v[68:71], v[144:147]\n\t"
      "v_mfma_f32_16x16x32_bf16 v[152:155], v[120:123], v[68:71], v[152:155]\n\t"
      "s_waitcnt lgkmcnt(6)\n\t"
      "v_mfma_f32_16x16x32_bf16 v[156:159], v[124:127], v[68:71], v[156:159]\n\t"
      "v_mfma_f32_16x16x32_bf16 v[140:143], v[124:127], v[64:67], v[140:143]\n\t"
      "v_mfma_f32_16x16x32_bf16 v[136:139], v[120:123], v[64:67], v[136:139]\n\t"
      "s_waitcnt vmcnt(0) lgkmcnt(0)\n\t"
      "s_barrier\n\t"
      "v_mfma_f32_16x16x32_bf16 v[168:171], v[120:123], v[72:75], v[168:171]\n\t"
      "ds_read_b128 v[64:67], %[vXr] offset:32768\n\t"
      "v_mfma_f32_16x16x32_bf16 v[172:175], v[124:127], v[72:75], v[172:175]\n\t"
      "s_add_u32 m0, s42, 0\n\t"
      "v_lshl_add_u64 v[48:49], %[vWst], 0, v[48:49]\n\t"
      "global_load_lds_dwordx4 v[48:49], off\n\t"
      "v_mfma_f32_16x16x32_bf16 v[188:191], v[124:127], v[76:79], v[188:191]\n\t"
      "v_mfma_f32_16x16x32_bf16 v[184:187], v[120:123], v[76:79], v[184:187]\n\t"
      "v_mfma_f32_16x16x32_bf16 v[176:179], v[112:115], v[76:79], v[176:179]\n\t"
      "ds_read_b128 v[68:71], %[vXr] offset:33024\n\t"
      "v_mfma_f32_16x16x32_bf16 v[180:183], v[116:119], v[76:79], v[180:183]\n\t"
      "ds_read_b64_tr_b16 v[96:97], %[vWr0] offset:32768\n\t"
      "v_mfma_f32_16x16x32_bf16 v[164:167], v[116:119], v[72:75], v[164:167]\n\t"
      "ds_read_b64_tr_b16 v[98:99], %[vWr0] offset:40960\n\t"
      "v_mfma_f32_16x16x32_bf16 v[160:163], v[112:115], v[72:75], v[160:163]\n\t"
      "v_mfma_f32_16x16x32_bf16 v[192:195], v[112:115], v[80:83], v[192:195]\n\t"
      "ds_read_b128 v[72:75], %[vXr] offset:36864\n\t"
      "v_mfma_f32_16x16x32_bf16 v[196:199], v[116:119], v[80:83], v[196:199]\n\t"
      "ds_read_b64_tr_b16 v[100:101], %[vWr1] offset:32768\n\t"
      "v_mfma_f32_16x16x32_bf16 v[212:215], v[116:119], v[84:87], v[212:215]\n\t"
      "ds_read_b64_tr_b16 v[102:103], %[vWr1] offset:40960\n\t"
      "v_mfma_f32_16x16x32_bf16 v[208:211], v[112:115], v[84:87], v[208:211]\n\t"
      "s_add_u32 m0, s42, 8192\n\t"
      "v_lshl_add_u64 v[48:49], %[vWst], 0, v[48:49]\n\t"
      "global_load_lds_dwordx4 v[48:49], off\n\t"
      "v_mfma_f32_16x16x32_bf16 v[216:219], v[120:123], v[84:87], v[216:219]\n\t"
      "ds_read_b128 v[76:79], %[vXr] offset:37120\n\t"
      "v_mfma_f32_16x16x32_bf16 v[220:223], v[124:127], v[84:87], v[220:223]\n\t"
      "ds_read_b64_tr_b16 v[104:105], %[vWr0] offset:33024\n\t"
      "v_mfma_f32_16x16x32_bf16 v[204:207], v[124:127], v[80:83], v[204:207]\n\t"
      "ds_read_b64_tr_b16 v[106:107], %[vWr0] offset:41216\n\t"
      "v_mfma_f32_16x16x32_bf16 v[200:203], v[120:123], v[80:83], v[200:203]\n\t"
      "v_mfma_f32_16x16x32_bf16 v[232:235], v[120:123], v[88:91], v[232:235]\n\t"
      "ds_read_b128 v[80:83], %[vXr] offset:40960\n\t"
      "v_mfma_f32_16x16x32_bf16 v[236:239], v[124:127], v[88:91], v[236:239]\n\t"
      "ds_read_b64_tr_b16 v[108:109], %[vWr1] offset:33024\n\t"
      "v_mfma_f32_16x16x32_bf16 v[252:255], v[124:127], v[92:95], v[252:255]\n\t"
      "ds_read_b64_tr_b16 v[110:111], %[vWr1] offset:41216\n\t"
      "v_mfma_f32_16x16x32_bf16 v[248:251], v[120:123], v[92:95], v[248:251]\n\t"
      "s_add_u32 m0, s42, 16384\n\t"
      "v_lshl_add_u64 v[48:49], %[vWst], 0, v[48:49]\n\t"
      "global_load_lds_dwordx4 v[48:49], off\n\t"
      "v_mfma_f32_16x16x32_bf16 v[240:243], v[112:115], v[92:95], v[240:243]\n\t"
      "ds_read_b128 v[84:87], %[vXr] offset:41216\n\t"
      "v_mfma_f32_16x16x32_bf16 v[244:247], v[116:119], v[92:95], v[244:247]\n\t"
      "s_add_u32 m0, s42, 24576\n\t"
      "v_lshl_add_u64 v[48:49], %[vWst], 0, v[48:49]\n\t"
      "global_load_lds_dwordx4 v[48:49], off\n\t"
      "v_mfma_f32_16x16x32_bf16 v[228:231], v[116:119], v[88:91], v[228:231]\n\t"
      "v_mfma_f32_16x16x32_bf16 v[224:227], v[112:115], v[88:91], v[224:227]\n\t"
      "s_waitcnt lgkmcnt(10)\n\t"
      "v_mfma_f32_16x16x32_bf16 v[128:131], v[96:99], v[64:67], v[128:131]\n\t"
      "ds_read_b128 v[88:91], %[vXr] offset:45056\n\t"
      "s_waitcnt lgkmcnt(8)\n\t"
      "v_mfma_f32_16x16x32_bf16 v[132:135], v[100:103], v[64:67], v[132:135]\n\t"
      "ds_read_b64_tr_b16 v[112:113], %[vWr0] offset:49152\n\t"
      "v_mfma_f32_16x16x32_bf16 v[148:151], v[100:103], v[68:71], v[148:151]\n\t"
      "ds_read_b64_tr_b16 v[114:115], %[vWr0] offset:57344\n\t"
      "v_mfma_f32_16x16x32_bf16 v[144:147], v[96:99], v[68:71], v[144:147]\n\t"
      "s_add_u32 m0, s41, 0\n\t"
      "s_add_u32 s40, s40, 4096\n\t"
      "buffer_load_dwordx4 %[vL16], %[srdWS], s40 offen offset:0 sc1 lds\n\t"
      "s_waitcnt lgkmcnt(7)\n\t"
      "v_mfma_f32_16x16x32_bf16 v[152:155], v[104:107], v[68:71], v[152:155]\n\t"
      "ds_read_b128 v[92:95], %[vXr] offset:45312\n\t"
      "s_waitcnt lgkmcnt(5)\n\t"
      "v_mfma_f32_16x16x32_bf16 v[156:159], v[108:111], v[68:71], v[156:159]\n\t"
      "ds_read_b64_tr_b16 v[116:117], %[vWr1] offset:49152\n\t"
      "v_mfma_f32_16x16x32_bf16 v[140:143], v[108:111], v[64:67], v[140:143]\n\t"
      "ds_read_b64_tr_b16 v[118:119], %[vWr1] offset:57344\n\t"
      "v_mfma_f32_16x16x32_bf16 v[136:139], v[104:107], v[64:67], v[136:139]\n\t"
      "v_mfma_f32_16x16x32_bf16 v[168:171], v[104:107], v[72:75], v[168:171]\n\t"
      "ds_read_b128 v[64:67], %[vXr] offset:34816\n\t"
      "v_mfma_f32_16x16x32_bf16 v[172:175], v[108:111], v[72:75], v[172:175]\n\t"
      "ds_read_b64_tr_b16 v[120:121], %[vWr0] offset:49408\n\t"
      "v_mfma_f32_16x16x32_bf16 v[188:191], v[108:111], v[76:79], v[188:191]\n\t"
      "ds_read_b64_tr_b16 v[122:123], %[vWr0] offset:57600\n\t"
      "v_mfma_f32_16x16x32_bf16 v[184:187], v[104:107], v[76:79], v[184:187]\n\t"
      "buffer_load_dwordx4 %[vL16], %[srdWS], s40 offen offset:1024 sc1 lds\n\t"
      "v_mfma_f32_16x16x32_bf16 v[176:179], v[96:99], v[76:79], v[176:179]\n\t"
      "ds_read_b128 v[68:71], %[vXr] offset:35072\n\t"
      "v_mfma_f32_16x16x32_bf16 v[180:183], v[100:103], v[76:79], v[180:183]\n\t"
      "ds_read_b64_tr_b16 v[124:125], %[vWr1] offset:49408\n\t"
      "v_mfma_f32_16x16x32_bf16 v[164:167], v[100:103], v[72:75], v[164:167]\n\t"
      "ds_read_b64_tr_b16 v[126:127], %[vWr1] offset:57600\n\t"
      "v_mfma_f32_16x16x32_bf16 v[160:163], v[96:99], v[72:75], v[160:163]\n\t"
      "v_mfma_f32_16x16x32_bf16 v[192:195], v[96:99], v[80:83], v[192:195]\n\t"
      "ds_read_b128 v[72:75], %[vXr] offset:38912\n\t"
      "v_mfma_f32_16x16x32_bf16 v[196:199], v[100:103], v[80:83], v[196:199]\n\t"
      "buffer_load_dwordx4 %[vL16], %[srdWS], s40 offen offset:2048 sc1 lds\n\t"
      "s_waitcnt lgkmcnt(13)\n\t"
      "v_mfma_f32_16x16x32_bf16 v[212:215], v[100:103], v[84:87], v[212:215]\n\t"
      "v_mfma_f32_16x16x32_bf16 v[208:211], v[96:99], v[84:87], v[208:211]\n\t"
      "v_mfma_f32_16x16x32_bf16 v[216:219], v[104:107], v[84:87], v[216:219]\n\t"
      "ds_read_b128 v[76:79], %[vXr] offset:39168\n\t"
      "v_mfma_f32_16x16x32_bf16 v[220:223], v[108:111], v[84:87], v[220:223]\n\t"
      "v_mfma_f32_16x16x32_bf16 v[204:207], v[108:111], v[80:83], v[204:207]\n\t"
      "v_mfma_f32_16x16x32_bf16 v[200:203], v[104:107], v[80:83], v[200:203]\n\t"
      "s_waitcnt lgkmcnt(13)\n\t"
      "v_mfma_f32_16x16x32_bf16 v[232:235], v[104:107], v[88:91], v[232:235]\n\t"
      "ds_read_b128 v[80:83], %[vXr] offset:43008\n\t"
      "v_mfma_f32_16x16x32_bf16 v[236:239], v[108:111], v[88:91], v[236:239]\n\t"
      "buffer_load_dwordx4 %[vL16], %[srdWS], s40 offen offset:3072 sc1 lds\n\t"
      "s_waitcnt lgkmcnt(11)\n\t"
      "v_mfma_f32_16x16x32_bf16 v[252:255], v[108:111], v[92:95], v[252:255]\n\t"
      "v_mfma_f32_16x16x32_bf16 v[248:251], v[104:107], v[92:95], v[248:251]\n\t"
      "v_mfma_f32_16x16x32_bf16 v[240:243], v[96:99], v[92:95], v[240:243]\n\t"
      "ds_read_b128 v[84:87], %[vXr] offset:43264\n\t"
      "v_mfma_f32_16x16x32_bf16 v[244:247], v[100:103], v[92:95], v[244:247]\n\t"
      "v_mfma_f32_16x16x32_bf16 v[228:231], v[100:103], v[88:91], v[228:231]\n\t"
      "v_mfma_f32_16x16x32_bf16 v[224:227], v[96:99], v[88:91], v[224:227]\n\t"
      "s_waitcnt lgkmcnt(9)\n\t"
      "v_mfma_f32_16x16x32_bf16 v[128:131], v[112:115], v[64:67], v[128:131]\n\t"
      "ds_read_b128 v[88:91], %[vXr] offset:47104\n\t"
      "v_mfma_f32_16x16x32_bf16 v[132:135], v[116:119], v[64:67], v[132:135]\n\t"
      "ds_read_b128 v[92:95], %[vXr] offset:47360\n\t"
      "s_waitcnt lgkmcnt(8)\n\t"
      "v_mfma_f32_16x16x32_bf16 v[148:151], v[116:119], v[68:71], v[148:151]\n\t"
      "v_mfma_f32_16x16x32_bf16 v[144:147], v[112:115], v[68:71], v[144:147]\n\t"
      "v_mfma_f32_16x16x32_bf16 v[152:155], v[120:123], v[68:71], v[152:155]\n\t"
      "s_waitcnt lgkmcnt(6)\n\t"
      "v_mfma_f32_16x16x32_bf16 v[156:159], v[124:127], v[68:71], v[156:159]\n\t"
      "v_mfma_f32_16x16x32_bf16 v[140:143], v[124:127], v[64:67], v[140:143]\n\t"
      "v_mfma_f32_16x16x32_bf16 v[136:139], v[120:123], v[64:67], v[136:139]\n\t"
      "s_waitcnt vmcnt(0) lgkmcnt(0)\n\t"
      "s_barrier\n\t"
      "v_mfma_f32_16x16x32_bf16 v[168:171], v[120:123], v[72:75], v[168:171]\n\t"
      "ds_read_b128 v[64:67], %[vXr] offset:0\n\t"
      "v_mfma_f32_16x16x32_bf16 v[172:175], v[124:127], v[72:75], v[172:175]\n\t"
      "s_add_u32 m0, s42, 32768\n\t"
      "v_lshl_add_u64 v[48:49], %[vWst], 0, v[48:49]\n\t"
      "global_load_lds_dwordx4 v[48:49], off\n\t"
      "v_mfma_f32_16x16x32_bf16 v[188:191], v[124:127], v[76:79], v[188:191]\n\t"
      "v_mfma_f32_16x16x32_bf16 v[184:187], v[120:123], v[76:79], v[184:187]\n\t"
      "v_mfma_f32_16x16x32_bf16 v[176:179], v[112:115], v[76:79], v[176:179]\n\t"
      "ds_read_b128 v[68:71], %[vXr] offset:256\n\t"
      "v_mfma_f32_16x16x32_bf16 v[180:183], v[116:119], v[76:79], v[180:183]\n\t"
      "ds_read_b64_tr_b16 v[96:97], %[vWr0] offset:0\n\t"
      "v_mfma_f32_16x16x32_bf16 v[164:167], v[116:119], v[72:75], v[164:167]\n\t"
      "ds_read_b64_tr_b16 v[98:99], %[vWr0] offset:8192\n\t"
      "v_mfma_f32_16x16x32_bf16 v[160:163], v[112:115], v[72:75], v[160:163]\n\t"
      "v_mfma_f32_16x16x32_bf16 v[192:195], v[112:115], v[80:83], v[192:195]\n\t"
      "ds_read_b128 v[72:75], %[vXr] offset:4096\n\t"
      "v_mfma_f32_16x16x32_bf16 v[196:199], v[116:119], v[80:83], v[196:199]\n\t"
      "ds_read_b64_tr_b16 v[100:101], %[vWr1] offset:0\n\t"
      "v_mfma_f32_16x16x32_bf16 v[212:215], v[116:119], v[84:87], v[212:215]\n\t"
      "ds_read_b64_tr_b16 v[102:103], %[vWr1] offset:8192\n\t"
      "v_mfma_f32_16x16x32_bf16 v[208:211], v[112:115], v[84:87], v[208:211]\n\t"
      "s_add_u32 m0, s42, 40960\n\t"
      "v_lshl_add_u64 v[48:49], %[vWst], 0, v[48:49]\n\t"
      "global_load_lds_dwordx4 v[48:49], off\n\t"
      "v_mfma_f32_16x16x32_bf16 v[216:219], v[120:123], v[84:87], v[216:219]\n\t"
      "ds_read_b128 v[76:79], %[vXr] offset:4352\n\t"
      "v_mfma_f32_16x16x32_bf16 v[220:223], v[124:127], v[84:87], v[220:223]\n\t"
      "ds_read_b64_tr_b16 v[104:105], %[vWr0] offset:256\n\t"
      "v_mfma_f32_16x16x32_bf16 v[204:207], v[124:127], v[80:83], v[204:207]\n\t"
      "ds_read_b64_tr_b16 v[106:107], %[vWr0] offset:8448\n\t"
      "v_mfma_f32_16x16x32_bf16 v[200:203], v[120:123], v[80:83], v[200:203]\n\t"
      "v_mfma_f32_16x16x32_bf16 v[232:235], v[120:123], v[88:91], v[232:235]\n\t"
      "ds_read_b128 v[80:83], %[vXr] offset:8192\n\t"
      "v_mfma_f32_16x16x32_bf16 v[236:239], v[124:127], v[88:91], v[236:239]\n\t"
      "ds_read_b64_tr_b16 v[108:109], %[vWr1] offset:256\n\t"
      "v_mfma_f32_16x16x32_bf16 v[252:255], v[124:127], v[92:95], v[252:255]\n\t"
      "ds_read_b64_tr_b16 v[110:111], %[vWr1] offset:8448\n\t"
      "v_mfma_f32_16x16x32_bf16 v[248:251], v[120:123], v[92:95], v[248:251]\n\t"
      "s_add_u32 m0, s42, 49152\n\t"
      "v_lshl_add_u64 v[48:49], %[vWst], 0, v[48:49]\n\t"
      "global_load_lds_dwordx4 v[48:49], off\n\t"
      "v_mfma_f32_16x16x32_bf16 v[240:243], v[112:115], v[92:95], v[240:243]\n\t"
      "ds_read_b128 v[84:87], %[vXr] offset:8448\n\t"
      "v_mfma_f32_16x16x32_bf16 v[244:247], v[116:119], v[92:95], v[244:247]\n\t"
      "s_add_u32 m0, s42, 57344\n\t"
      "v_lshl_add_u64 v[48:49], %[vWst], 0, v[48:49]\n\t"
      "global_load_lds_dwordx4 v[48:49], off\n\t"
      "v_mfma_f32_16x16x32_bf16 v[228:231], v[116:119], v[88:91], v[228:231]\n\t"
      "v_mfma_f32_16x16x32_bf16 v[224:227], v[112:115], v[88:91], v[224:227]\n\t"
      "s_sub_u32 s43, s43, 1\n\t"
      "s_cmp_lg_u32 s43, 0\n\t"
      "s_cbranch_scc1 P1_LIGHTO_%=\n\t"
      "s_branch P1_T14O_%=\n\t"
      "P1_HEAVYO_%=:\n\t"
      "s_add_u32 m0, s42, 147456\n\t"
      "s_add_u32 s48, s42, 57344\n\t"
      "buffer_load_dwordx4 %[vKVsrc], %[srdKW], s48 offen lds\n\t"
      "s_mov_b32 s43, 5\n\t"
      "P1_LOOPO_%=:\n\t"
      "s_waitcnt lgkmcnt(10)\n\t"
      "v_mfma_f32_16x16x32_bf16 v[128:131], v[96:99], v[64:67], v[128:131]\n\t"
      "ds_read_b128 v[88:91], %[vXr] offset:12288\n\t"
      "s_waitcnt lgkmcnt(8)\n\t"
      "v_mfma_f32_16x16x32_bf16 v[132:135], v[100:103], v[64:67], v[132:135]\n\t"
      "ds_read_b64_tr_b16 v[112:113], %[vWr0] offset:16384\n\t"
      "v_mfma_f32_16x16x32_bf16 v[148:151], v[100:103], v[68:71], v[148:151]\n\t"
      "ds_read_b64_tr_b16 v[114:115], %[vWr0] offset:24576\n\t"
      "v_mfma_f32_16x16x32_bf16 v[144:147], v[96:99], v[68:71], v[144:147]\n\t"
      "s_add_u32 m0, s41, 32768\n\t"
      "s_add_u32 s40, s40, 4096\n\t"
      "buffer_load_dwordx4 %[vL16], %[srdWS], s40 offen offset:0 sc1 lds\n\t"
      "s_waitcnt lgkmcnt(7)\n\t"
      "v_mfma_f32_16x16x32_bf16 v[152:155], v[104:107], v[68:71], v[152:155]\n\t"
      "ds_read_b128 v[92:95], %[vXr] offset:12544\n\t"
      "s_waitcnt lgkmcnt(5)\n\t"
      "v_mfma_f32_16x16x32_bf16 v[156:159], v[108:111], v[68:71], v[156:159]\n\t"
      "ds_read_b64_tr_b16 v[116:117], %[vWr1] offset:16384\n\t"
      "v_mfma_f32_16x16x32_bf16 v[140:143], v[108:111], v[64:67], v[140:143]\n\t"
      "ds_read_b64_tr_b16 v[118:119], %[vWr1] offset:24576\n\t"
      "v_mfma_f32_16x16x32_bf16 v[136:139], v[104:107], v[64:67], v[136:139]\n\t"
      "v_mfma_f32_16x16x32_bf16 v[168:171], v[104:107], v[72:75], v[168:171]\n\t"
      "ds_read_b128 v[64:67], %[vXr] offset:2048\n\t"
      "v_mfma_f32_16x16x32_bf16 v[172:175], v[108:111], v[72:75], v[172:175]\n\t"
      "ds_read_b64_tr_b16 v[120:121], %[vWr0] offset:16640\n\t"
      "v_mfma_f32_16x16x32_bf16 v[188:191], v[108:111], v[76:79], v[188:191]\n\t"
      "ds_read_b64_tr_b16 v[122:123], %[vWr0] offset:24832\n\t"
      "v_mfma_f32_16x16x32_bf16 v[184:187], v[104:107], v[76:79], v[184:187]\n\t"
      "buffer_load_dwordx4 %[vL16], %[srdWS], s40 offen offset:1024 sc1 lds\n\t"
      "v_mfma_f32_16x16x32_bf16 v[176:179], v[96:99], v[76:79], v[176:179]\n\t"
      "ds_read_b128 v[68:71], %[vXr] offset:2304\n\t"
      "v_mfma_f32_16x16x32_bf16 v[180:183], v[100:103], v[76:79], v[180:183]\n\t"
      "ds_read_b64_tr_b16 v[124:125], %[vWr1] offset:16640\n\t"
      "v_mfma_f32_16x16x32_bf16 v[164:167], v[100:103], v[72:75], v[164:167]\n\t"
      "ds_read_b64_tr_b16 v[126:127], %[vWr1] offset:24832\n\t"
      "v_mfma_f32_16x16x32_bf16 v[160:163], v[96:99], v[72:75], v[160:163]\n\t"
      "v_mfma_f32_16x16x32_bf16 v[192:195], v[96:99], v[80:83], v[192:195]\n\t"
      "ds_read_b128 v[72:75], %[vXr] offset:6144\n\t"
      "v_mfma_f32_16x16x32_bf16 v[196:199], v[100:103], v[80:83], v[196:199]\n\t"
      "ds_read_b64_tr_b16 v[52:53], v50 offset:0\n\t"
      "s_waitcnt lgkmcnt(14)\n\t"
      "v_mfma_f32_16x16x32_bf16 v[212:215], v[100:103], v[84:87], v[212:215]\n\t"
      "ds_read_b64_tr_b16 v[54:55], v50 offset:1024\n\t"
      "v_mfma_f32_16x16x32_bf16 v[208:211], v[96:99], v[84:87], v[208:211]\n\t"
      "buffer_load_dwordx4 %[vL16], %[srdWS], s40 offen offset:2048 sc1 lds\n\t"
      "v_mfma_f32_16x16x32_bf16 v[216:219], v[104:107], v[84:87], v[216:219]\n\t"
      "ds_read_b128 v[76:79], %[vXr] offset:6400\n\t"
      "v_mfma_f32_16x16x32_bf16 v[220:223], v[108:111], v[84:87], v[220:223]\n\t"
      "ds_read_b128 v[56:59], %[vXkv] offset:0\n\t"
      "v_mfma_f32_16x16x32_bf16 v[204:207], v[108:111], v[80:83], v[204:207]\n\t"
      "v_mfma_f32_16x16x32_bf16 v[200:203], v[104:107], v[80:83], v[200:203]\n\t"
      "s_waitcnt lgkmcnt(14)\n\t"
      "v_mfma_f32_16x16x32_bf16 v[232:235], v[104:107], v[88:91], v[232:235]\n\t"
      "ds_read_b128 v[80:83], %[vXr] offset:10240\n\t"
      "v_mfma_f32_16x16x32_bf16 v[236:239], v[108:111], v[88:91], v[236:239]\n\t"
      "buffer_load_dwordx4 %[vL16], %[srdWS], s40 offen offset:3072 sc1 lds\n\t"
      "s_waitcnt lgkmcnt(14)\n\t"
      "v_mfma_f32_16x16x32_bf16 v[252:255], v[108:111], v[92:95], v[252:255]\n\t"
      "v_mfma_f32_16x16x32_bf16 v[248:251], v[104:107], v[92:95], v[248:251]\n\t"
      "v_mfma_f32_16x16x32_bf16 v[240:243], v[96:99], v[92:95], v[240:243]\n\t"
      "ds_read_b128 v[84:87], %[vXr] offset:10496\n\t"
      "v_mfma_f32_16x16x32_bf16 v[244:247], v[100:103], v[92:95], v[244:247]\n\t"
      "v_mfma_f32_16x16x32_bf16 v[228:231], v[100:103], v[88:91], v[228:231]\n\t"
      "v_mfma_f32_16x16x32_bf16 v[224:227], v[96:99], v[88:91], v[224:227]\n\t"
      "s_waitcnt lgkmcnt(12)\n\t"
      "v_mfma_f32_16x16x32_bf16 v[128:131], v[112:115], v[64:67], v[128:131]\n\t"
      "ds_read_b128 v[88:91], %[vXr] offset:14336\n\t"
      "v_mfma_f32_16x16x32_bf16 v[132:135], v[116:119], v[64:67], v[132:135]\n\t"
      "ds_read_b128 v[92:95], %[vXr] offset:14592\n\t"
      "s_waitcnt lgkmcnt(11)\n\t"
      "v_mfma_f32_16x16x32_bf16 v[148:151], v[116:119], v[68:71], v[148:151]\n\t"
      "v_mfma_f32_16x16x32_bf16 v[144:147], v[112:115], v[68:71], v[144:147]\n\t"
      "v_mfma_f32_16x16x32_bf16 v[152:155], v[120:123], v[68:71], v[152:155]\n\t"
      "s_waitcnt lgkmcnt(9)\n\t"
      "v_mfma_f32_16x16x32_bf16 v[156:159], v[124:127], v[68:71], v[156:159]\n\t"
      "v_mfma_f32_16x16x32_bf16 v[140:143], v[124:127], v[64:67], v[140:143]\n\t"
      "v_mfma_f32_16x16x32_bf16 v[136:139], v[120:123], v[64:67], v[136:139]\n\t"
      "s_waitcnt vmcnt(0) lgkmcnt(0)\n\t"
      "s_barrier\n\t"
      "v_mfma_f32_16x16x32_bf16 v[60:63], v[52:55], v[56:59], v[60:63]\n\t"
      "v_mfma_f32_16x16x32_bf16 v[168:171], v[120:123], v[72:75], v[168:171]\n\t"
      "ds_read_b128 v[64:67], %[vXr] offset:32768\n\t"
      "v_mfma_f32_16x16x32_bf16 v[172:175], v[124:127], v[72:75], v[172:175]\n\t"
      "s_add_u32 m0, s42, 0\n\t"
      "v_lshl_add_u64 v[48:49], %[vWst], 0, v[48:49]\n\t"
      "global_load_lds_dwordx4 v[48:49], off\n\t"
      "v_mfma_f32_16x16x32_bf16 v[188:191], v[124:127], v[76:79], v[188:191]\n\t"
      "v_mfma_f32_16x16x32_bf16 v[184:187], v[120:123], v[76:79], v[184:187]\n\t"
      "v_mfma_f32_16x16x32_bf16 v[176:179], v[112:115], v[76:79], v[176:179]\n\t"
      "ds_read_b128 v[68:71], %[vXr] offset:33024\n\t"
      "v_mfma_f32_16x16x32_bf16 v[180:183], v[116:119], v[76:79], v[180:183]\n\t"
      "ds_read_b64_tr_b16 v[96:97], %[vWr0] offset:32768\n\t"
      "v_mfma_f32_16x16x32_bf16 v[164:167], v[116:119], v[72:75], v[164:167]\n\t"
      "ds_read_b64_tr_b16 v[98:99], %[vWr0] offset:40960\n\t"
      "v_mfma_f32_16x16x32_bf16 v[160:163], v[112:115], v[72:75], v[160:163]\n\t"
      "s_mov_b32 m0, s46\n\t"
      "s_xor_b32 s46, s46, 8192\n\t"
      "buffer_load_dwordx4 %[vKVsrc], %[srdKW], s45 offen lds\n\t"
      "s_add_u32 s45, s45, 8192\n\t"
      "v_mfma_f32_16x16x32_bf16 v[192:195], v[112:115], v[80:83], v[192:195]\n\t"
      "ds_read_b128 v[72:75], %[vXr] offset:36864\n\t"
      "v_mfma_f32_16x16x32_bf16 v[196:199], v[116:119], v[80:83], v[196:199]\n\t"
      "ds_read_b64_tr_b16 v[100:101], %[vWr1] offset:32768\n\t"
      "v_mfma_f32_16x16x32_bf16 v[212:215], v[116:119], v[84:87], v[212:215]\n\t"
      "ds_read_b64_tr_b16 v[102:103], %[vWr1] offset:40960\n\t"
      "v_mfma_f32_16x16x32_bf16 v[208:211], v[112:115], v[84:87], v[208:211]\n\t"
      "s_add_u32 m0, s42, 8192\n\t"
      "v_lshl_add_u64 v[48:49], %[vWst], 0, v[48:49]\n\t"
      "global_load_lds_dwordx4 v[48:49], off\n\t"
      "v_mfma_f32_16x16x32_bf16 v[216:219], v[120:123], v[84:87], v[216:219]\n\t"
      "ds_read_b128 v[76:79], %[vXr] offset:37120\n\t"
      "v_mfma_f32_16x16x32_bf16 v[220:223], v[124:127], v[84:87], v[220:223]\n\t"
      "ds_read_b64_tr_b16 v[104:105], %[vWr0] offset:33024\n\t"
      "v_mfma_f32_16x16x32_bf16 v[204:207], v[124:127], v[80:83], v[204:207]\n\t"
      "ds_read_b64_tr_b16 v[106:107], %[vWr0] offset:41216\n\t"
      "v_mfma_f32_16x16x32_bf16 v[200:203], v[120:123], v[80:83], v[200:203]\n\t"
      "v_mfma_f32_16x16x32_bf16 v[232:235], v[120:123], v[88:91], v[232:235]\n\t"
      "ds_read_b128 v[80:83], %[vXr] offset:40960\n\t"
      "v_mfma_f32_16x16x32_bf16 v[236:239], v[124:127], v[88:91], v[236:239]\n\t"
      "ds_read_b64_tr_b16 v[108:109], %[vWr1] offset:33024\n\t"
      "v_mfma_f32_16x16x32_bf16 v[252:255], v[124:127], v[92:95], v[252:255]\n\t"
      "ds_read_b64_tr_b16 v[110:111], %[vWr1] offset:41216\n\t"
      "v_mfma_f32_16x16x32_bf16 v[248:251], v[120:123], v[92:95], v[248:251]\n\t"
      "s_add_u32 m0, s42, 16384\n\t"
      "v_lshl_add_u64 v[48:49], %[vWst], 0, v[48:49]\n\t"
      "global_load_lds_dwordx4 v[48:49], off\n\t"
      "v_mfma_f32_16x16x32_bf16 v[240:243], v[112:115], v[92:95], v[240:243]\n\t"
      "ds_read_b128 v[84:87], %[vXr] offset:41216\n\t"
      "v_mfma_f32_16x16x32_bf16 v[244:247], v[116:119], v[92:95], v[244:247]\n\t"
      "s_add_u32 m0, s42, 24576\n\t"
      "v_lshl_add_u64 v[48:49], %[vWst], 0, v[48:49]\n\t"
      "global_load_lds_dwordx4 v[48:49], off\n\t"
      "v_mfma_f32_16x16x32_bf16 v[228:231], v[116:119], v[88:91], v[228:231]\n\t"
      "v_mfma_f32_16x16x32_bf16 v[224:227], v[112:115], v[88:91], v[224:227]\n\t"
      "s_waitcnt lgkmcnt(10)\n\t"
      "v_mfma_f32_16x16x32_bf16 v[128:131], v[96:99], v[64:67], v[128:131]\n\t"
      "ds_read_b128 v[88:91], %[vXr] offset:45056\n\t"
      "s_waitcnt lgkmcnt(8)\n\t"
      "v_mfma_f32_16x16x32_bf16 v[132:135], v[100:103], v[64:67], v[132:135]\n\t"
      "ds_read_b64_tr_b16 v[112:113], %[vWr0] offset:49152\n\t"
      "v_mfma_f32_16x16x32_bf16 v[148:151], v[100:103], v[68:71], v[148:151]\n\t"
      "ds_read_b64_tr_b16 v[114:115], %[vWr0] offset:57344\n\t"
      "v_mfma_f32_16x16x32_bf16 v[144:147], v[96:99], v[68:71], v[144:147]\n\t"
      "s_add_u32 m0, s41, 0\n\t"
      "s_add_u32 s40, s40, 4096\n\t"
      "buffer_load_dwordx4 %[vL16], %[srdWS], s40 offen offset:0 sc1 lds\n\t"
      "s_waitcnt lgkmcnt(7)\n\t"
      "v_mfma_f32_16x16x32_bf16 v[152:155], v[104:107], v[68:71], v[152:155]\n\t"
      "ds_read_b128 v[92:95], %[vXr] offset:45312\n\t"
      "s_waitcnt lgkmcnt(5)\n\t"
      "v_mfma_f32_16x16x32_bf16 v[156:159], v[108:111], v[68:71], v[156:159]\n\t"
      "ds_read_b64_tr_b16 v[116:117], %[vWr1] offset:49152\n\t"
      "v_mfma_f32_16x16x32_bf16 v[140:143], v[108:111], v[64:67], v[140:143]\n\t"
      "ds_read_b64_tr_b16 v[118:119], %[vWr1] offset:57344\n\t"
      "v_mfma_f32_16x16x32_bf16 v[136:139], v[104:107], v[64:67], v[136:139]\n\t"
      "v_mfma_f32_16x16x32_bf16 v[168:171], v[104:107], v[72:75], v[168:171]\n\t"
      "ds_read_b128 v[64:67], %[vXr] offset:34816\n\t"
      "v_mfma_f32_16x16x32_bf16 v[172:175], v[108:111], v[72:75], v[172:175]\n\t"
      "ds_read_b64_tr_b16 v[120:121], %[vWr0] offset:49408\n\t"
      "v_mfma_f32_16x16x32_bf16 v[188:191], v[108:111], v[76:79], v[188:191]\n\t"
      "ds_read_b64_tr_b16 v[122:123], %[vWr0] offset:57600\n\t"
      "v_mfma_f32_16x16x32_bf16 v[184:187], v[104:107], v[76:79], v[184:187]\n\t"
      "buffer_load_dwordx4 %[vL16], %[srdWS], s40 offen offset:1024 sc1 lds\n\t"
      "v_mfma_f32_16x16x32_bf16 v[176:179], v[96:99], v[76:79], v[176:179]\n\t"
      "ds_read_b128 v[68:71], %[vXr] offset:35072\n\t"
      "v_mfma_f32_16x16x32_bf16 v[180:183], v[100:103], v[76:79], v[180:183]\n\t"
      "ds_read_b64_tr_b16 v[124:125], %[vWr1] offset:49408\n\t"
      "v_mfma_f32_16x16x32_bf16 v[164:167], v[100:103], v[72:75], v[164:167]\n\t"
      "ds_read_b64_tr_b16 v[126:127], %[vWr1] offset:57600\n\t"
      "v_mfma_f32_16x16x32_bf16 v[160:163], v[96:99], v[72:75], v[160:163]\n\t"
      "v_mfma_f32_16x16x32_bf16 v[192:195], v[96:99], v[80:83], v[192:195]\n\t"
      "ds_read_b128 v[72:75], %[vXr] offset:38912\n\t"
      "v_mfma_f32_16x16x32_bf16 v[196:199], v[100:103], v[80:83], v[196:199]\n\t"
      "ds_read_b64_tr_b16 v[52:53], v50 offset:4096\n\t"
      "s_waitcnt lgkmcnt(14)\n\t"
      "v_mfma_f32_16x16x32_bf16 v[212:215], v[100:103], v[84:87], v[212:215]\n\t"
      "ds_read_b64_tr_b16 v[54:55], v50 offset:5120\n\t"
      "v_mfma_f32_16x16x32_bf16 v[208:211], v[96:99], v[84:87], v[208:211]\n\t"
      "buffer_load_dwordx4 %[vL16], %[srdWS], s40 offen offset:2048 sc1 lds\n\t"
      "v_mfma_f32_16x16x32_bf16 v[216:219], v[104:107], v[84:87], v[216:219]\n\t"
      "ds_read_b128 v[76:79], %[vXr] offset:39168\n\t"
      "v_mfma_f32_16x16x32_bf16 v[220:223], v[108:111], v[84:87], v[220:223]\n\t"
      "ds_read_b128 v[56:59], %[vXkv] offset:32768\n\t"
      "v_mfma_f32_16x16x32_bf16 v[204:207], v[108:111], v[80:83], v[204:207]\n\t"
      "v_mfma_f32_16x16x32_bf16 v[200:203], v[104:107], v[80:83], v[200:203]\n\t"
      "s_waitcnt lgkmcnt(14)\n\t"
      "v_mfma_f32_16x16x32_bf16 v[232:235], v[104:107], v[88:91], v[232:235]\n\t"
      "ds_read_b128 v[80:83], %[vXr] offset:43008\n\t"
      "v_mfma_f32_16x16x32_bf16 v[236:239], v[108:111], v[88:91], v[236:239]\n\t"
      "buffer_load_dwordx4 %[vL16], %[srdWS], s40 offen offset:3072 sc1 lds\n\t"
      "s_waitcnt lgkmcnt(14)\n\t"
      "v_mfma_f32_16x16x32_bf16 v[252:255], v[108:111], v[92:95], v[252:255]\n\t"
      "v_mfma_f32_16x16x32_bf16 v[248:251], v[104:107], v[92:95], v[248:251]\n\t"
      "v_mfma_f32_16x16x32_bf16 v[240:243], v[96:99], v[92:95], v[240:243]\n\t"
      "ds_read_b128 v[84:87], %[vXr] offset:43264\n\t"
      "v_mfma_f32_16x16x32_bf16 v[244:247], v[100:103], v[92:95], v[244:247]\n\t"
      "v_mfma_f32_16x16x32_bf16 v[228:231], v[100:103], v[88:91], v[228:231]\n\t"
      "v_mfma_f32_16x16x32_bf16 v[224:227], v[96:99], v[88:91], v[224:227]\n\t"
      "s_waitcnt lgkmcnt(12)\n\t"
      "v_mfma_f32_16x16x32_bf16 v[128:131], v[112:115], v[64:67], v[128:131]\n\t"
      "ds_read_b128 v[88:91], %[vXr] offset:47104\n\t"
      "v_mfma_f32_16x16x32_bf16 v[132:135], v[116:119], v[64:67], v[132:135]\n\t"
      "ds_read_b128 v[92:95], %[vXr] offset:47360\n\t"
      "s_waitcnt lgkmcnt(11)\n\t"
      "v_mfma_f32_16x16x32_bf16 v[148:151], v[116:119], v[68:71], v[148:151]\n\t"
      "v_mfma_f32_16x16x32_bf16 v[144:147], v[112:115], v[68:71], v[144:147]\n\t"
      "v_mfma_f32_16x16x32_bf16 v[152:155], v[120:123], v[68:71], v[152:155]\n\t"
      "s_waitcnt lgkmcnt(9)\n\t"
      "v_mfma_f32_16x16x32_bf16 v[156:159], v[124:127], v[68:71], v[156:159]\n\t"
      "v_mfma_f32_16x16x32_bf16 v[140:143], v[124:127], v[64:67], v[140:143]\n\t"
      "v_mfma_f32_16x16x32_bf16 v[136:139], v[120:123], v[64:67], v[136:139]\n\t"
      "s_waitcnt vmcnt(0) lgkmcnt(0)\n\t"
      "s_barrier\n\t"
      "v_mfma_f32_16x16x32_bf16 v[60:63], v[52:55], v[56:59], v[60:63]\n\t"
      "v_mfma_f32_16x16x32_bf16 v[168:171], v[120:123], v[72:75], v[168:171]\n\t"
      "ds_read_b128 v[64:67], %[vXr] offset:0\n\t"
      "v_mfma_f32_16x16x32_bf16 v[172:175], v[124:127], v[72:75], v[172:175]\n\t"
      "s_add_u32 m0, s42, 32768\n\t"
      "v_lshl_add_u64 v[48:49], %[vWst], 0, v[48:49]\n\t"
      "global_load_lds_dwordx4 v[48:49], off\n\t"
      "v_mfma_f32_16x16x32_bf16 v[188:191], v[124:127], v[76:79], v[188:191]\n\t"
      "v_mfma_f32_16x16x32_bf16 v[184:187], v[120:123], v[76:79], v[184:187]\n\t"
      "v_mfma_f32_16x16x32_bf16 v[176:179], v[112:115], v[76:79], v[176:179]\n\t"
      "ds_read_b128 v[68:71], %[vXr] offset:256\n\t"
      "v_mfma_f32_16x16x32_bf16 v[180:183], v[116:119], v[76:79], v[180:183]\n\t"
      "ds_read_b64_tr_b16 v[96:97], %[vWr0] offset:0\n\t"
      "v_mfma_f32_16x16x32_bf16 v[164:167], v[116:119], v[72:75], v[164:167]\n\t"
      "ds_read_b64_tr_b16 v[98:99], %[vWr0] offset:8192\n\t"
      "v_mfma_f32_16x16x32_bf16 v[160:163], v[112:115], v[72:75], v[160:163]\n\t"
      "v_mfma_f32_16x16x32_bf16 v[192:195], v[112:115], v[80:83], v[192:195]\n\t"
      "ds_read_b128 v[72:75], %[vXr] offset:4096\n\t"
      "v_mfma_f32_16x16x32_bf16 v[196:199], v[116:119], v[80:83], v[196:199]\n\t"
      "ds_read_b64_tr_b16 v[100:101], %[vWr1] offset:0\n\t"
      "v_mfma_f32_16x16x32_bf16 v[212:215], v[116:119], v[84:87], v[212:215]\n\t"
      "ds_read_b64_tr_b16 v[102:103], %[vWr1] offset:8192\n\t"
      "v_mfma_f32_16x16x32_bf16 v[208:211], v[112:115], v[84:87], v[208:211]\n\t"
      "s_add_u32 m0, s42, 40960\n\t"
      "v_lshl_add_u64 v[48:49], %[vWst], 0, v[48:49]\n\t"
      "global_load_lds_dwordx4 v[48:49], off\n\t"
      "v_mfma_f32_16x16x32_bf16 v[216:219], v[120:123], v[84:87], v[216:219]\n\t"
      "ds_read_b128 v[76:79], %[vXr] offset:4352\n\t"
      "v_mfma_f32_16x16x32_bf16 v[220:223], v[124:127], v[84:87], v[220:223]\n\t"
      "ds_read_b64_tr_b16 v[104:105], %[vWr0] offset:256\n\t"
      "v_mfma_f32_16x16x32_bf16 v[204:207], v[124:127], v[80:83], v[204:207]\n\t"
      "ds_read_b64_tr_b16 v[106:107], %[vWr0] offset:8448\n\t"
      "v_mfma_f32_16x16x32_bf16 v[200:203], v[120:123], v[80:83], v[200:203]\n\t"
      "v_mfma_f32_16x16x32_bf16 v[232:235], v[120:123], v[88:91], v[232:235]\n\t"
      "ds_read_b128 v[80:83], %[vXr] offset:8192\n\t"
      "v_mfma_f32_16x16x32_bf16 v[236:239], v[124:127], v[88:91], v[236:239]\n\t"
      "ds_read_b64_tr_b16 v[108:109], %[vWr1] offset:256\n\t"
      "v_mfma_f32_16x16x32_bf16 v[252:255], v[124:127], v[92:95], v[252:255]\n\t"
      "ds_read_b64_tr_b16 v[110:111], %[vWr1] offset:8448\n\t"
      "v_mfma_f32_16x16x32_bf16 v[248:251], v[120:123], v[92:95], v[248:251]\n\t"
      "s_add_u32 m0, s42, 49152\n\t"
      "v_lshl_add_u64 v[48:49], %[vWst], 0, v[48:49]\n\t"
      "global_load_lds_dwordx4 v[48:49], off\n\t"
      "v_mfma_f32_16x16x32_bf16 v[240:243], v[112:115], v[92:95], v[240:243]\n\t"
      "ds_read_b128 v[84:87], %[vXr] offset:8448\n\t"
      "v_mfma_f32_16x16x32_bf16 v[244:247], v[116:119], v[92:95], v[244:247]\n\t"
      "s_add_u32 m0, s42, 57344\n\t"
      "v_lshl_add_u64 v[48:49], %[vWst], 0, v[48:49]\n\t"
      "global_load_lds_dwordx4 v[48:49], off\n\t"
      "v_mfma_f32_16x16x32_bf16 v[228:231], v[116:119], v[88:91], v[228:231]\n\t"
      "v_mfma_f32_16x16x32_bf16 v[224:227], v[112:115], v[88:91], v[224:227]\n\t"
      "v_xor_b32 v50, 8192, v50\n\t"
      "s_sub_u32 s43, s43, 1\n\t"
      "s_cmp_lg_u32 s43, 0\n\t"
      "s_cbranch_scc1 P1_LOOPO_%=\n\t"
      "s_waitcnt lgkmcnt(10)\n\t"
      "v_mfma_f32_16x16x32_bf16 v[128:131], v[96:99], v[64:67], v[128:131]\n\t"
      "ds_read_b128 v[88:91], %[vXr] offset:12288\n\t"
      "s_waitcnt lgkmcnt(8)\n\t"
      "v_mfma_f32_16x16x32_bf16 v[132:135], v[100:103], v[64:67], v[132:135]\n\t"
      "ds_read_b64_tr_b16 v[112:113], %[vWr0] offset:16384\n\t"
      "v_mfma_f32_16x16x32_bf16 v[148:151], v[100:103], v[68:71], v[148:151]\n\t"
      "ds_read_b64_tr_b16 v[114:115], %[vWr0] offset:24576\n\t"
      "v_mfma_f32_16x16x32_bf16 v[144:147], v[96:99], v[68:71], v[144:147]\n\t"
      "s_mov_b32 m0, s46\n\t"
      "s_xor_b32 s46, s46, 8192\n\t"
      "buffer_load_dwordx4 %[vKVsrc], %[srdKW], s45 offen lds\n\t"
      "s_add_u32 s45, s45, 8192\n\t"
      "s_add_u32 m0, s41, 32768\n\t"
      "s_add_u32 s40, s40, 4096\n\t"
      "buffer_load_dwordx4 %[vL16], %[srdWS], s40 offen offset:0 sc1 lds\n\t"
      "s_waitcnt lgkmcnt(7)\n\t"
      "v_mfma_f32_16x16x32_bf16 v[152:155], v[104:107], v[68:71], v[152:155]\n\t"
      "ds_read_b128 v[92:95], %[vXr] offset:12544\n\t"
      "s_waitcnt lgkmcnt(5)\n\t"
      "v_mfma_f32_16x16x32_bf16 v[156:159], v[108:111], v[68:71], v[156:159]\n\t"
      "ds_read_b64_tr_b16 v[116:117], %[vWr1] offset:16384\n\t"
      "v_mfma_f32_16x16x32_bf16 v[140:143], v[108:111], v[64:67], v[140:143]\n\t"
      "ds_read_b64_tr_b16 v[118:119], %[vWr1] offset:24576\n\t"
      "v_mfma_f32_16x16x32_bf16 v[136:139], v[104:107], v[64:67], v[136:139]\n\t"
      "s_add_u32 s48, %[sXd], 0\n\t"
      "buffer_load_dwordx4 v[32:35], %[vOut], %[srdWS], s48 offen sc1\n\t"
      "v_mfma_f32_16x16x32_bf16 v[168:171], v[104:107], v[72:75], v[168:171]\n\t"
      "ds_read_b128 v[64:67], %[vXr] offset:2048\n\t"
      "v_mfma_f32_16x16x32_bf16 v[172:175], v[108:111], v[72:75], v[172:175]\n\t"
      "ds_read_b64_tr_b16 v[120:121], %[vWr0] offset:16640\n\t"
      "v_mfma_f32_16x16x32_bf16 v[188:191], v[108:111], v[76:79], v[188:191]\n\t"
      "ds_read_b64_tr_b16 v[122:123], %[vWr0] offset:24832\n\t"
      "v_mfma_f32_16x16x32_bf16 v[184:187], v[104:107], v[76:79], v[184:187]\n\t"
      "buffer_load_dwordx4 %[vL16], %[srdWS], s40 offen offset:1024 sc1 lds\n\t"
      "v_mfma_f32_16x16x32_bf16 v[176:179], v[96:99], v[76:79], v[176:179]\n\t"
      "ds_read_b128 v[68:71], %[vXr] offset:2304\n\t"
      "v_mfma_f32_16x16x32_bf16 v[180:183], v[100:103], v[76:79], v[180:183]\n\t"
      "ds_read_b64_tr_b16 v[124:125], %[vWr1] offset:16640\n\t"
      "v_mfma_f32_16x16x32_bf16 v[164:167], v[100:103], v[72:75], v[164:167]\n\t"
      "ds_read_b64_tr_b16 v[126:127], %[vWr1] offset:24832\n\t"
      "v_mfma_f32_16x16x32_bf16 v[160:163], v[96:99], v[72:75], v[160:163]\n\t"
      "s_add_u32 s48, %[sXd], 4096\n\t"
      "buffer_load_dwordx4 v[36:39], %[vOut], %[srdWS], s48 offen sc1\n\t"
      "v_mfma_f32_16x16x32_bf16 v[192:195], v[96:99], v[80:83], v[192:195]\n\t"
      "ds_read_b128 v[72:75], %[vXr] offset:6144\n\t"
      "v_mfma_f32_16x16x32_bf16 v[196:199], v[100:103], v[80:83], v[196:199]\n\t"
      "ds_read_b64_tr_b16 v[52:53], v50 offset:0\n\t"
      "s_waitcnt lgkmcnt(14)\n\t"
      "v_mfma_f32_16x16x32_bf16 v[212:215], v[100:103], v[84:87], v[212:215]\n\t"
      "ds_read_b64_tr_b16 v[54:55], v50 offset:1024\n\t"
      "v_mfma_f32_16x16x32_bf16 v[208:211], v[96:99], v[84:87], v[208:211]\n\t"
      "buffer_load_dwordx4 %[vL16], %[srdWS], s40 offen offset:2048 sc1 lds\n\t"
      "v_mfma_f32_16x16x32_bf16 v[216:219], v[104:107], v[84:87], v[216:219]\n\t"
      "ds_read_b128 v[76:79], %[vXr] offset:6400\n\t"
      "v_mfma_f32_16x16x32_bf16 v[220:223], v[108:111], v[84:87], v[220:223]\n\t"
      "ds_read_b128 v[56:59], %[vXkv] offset:0\n\t"
      "v_mfma_f32_16x16x32_bf16 v[204:207], v[108:111], v[80:83], v[204:207]\n\t"
      "v_mfma_f32_16x16x32_bf16 v[200:203], v[104:107], v[80:83], v[200:203]\n\t"
      "s_waitcnt lgkmcnt(14)\n\t"
      "v_mfma_f32_16x16x32_bf16 v[232:235], v[104:107], v[88:91], v[232:235]\n\t"
      "ds_read_b128 v[80:83], %[vXr] offset:10240\n\t"
      "v_mfma_f32_16x16x32_bf16 v[236:239], v[108:111], v[88:91], v[236:239]\n\t"
      "buffer_load_dwordx4 %[vL16], %[srdWS], s40 offen offset:3072 sc1 lds\n\t"
      "s_waitcnt lgkmcnt(14)\n\t"
      "v_mfma_f32_16x16x32_bf16 v[252:255], v[108:111], v[92:95], v[252:255]\n\t"
      "v_mfma_f32_16x16x32_bf16 v[248:251], v[104:107], v[92:95], v[248:251]\n\t"
      "v_mfma_f32_16x16x32_bf16 v[240:243], v[96:99], v[92:95], v[240:243]\n\t"
      "ds_read_b128 v[84:87], %[vXr] offset:10496\n\t"
      "v_mfma_f32_16x16x32_bf16 v[244:247], v[100:103], v[92:95], v[244:247]\n\t"
      "v_mfma_f32_16x16x32_bf16 v[228:231], v[100:103], v[88:91], v[228:231]\n\t"
      "v_mfma_f32_16x16x32_bf16 v[224:227], v[96:99], v[88:91], v[224:227]\n\t"
      "s_waitcnt lgkmcnt(12)\n\t"
      "v_mfma_f32_16x16x32_bf16 v[128:131], v[112:115], v[64:67], v[128:131]\n\t"
      "ds_read_b128 v[88:91], %[vXr] offset:14336\n\t"
      "v_mfma_f32_16x16x32_bf16 v[132:135], v[116:119], v[64:67], v[132:135]\n\t"
      "ds_read_b128 v[92:95], %[vXr] offset:14592\n\t"
      "s_waitcnt lgkmcnt(11)\n\t"
      "v_mfma_f32_16x16x32_bf16 v[148:151], v[116:119], v[68:71], v[148:151]\n\t"
      "v_mfma_f32_16x16x32_bf16 v[144:147], v[112:115], v[68:71], v[144:147]\n\t"
      "v_mfma_f32_16x16x32_bf16 v[152:155], v[120:123], v[68:71], v[152:155]\n\t"
      "s_waitcnt lgkmcnt(9)\n\t"
      "v_mfma_f32_16x16x32_bf16 v[156:159], v[124:127], v[68:71], v[156:159]\n\t"
      "v_mfma_f32_16x16x32_bf16 v[140:143], v[124:127], v[64:67], v[140:143]\n\t"
      "v_mfma_f32_16x16x32_bf16 v[136:139], v[120:123], v[64:67], v[136:139]\n\t"
      "s_waitcnt vmcnt(0) lgkmcnt(0)\n\t"
      "s_barrier\n\t"
      "s_add_u32 s48, %[sXd], 8192\n\t"
      "buffer_load_dwordx4 v[40:43], %[vOut], %[srdWS], s48 offen sc1\n\t"
      "s_add_u32 s48, %[sXd], 12288\n\t"
      "buffer_load_dwordx4 v[44:47], %[vOut], %[srdWS], s48 offen sc1\n\t"
      "v_mfma_f32_16x16x32_bf16 v[60:63], v[52:55], v[56:59], v[60:63]\n\t"
      "v_mfma_f32_16x16x32_bf16 v[168:171], v[120:123], v[72:75], v[168:171]\n\t"
      "ds_read_b64_tr_b16 v[52:53], %[vKVr] offset:0\n\t"
      "v_mfma_f32_16x16x32_bf16 v[172:175], v[124:127], v[72:75], v[172:175]\n\t"
      "ds_read_b64_tr_b16 v[54:55], %[vKVr] offset:1024\n\t"
      "v_mfma_f32_16x16x32_bf16 v[188:191], v[124:127], v[76:79], v[188:191]\n\t"
      "ds_read_b128 v[64:67], %[vXr] offset:32768\n\t"
      "v_mfma_f32_16x16x32_bf16 v[184:187], v[120:123], v[76:79], v[184:187]\n\t"
      "s_add_u32 m0, s42, 0\n\t"
      "v_lshl_add_u64 v[48:49], %[vWst], 0, v[48:49]\n\t"
      "global_load_lds_dwordx4 v[48:49], off\n\t"
      "v_mfma_f32_16x16x32_bf16 v[176:179], v[112:115], v[76:79], v[176:179]\n\t"
      "ds_read_b128 v[68:71], %[vXr] offset:33024\n\t"
      "v_mfma_f32_16x16x32_bf16 v[180:183], v[116:119], v[76:79], v[180:183]\n\t"
      "ds_read_b64_tr_b16 v[96:97], %[vWr0] offset:32768\n\t"
      "v_mfma_f32_16x16x32_bf16 v[164:167], v[116:119], v[72:75], v[164:167]\n\t"
      "ds_read_b64_tr_b16 v[98:99], %[vWr0] offset:40960\n\t"
      "v_mfma_f32_16x16x32_bf16 v[160:163], v[112:115], v[72:75], v[160:163]\n\t"
      "s_waitcnt lgkmcnt(4)\n\t"
      "v_mfma_f32_16x16x32_bf16 v[60:63], v[52:55], v[32:35], v[60:63]\n\t"
      "v_mfma_f32_16x16x32_bf16 v[192:195], v[112:115], v[80:83], v[192:195]\n\t"
      "ds_read_b64_tr_b16 v[52:53], %[vKVr] offset:4096\n\t"
      "v_mfma_f32_16x16x32_bf16 v[196:199], v[116:119], v[80:83], v[196:199]\n\t"
      "ds_read_b64_tr_b16 v[54:55], %[vKVr] offset:5120\n\t"
      "v_mfma_f32_16x16x32_bf16 v[212:215], v[116:119], v[84:87], v[212:215]\n\t"
      "ds_read_b128 v[72:75], %[vXr] offset:36864\n\t"
      "v_mfma_f32_16x16x32_bf16 v[208:211], v[112:115], v[84:87], v[208:211]\n\t"
      "ds_read_b64_tr_b16 v[100:101], %[vWr1] offset:32768\n\t"
      "ds_read_b64_tr_b16 v[102:103], %[vWr1] offset:40960\n\t"
      "s_add_u32 m0, s42, 8192\n\t"
      "v_lshl_add_u64 v[48:49], %[vWst], 0, v[48:49]\n\t"
      "global_load_lds_dwordx4 v[48:49], off\n\t"
      "v_mfma_f32_16x16x32_bf16 v[216:219], v[120:123], v[84:87], v[216:219]\n\t"
      "ds_read_b128 v[76:79], %[vXr] offset:37120\n\t"
      "v_mfma_f32_16x16x32_bf16 v[220:223], v[124:127], v[84:87], v[220:223]\n\t"
      "ds_read_b64_tr_b16 v[104:105], %[vWr0] offset:33024\n\t"
      "v_mfma_f32_16x16x32_bf16 v[204:207], v[124:127], v[80:83], v[204:207]\n\t"
      "ds_read_b64_tr_b16 v[106:107], %[vWr0] offset:41216\n\t"
      "v_mfma_f32_16x16x32_bf16 v[200:203], v[120:123], v[80:83], v[200:203]\n\t"
      "s_waitcnt lgkmcnt(6)\n\t"
      "v_mfma_f32_16x16x32_bf16 v[60:63], v[52:55], v[36:39], v[60:63]\n\t"
      "v_mfma_f32_16x16x32_bf16 v[232:235], v[120:123], v[88:91], v[232:235]\n\t"
      "ds_read_b128 v[80:83], %[vXr] offset:40960\n\t"
      "v_mfma_f32_16x16x32_bf16 v[236:239], v[124:127], v[88:91], v[236:239]\n\t"
      "ds_read_b64_tr_b16 v[108:109], %[vWr1] offset:33024\n\t"
      "v_mfma_f32_16x16x32_bf16 v[252:255], v[124:127], v[92:95], v[252:255]\n\t"
      "ds_read_b64_tr_b16 v[110:111], %[vWr1] offset:41216\n\t"
      "v_mfma_f32_16x16x32_bf16 v[248:251], v[120:123], v[92:95], v[248:251]\n\t"
      "s_add_u32 m0, s42, 16384\n\t"
      "v_lshl_add_u64 v[48:49], %[vWst], 0, v[48:49]\n\t"
      "global_load_lds_dwordx4 v[48:49], off\n\t"
      "v_mfma_f32_16x16x32_bf16 v[240:243], v[112:115], v[92:95], v[240:243]\n\t"
      "ds_read_b128 v[84:87], %[vXr] offset:41216\n\t"
      "v_mfma_f32_16x16x32_bf16 v[244:247], v[116:119], v[92:95], v[244:247]\n\t"
      "s_add_u32 m0, s42, 24576\n\t"
      "v_lshl_add_u64 v[48:49], %[vWst], 0, v[48:49]\n\t"
      "global_load_lds_dwordx4 v[48:49], off\n\t"
      "v_mfma_f32_16x16x32_bf16 v[228:231], v[116:119], v[88:91], v[228:231]\n\t"
      "v_mfma_f32_16x16x32_bf16 v[224:227], v[112:115], v[88:91], v[224:227]\n\t"
      "v_mfma_f32_16x16x32_bf16 v[128:131], v[96:99], v[64:67], v[128:131]\n\t"
      "ds_read_b64_tr_b16 v[52:53], %[vKVr] offset:12288\n\t"
      "s_waitcnt lgkmcnt(8)\n\t"
      "v_mfma_f32_16x16x32_bf16 v[132:135], v[100:103], v[64:67], v[132:135]\n\t"
      "ds_read_b64_tr_b16 v[54:55], %[vKVr] offset:13312\n\t"
      "v_mfma_f32_16x16x32_bf16 v[148:151], v[100:103], v[68:71], v[148:151]\n\t"
      "ds_read_b128 v[56:59], %[vXkv] offset:32768\n\t"
      "v_mfma_f32_16x16x32_bf16 v[144:147], v[96:99], v[68:71], v[144:147]\n\t"
      "ds_read_b128 v[88:91], %[vXr] offset:45056\n\t"
      "ds_read_b64_tr_b16 v[112:113], %[vWr0] offset:49152\n\t"
      "ds_read_b64_tr_b16 v[114:115], %[vWr0] offset:57344\n\t"
      "s_add_u32 m0, s41, 0\n\t"
      "s_add_u32 s40, s40, 4096\n\t"
      "buffer_load_dwordx4 %[vL16], %[srdWS], s40 offen offset:0 sc1 lds\n\t"
      "s_waitcnt lgkmcnt(10)\n\t"
      "v_mfma_f32_16x16x32_bf16 v[152:155], v[104:107], v[68:71], v[152:155]\n\t"
      "ds_read_b128 v[92:95], %[vXr] offset:45312\n\t"
      "s_waitcnt lgkmcnt(8)\n\t"
      "v_mfma_f32_16x16x32_bf16 v[156:159], v[108:111], v[68:71], v[156:159]\n\t"
      "ds_read_b64_tr_b16 v[116:117], %[vWr1] offset:49152\n\t"
      "v_mfma_f32_16x16x32_bf16 v[140:143], v[108:111], v[64:67], v[140:143]\n\t"
      "ds_read_b64_tr_b16 v[118:119], %[vWr1] offset:57344\n\t"
      "v_mfma_f32_16x16x32_bf16 v[136:139], v[104:107], v[64:67], v[136:139]\n\t"
      "s_waitcnt lgkmcnt(6)\n\t"
      "v_mfma_f32_16x16x32_bf16 v[60:63], v[52:55], v[56:59], v[60:63]\n\t"
      "v_mfma_f32_16x16x32_bf16 v[168:171], v[104:107], v[72:75], v[168:171]\n\t"
      "ds_read_b64_tr_b16 v[52:53], %[vKVr] offset:16384\n\t"
      "v_mfma_f32_16x16x32_bf16 v[172:175], v[108:111], v[72:75], v[172:175]\n\t"
      "ds_read_b64_tr_b16 v[54:55], %[vKVr] offset:17408\n\t"
      "v_mfma_f32_16x16x32_bf16 v[188:191], v[108:111], v[76:79], v[188:191]\n\t"
      "ds_read_b128 v[64:67], %[vXr] offset:34816\n\t"
      "v_mfma_f32_16x16x32_bf16 v[184:187], v[104:107], v[76:79], v[184:187]\n\t"
      "ds_read_b64_tr_b16 v[120:121], %[vWr0] offset:49408\n\t"
      "ds_read_b64_tr_b16 v[122:123], %[vWr0] offset:57600\n\t"
      "buffer_load_dwordx4 %[vL16], %[srdWS], s40 offen offset:1024 sc1 lds\n\t"
      "v_mfma_f32_16x16x32_bf16 v[176:179], v[96:99], v[76:79], v[176:179]\n\t"
      "ds_read_b128 v[68:71], %[vXr] offset:35072\n\t"
      "v_mfma_f32_16x16x32_bf16 v[180:183], v[100:103], v[76:79], v[180:183]\n\t"
      "ds_read_b64_tr_b16 v[124:125], %[vWr1] offset:49408\n\t"
      "v_mfma_f32_16x16x32_bf16 v[164:167], v[100:103], v[72:75], v[164:167]\n\t"
      "ds_read_b64_tr_b16 v[126:127], %[vWr1] offset:57600\n\t"
      "v_mfma_f32_16x16x32_bf16 v[160:163], v[96:99], v[72:75], v[160:163]\n\t"
      "s_waitcnt vmcnt(6)\n\t"
      "s_waitcnt lgkmcnt(6)\n\t"
      "v_mfma_f32_16x16x32_bf16 v[60:63], v[52:55], v[40:43], v[60:63]\n\t"
      "v_mfma_f32_16x16x32_bf16 v[192:195], v[96:99], v[80:83], v[192:195]\n\t"
      "ds_read_b64_tr_b16 v[52:53], %[vKVr] offset:20480\n\t"
      "v_mfma_f32_16x16x32_bf16 v[196:199], v[100:103], v[80:83], v[196:199]\n\t"
      "ds_read_b64_tr_b16 v[54:55], %[vKVr] offset:21504\n\t"
      "v_mfma_f32_16x16x32_bf16 v[212:215], v[100:103], v[84:87], v[212:215]\n\t"
      "ds_read_b128 v[72:75], %[vXr] offset:38912\n\t"
      "v_mfma_f32_16x16x32_bf16 v[208:211], v[96:99], v[84:87], v[208:211]\n\t"
      "buffer_load_dwordx4 %[vL16], %[srdWS], s40 offen offset:2048 sc1 lds\n\t"
      "v_mfma_f32_16x16x32_bf16 v[216:219], v[104:107], v[84:87], v[216:219]\n\t"
      "ds_read_b128 v[76:79], %[vXr] offset:39168\n\t"
      "v_mfma_f32_16x16x32_bf16 v[220:223], v[108:111], v[84:87], v[220:223]\n\t"
      "v_mfma_f32_16x16x32_bf16 v[204:207], v[108:111], v[80:83], v[204:207]\n\t"
      "v_mfma_f32_16x16x32_bf16 v[200:203], v[104:107], v[80:83], v[200:203]\n\t"
      "s_waitcnt lgkmcnt(2)\n\t"
      "v_mfma_f32_16x16x32_bf16 v[60:63], v[52:55], v[44:47], v[60:63]\n\t"
      "v_mfma_f32_16x16x32_bf16 v[232:235], v[104:107], v[88:91], v[232:235]\n\t"
      "ds_read_b128 v[80:83], %[vXr] offset:43008\n\t"
      "v_mfma_f32_16x16x32_bf16 v[236:239], v[108:111], v[88:91], v[236:239]\n\t"
      "buffer_load_dwordx4 %[vL16], %[srdWS], s40 offen offset:3072 sc1 lds\n\t"
      "v_mfma_f32_16x16x32_bf16 v[252:255], v[108:111], v[92:95], v[252:255]\n\t"
      "v_mfma_f32_16x16x32_bf16 v[248:251], v[104:107], v[92:95], v[248:251]\n\t"
      "v_mfma_f32_16x16x32_bf16 v[240:243], v[96:99], v[92:95], v[240:243]\n\t"
      "ds_read_b128 v[84:87], %[vXr] offset:43264\n\t"
      "v_mfma_f32_16x16x32_bf16 v[244:247], v[100:103], v[92:95], v[244:247]\n\t"
      "v_mfma_f32_16x16x32_bf16 v[228:231], v[100:103], v[88:91], v[228:231]\n\t"
      "v_mfma_f32_16x16x32_bf16 v[224:227], v[96:99], v[88:91], v[224:227]\n\t"
      "v_mfma_f32_16x16x32_bf16 v[128:131], v[112:115], v[64:67], v[128:131]\n\t"
      "ds_read_b128 v[88:91], %[vXr] offset:47104\n\t"
      "v_mfma_f32_16x16x32_bf16 v[132:135], v[116:119], v[64:67], v[132:135]\n\t"
      "ds_read_b128 v[92:95], %[vXr] offset:47360\n\t"
      "v_mfma_f32_16x16x32_bf16 v[148:151], v[116:119], v[68:71], v[148:151]\n\t"
      "v_mov_b32 v50, %[vKVw]\n\t"
      "ds_write_b32 v50, v60\n\t"
      "v_add_u32 v50, %[sKVst], v50\n\t"
      "ds_write_b32 v50, v61\n\t"
      "v_add_u32 v50, %[sKVst], v50\n\t"
      "ds_write_b32 v50, v62\n\t"
      "v_add_u32 v50, %[sKVst], v50\n\t"
      "ds_write_b32 v50, v63\n\t"
      "v_mfma_f32_16x16x32_bf16 v[144:147], v[112:115], v[68:71], v[144:147]\n\t"
      "v_mfma_f32_16x16x32_bf16 v[152:155], v[120:123], v[68:71], v[152:155]\n\t"
      "v_mfma_f32_16x16x32_bf16 v[156:159], v[124:127], v[68:71], v[156:159]\n\t"
      "v_mfma_f32_16x16x32_bf16 v[140:143], v[124:127], v[64:67], v[140:143]\n\t"
      "v_mfma_f32_16x16x32_bf16 v[136:139], v[120:123], v[64:67], v[136:139]\n\t"
      "s_waitcnt vmcnt(0) lgkmcnt(0)\n\t"
      "s_barrier\n\t"
      "v_mfma_f32_16x16x32_bf16 v[168:171], v[120:123], v[72:75], v[168:171]\n\t"
      "s_and_b32 s49, %[w], 1\n\t"
      "s_lshl_b32 s49, s49, 10\n\t"
      "ds_read_b128 v[64:67], %[vXr] offset:0\n\t"
      "v_mfma_f32_16x16x32_bf16 v[172:175], v[124:127], v[72:75], v[172:175]\n\t"
      "s_add_u32 s49, s49, 155648\n\t"
      "v_add_u32 v50, s49, %[vL16]\n\t"
      "s_add_u32 m0, s42, 32768\n\t"
      "v_lshl_add_u64 v[48:49], %[vWst], 0, v[48:49]\n\t"
      "global_load_lds_dwordx4 v[48:49], off\n\t"
      "v_mfma_f32_16x16x32_bf16 v[188:191], v[124:127], v[76:79], v[188:191]\n\t"
      "ds_read_b128 v[32:35], v50 offset:0\n\t"
      "v_mfma_f32_16x16x32_bf16 v[184:187], v[120:123], v[76:79], v[184:187]\n\t"
      "ds_read_b128 v[52:55], v50 offset:4096\n\t"
      "v_mfma_f32_16x16x32_bf16 v[176:179], v[112:115], v[76:79], v[176:179]\n\t"
      "ds_read_b128 v[36:39], v50 offset:2048\n\t"
      "ds_read_b128 v[68:71], %[vXr] offset:256\n\t"
      "v_mfma_f32_16x16x32_bf16 v[180:183], v[116:119], v[76:79], v[180:183]\n\t"
      "ds_read_b128 v[56:59], v50 offset:6144\n\t"
      "ds_read_b64_tr_b16 v[96:97], %[vWr0] offset:0\n\t"
      "v_mfma_f32_16x16x32_bf16 v[164:167], v[116:119], v[72:75], v[164:167]\n\t"
      "ds_read_b64_tr_b16 v[98:99], %[vWr0] offset:8192\n\t"
      "v_mfma_f32_16x16x32_bf16 v[160:163], v[112:115], v[72:75], v[160:163]\n\t"
      "v_mfma_f32_16x16x32_bf16 v[192:195], v[112:115], v[80:83], v[192:195]\n\t"
      "ds_read_b128 v[72:75], %[vXr] offset:4096\n\t"
      "v_mfma_f32_16x16x32_bf16 v[196:199], v[116:119], v[80:83], v[196:199]\n\t"
      "ds_read_b64_tr_b16 v[100:101], %[vWr1] offset:0\n\t"
      "v_mfma_f32_16x16x32_bf16 v[212:215], v[116:119], v[84:87], v[212:215]\n\t"
      "s_waitcnt lgkmcnt(7)\n\t"
      "v_add_f32 v32, v32, v52\n\t"
      "v_add_f32 v33, v33, v53\n\t"
      "ds_read_b64_tr_b16 v[102:103], %[vWr1] offset:8192\n\t"
      "v_mfma_f32_16x16x32_bf16 v[208:211], v[112:115], v[84:87], v[208:211]\n\t"
      "v_add_f32 v34, v34, v54\n\t"
      "v_add_f32 v35, v35, v55\n\t"
      "s_add_u32 m0, s42, 40960\n\t"
      "v_lshl_add_u64 v[48:49], %[vWst], 0, v[48:49]\n\t"
      "global_load_lds_dwordx4 v[48:49], off\n\t"
      "v_mfma_f32_16x16x32_bf16 v[216:219], v[120:123], v[84:87], v[216:219]\n\t"
      "s_waitcnt lgkmcnt(5)\n\t"
      "v_add_f32 v36, v36, v56\n\t"
      "v_add_f32 v37, v37, v57\n\t"
      "ds_read_b128 v[76:79], %[vXr] offset:4352\n\t"
      "v_mfma_f32_16x16x32_bf16 v[220:223], v[124:127], v[84:87], v[220:223]\n\t"
      "v_add_f32 v38, v38, v58\n\t"
      "v_add_f32 v39, v39, v59\n\t"
      "ds_read_b64_tr_b16 v[104:105], %[vWr0] offset:256\n\t"
      "v_mfma_f32_16x16x32_bf16 v[204:207], v[124:127], v[80:83], v[204:207]\n\t"
      "v_cvt_pk_bf16_f32 v60, v32, v33\n\t"
      "v_cvt_pk_bf16_f32 v61, v34, v35\n\t"
      "ds_read_b64_tr_b16 v[106:107], %[vWr0] offset:8448\n\t"
      "v_mfma_f32_16x16x32_bf16 v[200:203], v[120:123], v[80:83], v[200:203]\n\t"
      "v_cvt_pk_bf16_f32 v62, v36, v37\n\t"
      "v_cvt_pk_bf16_f32 v63, v38, v39\n\t"
      "v_mfma_f32_16x16x32_bf16 v[232:235], v[120:123], v[88:91], v[232:235]\n\t"
      "buffer_store_dwordx4 v[60:63], %[vKVo], %[srdWS], 0 offen sc1\n\t"
      "v_lshlrev_b32 v32, 16, v60\n\t"
      "ds_read_b128 v[80:83], %[vXr] offset:8192\n\t"
      "v_mfma_f32_16x16x32_bf16 v[236:239], v[124:127], v[88:91], v[236:239]\n\t"
      "v_and_b32 v33, 0xffff0000, v60\n\t"
      "v_lshlrev_b32 v34, 16, v61\n\t"
      "ds_read_b64_tr_b16 v[108:109], %[vWr1] offset:256\n\t"
      "v_mfma_f32_16x16x32_bf16 v[252:255], v[124:127], v[92:95], v[252:255]\n\t"
      "v_and_b32 v35, 0xffff0000, v61\n\t"
      "v_lshlrev_b32 v36, 16, v62\n\t"
      "ds_read_b64_tr_b16 v[110:111], %[vWr1] offset:8448\n\t"
      "v_mfma_f32_16x16x32_bf16 v[248:251], v[120:123], v[92:95], v[248:251]\n\t"
      "v_and_b32 v37, 0xffff0000, v62\n\t"
      "v_lshlrev_b32 v38, 16, v63\n\t"
      "s_add_u32 m0, s42, 49152\n\t"
      "v_lshl_add_u64 v[48:49], %[vWst], 0, v[48:49]\n\t"
      "global_load_lds_dwordx4 v[48:49], off\n\t"
      "v_mfma_f32_16x16x32_bf16 v[240:243], v[112:115], v[92:95], v[240:243]\n\t"
      "v_and_b32 v39, 0xffff0000, v63\n\t"
      "v_mul_f32 v52, v32, v32\n\t"
      "ds_read_b128 v[84:87], %[vXr] offset:8448\n\t"
      "v_mfma_f32_16x16x32_bf16 v[244:247], v[116:119], v[92:95], v[244:247]\n\t"
      "v_fmac_f32 v52, v33, v33\n\t"
      "v_fmac_f32 v52, v34, v34\n\t"
      "s_add_u32 m0, s42, 57344\n\t"
      "v_lshl_add_u64 v[48:49], %[vWst], 0, v[48:49]\n\t"
      "global_load_lds_dwordx4 v[48:49], off\n\t"
      "v_mfma_f32_16x16x32_bf16 v[228:231], v[116:119], v[88:91], v[228:231]\n\t"
      "v_fmac_f32 v52, v35, v35\n\t"
      "v_fmac_f32 v52, v36, v36\n\t"
      "v_mfma_f32_16x16x32_bf16 v[224:227], v[112:115], v[88:91], v[224:227]\n\t"
      "v_fmac_f32 v52, v37, v37\n\t"
      "v_fmac_f32 v52, v38, v38\n\t"
      "s_waitcnt lgkmcnt(10)\n\t"
      "v_mfma_f32_16x16x32_bf16 v[128:131], v[96:99], v[64:67], v[128:131]\n\t"
      "v_fmac_f32 v52, v39, v39\n\t"
      "v_mov_b32 v53, v52\n\t"
      "ds_read_b128 v[88:91], %[vXr] offset:12288\n\t"
      "s_waitcnt lgkmcnt(8)\n\t"
      "v_mfma_f32_16x16x32_bf16 v[132:135], v[100:103], v[64:67], v[132:135]\n\t"
      "ds_read_b64_tr_b16 v[112:113], %[vWr0] offset:16384\n\t"
      "v_mfma_f32_16x16x32_bf16 v[148:151], v[100:103], v[68:71], v[148:151]\n\t"
      "v_permlane16_swap_b32 v52, v53\n\t"
      "ds_read_b64_tr_b16 v[114:115], %[vWr0] offset:24576\n\t"
      "v_mfma_f32_16x16x32_bf16 v[144:147], v[96:99], v[68:71], v[144:147]\n\t"
      "v_add_f32 v52, v52, v53\n\t"
      "v_mov_b32 v53, v52\n\t"
      "s_add_u32 m0, s41, 32768\n\t"
      "s_add_u32 s40, s40, 4096\n\t"
      "buffer_load_dwordx4 %[vL16], %[srdWS], s40 offen offset:0 sc1 lds\n\t"
      "s_waitcnt lgkmcnt(7)\n\t"
      "v_mfma_f32_16x16x32_bf16 v[152:155], v[104:107], v[68:71], v[152:155]\n\t"
      "ds_read_b128 v[92:95], %[vXr] offset:12544\n\t"
      "s_waitcnt lgkmcnt(5)\n\t"
      "v_mfma_f32_16x16x32_bf16 v[156:159], v[108:111], v[68:71], v[156:159]\n\t"
      "v_permlane32_swap_b32 v52, v53\n\t"
      "ds_read_b64_tr_b16 v[116:117], %[vWr1] offset:16384\n\t"
      "v_mfma_f32_16x16x32_bf16 v[140:143], v[108:111], v[64:67], v[140:143]\n\t"
      "v_add_f32 v52, v52, v53\n\t"
      "v_and_b32 v52, 0x7fffffff, v52\n\t"
      "ds_read_b64_tr_b16 v[118:119], %[vWr1] offset:24576\n\t"
      "v_mfma_f32_16x16x32_bf16 v[136:139], v[104:107], v[64:67], v[136:139]\n\t"
      "v_mfma_f32_16x16x32_bf16 v[168:171], v[104:107], v[72:75], v[168:171]\n\t"
      "v_max_u32_dpp v52, v52, v52 quad_perm:[1,0,3,2] row_mask:0xf bank_mask:0xf\n\t"
      "ds_read_b128 v[64:67], %[vXr] offset:2048\n\t"
      "v_mfma_f32_16x16x32_bf16 v[172:175], v[108:111], v[72:75], v[172:175]\n\t"
      "ds_read_b64_tr_b16 v[120:121], %[vWr0] offset:16640\n\t"
      "v_mfma_f32_16x16x32_bf16 v[188:191], v[108:111], v[76:79], v[188:191]\n\t"
      "v_max_u32_dpp v52, v52, v52 quad_perm:[2,3,0,1] row_mask:0xf bank_mask:0xf\n\t"
      "ds_read_b64_tr_b16 v[122:123], %[vWr0] offset:24832\n\t"
      "v_mfma_f32_16x16x32_bf16 v[184:187], v[104:107], v[76:79], v[184:187]\n\t"
      "buffer_load_dwordx4 %[vL16], %[srdWS], s40 offen offset:1024 sc1 lds\n\t"
      "v_mfma_f32_16x16x32_bf16 v[176:179], v[96:99], v[76:79], v[176:179]\n\t"
      "v_max_u32_dpp v52, v52, v52 row_half_mirror row_mask:0xf bank_mask:0xf\n\t"
      "ds_read_b128 v[68:71], %[vXr] offset:2304\n\t"
      "v_mfma_f32_16x16x32_bf16 v[180:183], v[100:103], v[76:79], v[180:183]\n\t"
      "ds_read_b64_tr_b16 v[124:125], %[vWr1] offset:16640\n\t"
      "v_mfma_f32_16x16x32_bf16 v[164:167], v[100:103], v[72:75], v[164:167]\n\t"
      "v_max_u32_dpp v52, v52, v52 row_mirror row_mask:0xf bank_mask:0xf\n\t"
      "ds_read_b64_tr_b16 v[126:127], %[vWr1] offset:24832\n\t"
      "v_mfma_f32_16x16x32_bf16 v[160:163], v[96:99], v[72:75], v[160:163]\n\t"
      "v_mfma_f32_16x16x32_bf16 v[192:195], v[96:99], v[80:83], v[192:195]\n\t"
      "ds_read_b128 v[72:75], %[vXr] offset:6144\n\t"
      "v_mfma_f32_16x16x32_bf16 v[196:199], v[100:103], v[80:83], v[196:199]\n\t"
      "buffer_load_dwordx4 %[vL16], %[srdWS], s40 offen offset:2048 sc1 lds\n\t"
      "s_waitcnt lgkmcnt(13)\n\t"
      "v_mfma_f32_16x16x32_bf16 v[212:215], v[100:103], v[84:87], v[212:215]\n\t"
      "v_mfma_f32_16x16x32_bf16 v[208:211], v[96:99], v[84:87], v[208:211]\n\t"
      "v_mfma_f32_16x16x32_bf16 v[216:219], v[104:107], v[84:87], v[216:219]\n\t"
      "ds_read_b128 v[76:79], %[vXr] offset:6400\n\t"
      "v_mfma_f32_16x16x32_bf16 v[220:223], v[108:111], v[84:87], v[220:223]\n\t"
      "v_mfma_f32_16x16x32_bf16 v[204:207], v[108:111], v[80:83], v[204:207]\n\t"
      "v_mfma_f32_16x16x32_bf16 v[200:203], v[104:107], v[80:83], v[200:203]\n\t"
      "s_waitcnt lgkmcnt(13)\n\t"
      "v_mfma_f32_16x16x32_bf16 v[232:235], v[104:107], v[88:91], v[232:235]\n\t"
      "ds_read_b128 v[80:83], %[vXr] offset:10240\n\t"
      "v_mfma_f32_16x16x32_bf16 v[236:239], v[108:111], v[88:91], v[236:239]\n\t"
      "buffer_load_dwordx4 %[vL16], %[srdWS], s40 offen offset:3072 sc1 lds\n\t"
      "s_waitcnt lgkmcnt(11)\n\t"
      "v_mfma_f32_16x16x32_bf16 v[252:255], v[108:111], v[92:95], v[252:255]\n\t"
      "v_mfma_f32_16x16x32_bf16 v[248:251], v[104:107], v[92:95], v[248:251]\n\t"
      "v_mfma_f32_16x16x32_bf16 v[240:243], v[96:99], v[92:95], v[240:243]\n\t"
      "ds_read_b128 v[84:87], %[vXr] offset:10496\n\t"
      "v_mfma_f32_16x16x32_bf16 v[244:247], v[100:103], v[92:95], v[244:247]\n\t"
      "v_mfma_f32_16x16x32_bf16 v[228:231], v[100:103], v[88:91], v[228:231]\n\t"
      "v_mfma_f32_16x16x32_bf16 v[224:227], v[96:99], v[88:91], v[224:227]\n\t"
      "s_waitcnt lgkmcnt(9)\n\t"
      "v_mfma_f32_16x16x32_bf16 v[128:131], v[112:115], v[64:67], v[128:131]\n\t"
      "ds_read_b128 v[88:91], %[vXr] offset:14336\n\t"
      "v_mfma_f32_16x16x32_bf16 v[132:135], v[116:119], v[64:67], v[132:135]\n\t"
      "ds_read_b128 v[92:95], %[vXr] offset:14592\n\t"
      "s_waitcnt lgkmcnt(8)\n\t"
      "v_mfma_f32_16x16x32_bf16 v[148:151], v[116:119], v[68:71], v[148:151]\n\t"
      "v_mfma_f32_16x16x32_bf16 v[144:147], v[112:115], v[68:71], v[144:147]\n\t"
      "v_mfma_f32_16x16x32_bf16 v[152:155], v[120:123], v[68:71], v[152:155]\n\t"
      "s_waitcnt lgkmcnt(6)\n\t"
      "v_mfma_f32_16x16x32_bf16 v[156:159], v[124:127], v[68:71], v[156:159]\n\t"
      "v_mfma_f32_16x16x32_bf16 v[140:143], v[124:127], v[64:67], v[140:143]\n\t"
      "v_mfma_f32_16x16x32_bf16 v[136:139], v[120:123], v[64:67], v[136:139]\n\t"
      "s_waitcnt vmcnt(0) lgkmcnt(0)\n\t"
      "s_cmp_lg_u32 %[kvno], 0\n\t"
      "s_cbranch_scc1 P1_NOKF_%=\n\t"
      "v_lshlrev_b32 v56, 9, %[vL16]\n\t"
      "s_lshl_b32 s48, %[sKVi], 5\n\t"
      "s_add_u32 s48, s48, 9633792\n\t"
      "v_mov_b32 v53, %[taglo]\n\t"
      "v_mov_b32 v54, v52\n\t"
      "v_mov_b32 v55, %[taghi]\n\t"
      "s_mov_b64 exec, 0xff\n\t"
      "buffer_store_dwordx4 v[52:55], v56, %[srdWS], s48 offen sc1\n\t"
      "s_mov_b64 exec, -1\n\t"
      "s_nop 3\n\t"
      "P1_NOKF_%=:\n\t"
      "s_barrier\n\t"
      "v_mfma_f32_16x16x32_bf16 v[168:171], v[120:123], v[72:75], v[168:171]\n\t"
      "ds_read_b128 v[64:67], %[vXr] offset:32768\n\t"
      "v_mfma_f32_16x16x32_bf16 v[172:175], v[124:127], v[72:75], v[172:175]\n\t"
      "s_add_u32 m0, s42, 0\n\t"
      "v_lshl_add_u64 v[48:49], %[vWst], 0, v[48:49]\n\t"
      "global_load_lds_dwordx4 v[48:49], off\n\t"
      "v_mfma_f32_16x16x32_bf16 v[188:191], v[124:127], v[76:79], v[188:191]\n\t"
      "v_mfma_f32_16x16x32_bf16 v[184:187], v[120:123], v[76:79], v[184:187]\n\t"
      "v_mfma_f32_16x16x32_bf16 v[176:179], v[112:115], v[76:79], v[176:179]\n\t"
      "ds_read_b128 v[68:71], %[vXr] offset:33024\n\t"
      "v_mfma_f32_16x16x32_bf16 v[180:183], v[116:119], v[76:79], v[180:183]\n\t"
      "ds_read_b64_tr_b16 v[96:97], %[vWr0] offset:32768\n\t"
      "v_mfma_f32_16x16x32_bf16 v[164:167], v[116:119], v[72:75], v[164:167]\n\t"
      "ds_read_b64_tr_b16 v[98:99], %[vWr0] offset:40960\n\t"
      "v_mfma_f32_16x16x32_bf16 v[160:163], v[112:115], v[72:75], v[160:163]\n\t"
      "v_mfma_f32_16x16x32_bf16 v[192:195], v[112:115], v[80:83], v[192:195]\n\t"
      "ds_read_b128 v[72:75], %[vXr] offset:36864\n\t"
      "v_mfma_f32_16x16x32_bf16 v[196:199], v[116:119], v[80:83], v[196:199]\n\t"
      "ds_read_b64_tr_b16 v[100:101], %[vWr1] offset:32768\n\t"
      "v_mfma_f32_16x16x32_bf16 v[212:215], v[116:119], v[84:87], v[212:215]\n\t"
      "ds_read_b64_tr_b16 v[102:103], %[vWr1] offset:40960\n\t"
      "v_mfma_f32_16x16x32_bf16 v[208:211], v[112:115], v[84:87], v[208:211]\n\t"
      "s_add_u32 m0, s42, 8192\n\t"
      "v_lshl_add_u64 v[48:49], %[vWst], 0, v[48:49]\n\t"
      "global_load_lds_dwordx4 v[48:49], off\n\t"
      "v_mfma_f32_16x16x32_bf16 v[216:219], v[120:123], v[84:87], v[216:219]\n\t"
      "ds_read_b128 v[76:79], %[vXr] offset:37120\n\t"
      "v_mfma_f32_16x16x32_bf16 v[220:223], v[124:127], v[84:87], v[220:223]\n\t"
      "ds_read_b64_tr_b16 v[104:105], %[vWr0] offset:33024\n\t"
      "v_mfma_f32_16x16x32_bf16 v[204:207], v[124:127], v[80:83], v[204:207]\n\t"
      "ds_read_b64_tr_b16 v[106:107], %[vWr0] offset:41216\n\t"
      "v_mfma_f32_16x16x32_bf16 v[200:203], v[120:123], v[80:83], v[200:203]\n\t"
      "v_mfma_f32_16x16x32_bf16 v[232:235], v[120:123], v[88:91], v[232:235]\n\t"
      "ds_read_b128 v[80:83], %[vXr] offset:40960\n\t"
      "v_mfma_f32_16x16x32_bf16 v[236:239], v[124:127], v[88:91], v[236:239]\n\t"
      "ds_read_b64_tr_b16 v[108:109], %[vWr1] offset:33024\n\t"
      "v_mfma_f32_16x16x32_bf16 v[252:255], v[124:127], v[92:95], v[252:255]\n\t"
      "ds_read_b64_tr_b16 v[110:111], %[vWr1] offset:41216\n\t"
      "v_mfma_f32_16x16x32_bf16 v[248:251], v[120:123], v[92:95], v[248:251]\n\t"
      "s_add_u32 m0, s42, 16384\n\t"
      "v_lshl_add_u64 v[48:49], %[vWst], 0, v[48:49]\n\t"
      "global_load_lds_dwordx4 v[48:49], off\n\t"
      "v_mfma_f32_16x16x32_bf16 v[240:243], v[112:115], v[92:95], v[240:243]\n\t"
      "ds_read_b128 v[84:87], %[vXr] offset:41216\n\t"
      "v_mfma_f32_16x16x32_bf16 v[244:247], v[116:119], v[92:95], v[244:247]\n\t"
      "s_add_u32 m0, s42, 24576\n\t"
      "v_lshl_add_u64 v[48:49], %[vWst], 0, v[48:49]\n\t"
      "global_load_lds_dwordx4 v[48:49], off\n\t"
      "v_mfma_f32_16x16x32_bf16 v[228:231], v[116:119], v[88:91], v[228:231]\n\t"
      "v_mfma_f32_16x16x32_bf16 v[224:227], v[112:115], v[88:91], v[224:227]\n\t"
      "s_waitcnt lgkmcnt(10)\n\t"
      "v_mfma_f32_16x16x32_bf16 v[128:131], v[96:99], v[64:67], v[128:131]\n\t"
      "ds_read_b128 v[88:91], %[vXr] offset:45056\n\t"
      "s_waitcnt lgkmcnt(8)\n\t"
      "v_mfma_f32_16x16x32_bf16 v[132:135], v[100:103], v[64:67], v[132:135]\n\t"
      "ds_read_b64_tr_b16 v[112:113], %[vWr0] offset:49152\n\t"
      "v_mfma_f32_16x16x32_bf16 v[148:151], v[100:103], v[68:71], v[148:151]\n\t"
      "ds_read_b64_tr_b16 v[114:115], %[vWr0] offset:57344\n\t"
      "v_mfma_f32_16x16x32_bf16 v[144:147], v[96:99], v[68:71], v[144:147]\n\t"
      "s_add_u32 m0, s41, 0\n\t"
      "s_add_u32 s40, s40, 4096\n\t"
      "buffer_load_dwordx4 %[vL16], %[srdWS], s40 offen offset:0 sc1 lds\n\t"
      "s_waitcnt lgkmcnt(7)\n\t"
      "v_mfma_f32_16x16x32_bf16 v[152:155], v[104:107], v[68:71], v[152:155]\n\t"
      "ds_read_b128 v[92:95], %[vXr] offset:45312\n\t"
      "s_waitcnt lgkmcnt(5)\n\t"
      "v_mfma_f32_16x16x32_bf16 v[156:159], v[108:111], v[68:71], v[156:159]\n\t"
      "ds_read_b64_tr_b16 v[116:117], %[vWr1] offset:49152\n\t"
      "v_mfma_f32_16x16x32_bf16 v[140:143], v[108:111], v[64:67], v[140:143]\n\t"
      "ds_read_b64_tr_b16 v[118:119], %[vWr1] offset:57344\n\t"
      "v_mfma_f32_16x16x32_bf16 v[136:139], v[104:107], v[64:67], v[136:139]\n\t"
      "v_mfma_f32_16x16x32_bf16 v[168:171], v[104:107], v[72:75], v[168:171]\n\t"
      "ds_read_b128 v[64:67], %[vXr] offset:34816\n\t"
      "v_mfma_f32_16x16x32_bf16 v[172:175], v[108:111], v[72:75], v[172:175]\n\t"
      "ds_read_b64_tr_b16 v[120:121], %[vWr0] offset:49408\n\t"
      "v_mfma_f32_16x16x32_bf16 v[188:191], v[108:111], v[76:79], v[188:191]\n\t"
      "ds_read_b64_tr_b16 v[122:123], %[vWr0] offset:57600\n\t"
      "v_mfma_f32_16x16x32_bf16 v[184:187], v[104:107], v[76:79], v[184:187]\n\t"
      "buffer_load_dwordx4 %[vL16], %[srdWS], s40 offen offset:1024 sc1 lds\n\t"
      "v_mfma_f32_16x16x32_bf16 v[176:179], v[96:99], v[76:79], v[176:179]\n\t"
      "ds_read_b128 v[68:71], %[vXr] offset:35072\n\t"
      "v_mfma_f32_16x16x32_bf16 v[180:183], v[100:103], v[76:79], v[180:183]\n\t"
      "ds_read_b64_tr_b16 v[124:125], %[vWr1] offset:49408\n\t"
      "v_mfma_f32_16x16x32_bf16 v[164:167], v[100:103], v[72:75], v[164:167]\n\t"
      "ds_read_b64_tr_b16 v[126:127], %[vWr1] offset:57600\n\t"
      "v_mfma_f32_16x16x32_bf16 v[160:163], v[96:99], v[72:75], v[160:163]\n\t"
      "v_mfma_f32_16x16x32_bf16 v[192:195], v[96:99], v[80:83], v[192:195]\n\t"
      "ds_read_b128 v[72:75], %[vXr] offset:38912\n\t"
      "v_mfma_f32_16x16x32_bf16 v[196:199], v[100:103], v[80:83], v[196:199]\n\t"
      "buffer_load_dwordx4 %[vL16], %[srdWS], s40 offen offset:2048 sc1 lds\n\t"
      "s_waitcnt lgkmcnt(13)\n\t"
      "v_mfma_f32_16x16x32_bf16 v[212:215], v[100:103], v[84:87], v[212:215]\n\t"
      "v_mfma_f32_16x16x32_bf16 v[208:211], v[96:99], v[84:87], v[208:211]\n\t"
      "v_mfma_f32_16x16x32_bf16 v[216:219], v[104:107], v[84:87], v[216:219]\n\t"
      "ds_read_b128 v[76:79], %[vXr] offset:39168\n\t"
      "v_mfma_f32_16x16x32_bf16 v[220:223], v[108:111], v[84:87], v[220:223]\n\t"
      "v_mfma_f32_16x16x32_bf16 v[204:207], v[108:111], v[80:83], v[204:207]\n\t"
      "v_mfma_f32_16x16x32_bf16 v[200:203], v[104:107], v[80:83], v[200:203]\n\t"
      "s_waitcnt lgkmcnt(13)\n\t"
      "v_mfma_f32_16x16x32_bf16 v[232:235], v[104:107], v[88:91], v[232:235]\n\t"
      "ds_read_b128 v[80:83], %[vXr] offset:43008\n\t"
      "v_mfma_f32_16x16x32_bf16 v[236:239], v[108:111], v[88:91], v[236:239]\n\t"
      "buffer_load_dwordx4 %[vL16], %[srdWS], s40 offen offset:3072 sc1 lds\n\t"
      "s_waitcnt lgkmcnt(11)\n\t"
      "v_mfma_f32_16x16x32_bf16 v[252:255], v[108:111], v[92:95], v[252:255]\n\t"
      "v_mfma_f32_16x16x32_bf16 v[248:251], v[104:107], v[92:95], v[248:251]\n\t"
      "v_mfma_f32_16x16x32_bf16 v[240:243], v[96:99], v[92:95], v[240:243]\n\t"
      "ds_read_b128 v[84:87], %[vXr] offset:43264\n\t"
      "v_mfma_f32_16x16x32_bf16 v[244:247], v[100:103], v[92:95], v[244:247]\n\t"
      "v_mfma_f32_16x16x32_bf16 v[228:231], v[100:103], v[88:91], v[228:231]\n\t"
      "v_mfma_f32_16x16x32_bf16 v[224:227], v[96:99], v[88:91], v[224:227]\n\t"
      "s_waitcnt lgkmcnt(9)\n\t"
      "v_mfma_f32_16x16x32_bf16 v[128:131], v[112:115], v[64:67], v[128:131]\n\t"
      "ds_read_b128 v[88:91], %[vXr] offset:47104\n\t"
      "v_mfma_f32_16x16x32_bf16 v[132:135], v[116:119], v[64:67], v[132:135]\n\t"
      "ds_read_b128 v[92:95], %[vXr] offset:47360\n\t"
      "s_waitcnt lgkmcnt(8)\n\t"
      "v_mfma_f32_16x16x32_bf16 v[148:151], v[116:119], v[68:71], v[148:151]\n\t"
      "v_mfma_f32_16x16x32_bf16 v[144:147], v[112:115], v[68:71], v[144:147]\n\t"
      "v_mfma_f32_16x16x32_bf16 v[152:155], v[120:123], v[68:71], v[152:155]\n\t"
      "s_waitcnt lgkmcnt(6)\n\t"
      "v_mfma_f32_16x16x32_bf16 v[156:159], v[124:127], v[68:71], v[156:159]\n\t"
      "v_mfma_f32_16x16x32_bf16 v[140:143], v[124:127], v[64:67], v[140:143]\n\t"
      "v_mfma_f32_16x16x32_bf16 v[136:139], v[120:123], v[64:67], v[136:139]\n\t"
      "s_waitcnt vmcnt(0) lgkmcnt(0)\n\t"
      "s_barrier\n\t"
      "v_mfma_f32_16x16x32_bf16 v[168:171], v[120:123], v[72:75], v[168:171]\n\t"
      "ds_read_b128 v[64:67], %[vXr] offset:0\n\t"
      "v_mfma_f32_16x16x32_bf16 v[172:175], v[124:127], v[72:75], v[172:175]\n\t"
      "s_add_u32 m0, s42, 32768\n\t"
      "v_lshl_add_u64 v[48:49], %[vWst], 0, v[48:49]\n\t"
      "global_load_lds_dwordx4 v[48:49], off\n\t"
      "v_mfma_f32_16x16x32_bf16 v[188:191], v[124:127], v[76:79], v[188:191]\n\t"
      "v_mfma_f32_16x16x32_bf16 v[184:187], v[120:123], v[76:79], v[184:187]\n\t"
      "v_mfma_f32_16x16x32_bf16 v[176:179], v[112:115], v[76:79], v[176:179]\n\t"
      "ds_read_b128 v[68:71], %[vXr] offset:256\n\t"
      "v_mfma_f32_16x16x32_bf16 v[180:183], v[116:119], v[76:79], v[180:183]\n\t"
      "ds_read_b64_tr_b16 v[96:97], %[vWr0] offset:0\n\t"
      "v_mfma_f32_16x16x32_bf16 v[164:167], v[116:119], v[72:75], v[164:167]\n\t"
      "ds_read_b64_tr_b16 v[98:99], %[vWr0] offset:8192\n\t"
      "v_mfma_f32_16x16x32_bf16 v[160:163], v[112:115], v[72:75], v[160:163]\n\t"
      "v_mfma_f32_16x16x32_bf16 v[192:195], v[112:115], v[80:83], v[192:195]\n\t"
      "ds_read_b128 v[72:75], %[vXr] offset:4096\n\t"
      "v_mfma_f32_16x16x32_bf16 v[196:199], v[116:119], v[80:83], v[196:199]\n\t"
      "ds_read_b64_tr_b16 v[100:101], %[vWr1] offset:0\n\t"
      "v_mfma_f32_16x16x32_bf16 v[212:215], v[116:119], v[84:87], v[212:215]\n\t"
      "ds_read_b64_tr_b16 v[102:103], %[vWr1] offset:8192\n\t"
      "v_mfma_f32_16x16x32_bf16 v[208:211], v[112:115], v[84:87], v[208:211]\n\t"
      "s_add_u32 m0, s42, 40960\n\t"
      "v_lshl_add_u64 v[48:49], %[vWst], 0, v[48:49]\n\t"
      "global_load_lds_dwordx4 v[48:49], off\n\t"
      "v_mfma_f32_16x16x32_bf16 v[216:219], v[120:123], v[84:87], v[216:219]\n\t"
      "ds_read_b128 v[76:79], %[vXr] offset:4352\n\t"
      "v_mfma_f32_16x16x32_bf16 v[220:223], v[124:127], v[84:87], v[220:223]\n\t"
      "ds_read_b64_tr_b16 v[104:105], %[vWr0] offset:256\n\t"
      "v_mfma_f32_16x16x32_bf16 v[204:207], v[124:127], v[80:83], v[204:207]\n\t"
      "ds_read_b64_tr_b16 v[106:107], %[vWr0] offset:8448\n\t"
      "v_mfma_f32_16x16x32_bf16 v[200:203], v[120:123], v[80:83], v[200:203]\n\t"
      "v_mfma_f32_16x16x32_bf16 v[232:235], v[120:123], v[88:91], v[232:235]\n\t"
      "ds_read_b128 v[80:83], %[vXr] offset:8192\n\t"
      "v_mfma_f32_16x16x32_bf16 v[236:239], v[124:127], v[88:91], v[236:239]\n\t"
      "ds_read_b64_tr_b16 v[108:109], %[vWr1] offset:256\n\t"
      "v_mfma_f32_16x16x32_bf16 v[252:255], v[124:127], v[92:95], v[252:255]\n\t"
      "ds_read_b64_tr_b16 v[110:111], %[vWr1] offset:8448\n\t"
      "v_mfma_f32_16x16x32_bf16 v[248:251], v[120:123], v[92:95], v[248:251]\n\t"
      "s_add_u32 m0, s42, 49152\n\t"
      "v_lshl_add_u64 v[48:49], %[vWst], 0, v[48:49]\n\t"
      "global_load_lds_dwordx4 v[48:49], off\n\t"
      "v_mfma_f32_16x16x32_bf16 v[240:243], v[112:115], v[92:95], v[240:243]\n\t"
      "ds_read_b128 v[84:87], %[vXr] offset:8448\n\t"
      "v_mfma_f32_16x16x32_bf16 v[244:247], v[116:119], v[92:95], v[244:247]\n\t"
      "s_add_u32 m0, s42, 57344\n\t"
      "v_lshl_add_u64 v[48:49], %[vWst], 0, v[48:49]\n\t"
      "global_load_lds_dwordx4 v[48:49], off\n\t"
      "v_mfma_f32_16x16x32_bf16 v[228:231], v[116:119], v[88:91], v[228:231]\n\t"
      "v_mfma_f32_16x16x32_bf16 v[224:227], v[112:115], v[88:91], v[224:227]\n\t"
      "P1_T14O_%=:\n\t"
      "s_waitcnt lgkmcnt(10)\n\t"
      "v_mfma_f32_16x16x32_bf16 v[128:131], v[96:99], v[64:67], v[128:131]\n\t"
      "ds_read_b128 v[88:91], %[vXr] offset:12288\n\t"
      "s_waitcnt lgkmcnt(8)\n\t"
      "v_mfma_f32_16x16x32_bf16 v[132:135], v[100:103], v[64:67], v[132:135]\n\t"
      "ds_read_b64_tr_b16 v[112:113], %[vWr0] offset:16384\n\t"
      "v_mfma_f32_16x16x32_bf16 v[148:151], v[100:103], v[68:71], v[148:151]\n\t"
      "ds_read_b64_tr_b16 v[114:115], %[vWr0] offset:24576\n\t"
      "v_mfma_f32_16x16x32_bf16 v[144:147], v[96:99], v[68:71], v[144:147]\n\t"
      "s_add_u32 m0, s41, 32768\n\t"
      "s_add_u32 s40, s40, 4096\n\t"
      "buffer_load_dwordx4 %[vL16], %[srdWS], s40 offen offset:0 sc1 lds\n\t"
      "s_waitcnt lgkmcnt(7)\n\t"
      "v_mfma_f32_16x16x32_bf16 v[152:155], v[104:107], v[68:71], v[152:155]\n\t"
      "ds_read_b128 v[92:95], %[vXr] offset:12544\n\t"
      "s_waitcnt lgkmcnt(5)\n\t"
      "v_mfma_f32_16x16x32_bf16 v[156:159], v[108:111], v[68:71], v[156:159]\n\t"
      "ds_read_b64_tr_b16 v[116:117], %[vWr1] offset:16384\n\t"
      "v_mfma_f32_16x16x32_bf16 v[140:143], v[108:111], v[64:67], v[140:143]\n\t"
      "ds_read_b64_tr_b16 v[118:119], %[vWr1] offset:24576\n\t"
      "v_mfma_f32_16x16x32_bf16 v[136:139], v[104:107], v[64:67], v[136:139]\n\t"
      "v_mfma_f32_16x16x32_bf16 v[168:171], v[104:107], v[72:75], v[168:171]\n\t"
      "ds_read_b128 v[64:67], %[vXr] offset:2048\n\t"
      "v_mfma_f32_16x16x32_bf16 v[172:175], v[108:111], v[72:75], v[172:175]\n\t"
      "ds_read_b64_tr_b16 v[120:121], %[vWr0] offset:16640\n\t"
      "v_mfma_f32_16x16x32_bf16 v[188:191], v[108:111], v[76:79], v[188:191]\n\t"
      "ds_read_b64_tr_b16 v[122:123], %[vWr0] offset:24832\n\t"
      "v_mfma_f32_16x16x32_bf16 v[184:187], v[104:107], v[76:79], v[184:187]\n\t"
      "buffer_load_dwordx4 %[vL16], %[srdWS], s40 offen offset:1024 sc1 lds\n\t"
      "v_mfma_f32_16x16x32_bf16 v[176:179], v[96:99], v[76:79], v[176:179]\n\t"
      "ds_read_b128 v[68:71], %[vXr] offset:2304\n\t"
      "v_mfma_f32_16x16x32_bf16 v[180:183], v[100:103], v[76:79], v[180:183]\n\t"
      "ds_read_b64_tr_b16 v[124:125], %[vWr1] offset:16640\n\t"
      "v_mfma_f32_16x16x32_bf16 v[164:167], v[100:103], v[72:75], v[164:167]\n\t"
      "ds_read_b64_tr_b16 v[126:127], %[vWr1] offset:24832\n\t"
      "v_mfma_f32_16x16x32_bf16 v[160:163], v[96:99], v[72:75], v[160:163]\n\t"
      "buffer_load_dwordx4 v[32:35], %[vKi], %[srdWS], %[sPkv] offen sc1\n\t"
      "v_mfma_f32_16x16x32_bf16 v[192:195], v[96:99], v[80:83], v[192:195]\n\t"
      "ds_read_b128 v[72:75], %[vXr] offset:6144\n\t"
      "v_mfma_f32_16x16x32_bf16 v[196:199], v[100:103], v[80:83], v[196:199]\n\t"
      "s_lshl_b32 s48, %[w], 2\n\t"
      "s_add_u32 s48, s48, 131072\n\t"
      "v_lshrrev_b32 v56, 9, %[vL16]\n\t"
      "v_lshl_add_u32 v56, v56, 4, s48\n\t"
      "v_mov_b32 v57, -1\n\t"
      "ds_write_b32 v56, v57\n\t"
      "v_mov_b32 v58, 131072\n\t"
      "v_mov_b32 v59, 0\n\t"
      "ds_write_b32 v58, v59 offset:32\n\t"
      "s_waitcnt lgkmcnt(14)\n\t"
      "v_mfma_f32_16x16x32_bf16 v[212:215], v[100:103], v[84:87], v[212:215]\n\t"
      "buffer_load_dwordx4 %[vL16], %[srdWS], s40 offen offset:2048 sc1 lds\n\t"
      "v_mfma_f32_16x16x32_bf16 v[208:211], v[96:99], v[84:87], v[208:211]\n\t"
      "v_mfma_f32_16x16x32_bf16 v[216:219], v[104:107], v[84:87], v[216:219]\n\t"
      "ds_read_b128 v[76:79], %[vXr] offset:6400\n\t"
      "v_mfma_f32_16x16x32_bf16 v[220:223], v[108:111], v[84:87], v[220:223]\n\t"
      "s_add_u32 s52, %[wslo], 8388608\n\t"
      "s_addc_u32 s53, %[wshi], 0\n\t"
      "s_mov_b32 s54, 262144\n\t"
      "s_mov_b32 s55, 0x20000\n\t"
      "s_lshl_b32 s46, %[w], 12\n\t"
      "s_sub_u32 s45, s46, 16384\n\t"
      "s_add_u32 s45, s45, %[kr]\n\t"
      "v_mfma_f32_16x16x32_bf16 v[204:207], v[108:111], v[80:83], v[204:207]\n\t"
      "v_mfma_f32_16x16x32_bf16 v[200:203], v[104:107], v[80:83], v[200:203]\n\t"
      "v_mfma_f32_16x16x32_bf16 v[232:235], v[104:107], v[88:91], v[232:235]\n\t"
      "ds_read_b128 v[80:83], %[vXr] offset:10240\n\t"
      "v_mfma_f32_16x16x32_bf16 v[236:239], v[108:111], v[88:91], v[236:239]\n\t"
      "buffer_load_dwordx4 %[vL16], %[srdWS], s40 offen offset:3072 sc1 lds\n\t"
      "s_waitcnt lgkmcnt(13)\n\t"
      "v_mfma_f32_16x16x32_bf16 v[252:255], v[108:111], v[92:95], v[252:255]\n\t"
      "v_mfma_f32_16x16x32_bf16 v[248:251], v[104:107], v[92:95], v[248:251]\n\t"
      "v_mfma_f32_16x16x32_bf16 v[240:243], v[96:99], v[92:95], v[240:243]\n\t"
      "ds_read_b128 v[84:87], %[vXr] offset:10496\n\t"
      "v_mfma_f32_16x16x32_bf16 v[244:247], v[100:103], v[92:95], v[244:247]\n\t"
      "v_mfma_f32_16x16x32_bf16 v[228:231], v[100:103], v[88:91], v[228:231]\n\t"
      "v_mfma_f32_16x16x32_bf16 v[224:227], v[96:99], v[88:91], v[224:227]\n\t"
      "s_waitcnt lgkmcnt(11)\n\t"
      "v_mfma_f32_16x16x32_bf16 v[128:131], v[112:115], v[64:67], v[128:131]\n\t"
      "ds_read_b128 v[88:91], %[vXr] offset:14336\n\t"
      "v_mfma_f32_16x16x32_bf16 v[132:135], v[116:119], v[64:67], v[132:135]\n\t"
      "ds_read_b128 v[92:95], %[vXr] offset:14592\n\t"
      "s_waitcnt lgkmcnt(10)\n\t"
      "v_mfma_f32_16x16x32_bf16 v[148:151], v[116:119], v[68:71], v[148:151]\n\t"
      "v_mfma_f32_16x16x32_bf16 v[144:147], v[112:115], v[68:71], v[144:147]\n\t"
      "v_mfma_f32_16x16x32_bf16 v[152:155], v[120:123], v[68:71], v[152:155]\n\t"
      "s_waitcnt lgkmcnt(8)\n\t"
      "v_mfma_f32_16x16x32_bf16 v[156:159], v[124:127], v[68:71], v[156:159]\n\t"
      "v_mfma_f32_16x16x32_bf16 v[140:143], v[124:127], v[64:67], v[140:143]\n\t"
      "v_mfma_f32_16x16x32_bf16 v[136:139], v[120:123], v[64:67], v[136:139]\n\t"
      "s_waitcnt vmcnt(0) lgkmcnt(0)\n\t"
      "s_mov_b32 s49, 0\n\t"
      "s_branch P1_PK_CHK_%=\n\t"
      "P1_PK_%=:\n\t"
      "buffer_load_dwordx4 v[32:35], %[vKi], %[srdWS], %[sPkv] offen sc1\n\t"
      "s_waitcnt vmcnt(0)\n\t"
      "P1_PK_CHK_%=:\n\t"
      "v_cmp_eq_u32_e64 s[56:57], s50, v33\n\t"
      "v_cmp_eq_u32 vcc, s51, v35\n\t"
      "s_add_u32 s49, s49, 1\n\t"
      "s_and_b64 vcc, vcc, s[56:57]\n\t"
      "s_cmp_eq_u64 vcc, -1\n\t"
      "s_cbranch_scc1 P1_PK_OK_%=\n\t"
      "s_cmp_lt_u32 s49, 0x40000\n\t"
      "s_cbranch_scc1 P1_PK_%=\n\t"
      "P1_PK_OK_%=:\n\t"
      "v_max_u32 v51, v32, v34\n\t"
      "s_barrier\n\t"
      "v_mfma_f32_16x16x32_bf16 v[168:171], v[120:123], v[72:75], v[168:171]\n\t"
      "ds_read_b128 v[64:67], %[vXr] offset:32768\n\t"
      "v_mfma_f32_16x16x32_bf16 v[172:175], v[124:127], v[72:75], v[172:175]\n\t"
      "s_add_u32 m0, s46, 0\n\t"
      "s_add_u32 s45, s45, 16384\n\t"
      "s_bitset0_b32 s45, 18\n\t"
      "buffer_load_dwordx4 %[vL16], s[52:55], s45 offen offset:0 sc1 lds\n\t"
      "v_mfma_f32_16x16x32_bf16 v[188:191], v[124:127], v[76:79], v[188:191]\n\t"
      "v_mfma_f32_16x16x32_bf16 v[184:187], v[120:123], v[76:79], v[184:187]\n\t"
      "v_mfma_f32_16x16x32_bf16 v[176:179], v[112:115], v[76:79], v[176:179]\n\t"
      "ds_read_b128 v[68:71], %[vXr] offset:33024\n\t"
      "v_mfma_f32_16x16x32_bf16 v[180:183], v[116:119], v[76:79], v[180:183]\n\t"
      "ds_read_b64_tr_b16 v[96:97], %[vWr0] offset:32768\n\t"
      "v_mfma_f32_16x16x32_bf16 v[164:167], v[116:119], v[72:75], v[164:167]\n\t"
      "ds_read_b64_tr_b16 v[98:99], %[vWr0] offset:40960\n\t"
      "v_mfma_f32_16x16x32_bf16 v[160:163], v[112:115], v[72:75], v[160:163]\n\t"
      "buffer_load_dwordx4 %[vL16], s[52:55], s45 offen offset:1024 sc1 lds\n\t"
      "v_mfma_f32_16x16x32_bf16 v[192:195], v[112:115], v[80:83], v[192:195]\n\t"
      "ds_read_b128 v[72:75], %[vXr] offset:36864\n\t"
      "v_mfma_f32_16x16x32_bf16 v[196:199], v[116:119], v[80:83], v[196:199]\n\t"
      "ds_read_b64_tr_b16 v[100:101], %[vWr1] offset:32768\n\t"
      "v_mfma_f32_16x16x32_bf16 v[212:215], v[116:119], v[84:87], v[212:215]\n\t"
      "ds_read_b64_tr_b16 v[102:103], %[vWr1] offset:40960\n\t"
      "v_mfma_f32_16x16x32_bf16 v[208:211], v[112:115], v[84:87], v[208:211]\n\t"
      "buffer_load_dwordx4 %[vL16], s[52:55], s45 offen offset:2048 sc1 lds\n\t"
      "v_mfma_f32_16x16x32_bf16 v[216:219], v[120:123], v[84:87], v[216:219]\n\t"
      "ds_read_b128 v[76:79], %[vXr] offset:37120\n\t"
      "v_mfma_f32_16x16x32_bf16 v[220:223], v[124:127], v[84:87], v[220:223]\n\t"
      "ds_read_b64_tr_b16 v[104:105], %[vWr0] offset:33024\n\t"
      "v_mfma_f32_16x16x32_bf16 v[204:207], v[124:127], v[80:83], v[204:207]\n\t"
      "ds_read_b64_tr_b16 v[106:107], %[vWr0] offset:41216\n\t"
      "v_mfma_f32_16x16x32_bf16 v[200:203], v[120:123], v[80:83], v[200:203]\n\t"
      "buffer_load_dwordx4 %[vL16], s[52:55], s45 offen offset:3072 sc1 lds\n\t"
      "v_mfma_f32_16x16x32_bf16 v[232:235], v[120:123], v[88:91], v[232:235]\n\t"
      "ds_read_b128 v[80:83], %[vXr] offset:40960\n\t"
      "v_mfma_f32_16x16x32_bf16 v[236:239], v[124:127], v[88:91], v[236:239]\n\t"
      "ds_read_b64_tr_b16 v[108:109], %[vWr1] offset:33024\n\t"
      "v_mfma_f32_16x16x32_bf16 v[252:255], v[124:127], v[92:95], v[252:255]\n\t"
      "ds_read_b64_tr_b16 v[110:111], %[vWr1] offset:41216\n\t"
      "v_mfma_f32_16x16x32_bf16 v[248:251], v[120:123], v[92:95], v[248:251]\n\t"
      "s_add_u32 m0, s46, 16384\n\t"
      "s_add_u32 s45, s45, 16384\n\t"
      "s_bitset0_b32 s45, 18\n\t"
      "buffer_load_dwordx4 %[vL16], s[52:55], s45 offen offset:0 sc1 lds\n\t"
      "v_mfma_f32_16x16x32_bf16 v[240:243], v[112:115], v[92:95], v[240:243]\n\t"
      "ds_read_b128 v[84:87], %[vXr] offset:41216\n\t"
      "v_mfma_f32_16x16x32_bf16 v[244:247], v[116:119], v[92:95], v[244:247]\n\t"
      "buffer_load_dwordx4 %[vL16], s[52:55], s45 offen offset:1024 sc1 lds\n\t"
      "v_mfma_f32_16x16x32_bf16 v[228:231], v[116:119], v[88:91], v[228:231]\n\t"
      "v_mfma_f32_16x16x32_bf16 v[224:227], v[112:115], v[88:91], v[224:227]\n\t"
      "s_waitcnt lgkmcnt(10)\n\t"
      "v_mfma_f32_16x16x32_bf16 v[128:131], v[96:99], v[64:67], v[128:131]\n\t"
      "ds_read_b128 v[88:91], %[vXr] offset:45056\n\t"
      "s_waitcnt lgkmcnt(8)\n\t"
      "v_mfma_f32_16x16x32_bf16 v[132:135], v[100:103], v[64:67], v[132:135]\n\t"
      "ds_read_b64_tr_b16 v[112:113], %[vWr0] offset:49152\n\t"
      "v_mfma_f32_16x16x32_bf16 v[148:151], v[100:103], v[68:71], v[148:151]\n\t"
      "ds_read_b64_tr_b16 v[114:115], %[vWr0] offset:57344\n\t"
      "v_mfma_f32_16x16x32_bf16 v[144:147], v[96:99], v[68:71], v[144:147]\n\t"
      "buffer_load_dwordx4 %[vL16], s[52:55], s45 offen offset:2048 sc1 lds\n\t"
      "s_waitcnt lgkmcnt(7)\n\t"
      "v_mfma_f32_16x16x32_bf16 v[152:155], v[104:107], v[68:71], v[152:155]\n\t"
      "ds_read_b128 v[92:95], %[vXr] offset:45312\n\t"
      "s_waitcnt lgkmcnt(5)\n\t"
      "v_mfma_f32_16x16x32_bf16 v[156:159], v[108:111], v[68:71], v[156:159]\n\t"
      "ds_read_b64_tr_b16 v[116:117], %[vWr1] offset:49152\n\t"
      "v_mfma_f32_16x16x32_bf16 v[140:143], v[108:111], v[64:67], v[140:143]\n\t"
      "ds_read_b64_tr_b16 v[118:119], %[vWr1] offset:57344\n\t"
      "v_mfma_f32_16x16x32_bf16 v[136:139], v[104:107], v[64:67], v[136:139]\n\t"
      "buffer_load_dwordx4 %[vL16], s[52:55], s45 offen offset:3072 sc1 lds\n\t"
      "v_mfma_f32_16x16x32_bf16 v[168:171], v[104:107], v[72:75], v[168:171]\n\t"
      "ds_read_b128 v[64:67], %[vXr] offset:34816\n\t"
      "v_mfma_f32_16x16x32_bf16 v[172:175], v[108:111], v[72:75], v[172:175]\n\t"
      "ds_read_b64_tr_b16 v[120:121], %[vWr0] offset:49408\n\t"
      "v_mfma_f32_16x16x32_bf16 v[188:191], v[108:111], v[76:79], v[188:191]\n\t"
      "ds_read_b64_tr_b16 v[122:123], %[vWr0] offset:57600\n\t"
      "v_mfma_f32_16x16x32_bf16 v[184:187], v[104:107], v[76:79], v[184:187]\n\t"
      "s_add_u32 m0, s46, 65536\n\t"
      "s_add_u32 s45, s45, 16384\n\t"
      "s_bitset0_b32 s45, 18\n\t"
      "buffer_load_dwordx4 %[vL16], s[52:55], s45 offen offset:0 sc1 lds\n\t"
      "v_mfma_f32_16x16x32_bf16 v[176:179], v[96:99], v[76:79], v[176:179]\n\t"
      "ds_read_b128 v[68:71], %[vXr] offset:35072\n\t"
      "v_mfma_f32_16x16x32_bf16 v[180:183], v[100:103], v[76:79], v[180:183]\n\t"
      "ds_read_b64_tr_b16 v[124:125], %[vWr1] offset:49408\n\t"
      "v_mfma_f32_16x16x32_bf16 v[164:167], v[100:103], v[72:75], v[164:167]\n\t"
      "ds_read_b64_tr_b16 v[126:127], %[vWr1] offset:57600\n\t"
      "v_mfma_f32_16x16x32_bf16 v[160:163], v[96:99], v[72:75], v[160:163]\n\t"
      "buffer_load_dwordx4 %[vL16], s[52:55], s45 offen offset:1024 sc1 lds\n\t"
      "v_mfma_f32_16x16x32_bf16 v[192:195], v[96:99], v[80:83], v[192:195]\n\t"
      "ds_read_b128 v[72:75], %[vXr] offset:38912\n\t"
      "v_mfma_f32_16x16x32_bf16 v[196:199], v[100:103], v[80:83], v[196:199]\n\t"
      "buffer_load_dwordx4 %[vL16], s[52:55], s45 offen offset:2048 sc1 lds\n\t"
      "s_waitcnt lgkmcnt(13)\n\t"
      "v_mfma_f32_16x16x32_bf16 v[212:215], v[100:103], v[84:87], v[212:215]\n\t"
      "v_mfma_f32_16x16x32_bf16 v[208:211], v[96:99], v[84:87], v[208:211]\n\t"
      "v_mfma_f32_16x16x32_bf16 v[216:219], v[104:107], v[84:87], v[216:219]\n\t"
      "ds_read_b128 v[76:79], %[vXr] offset:39168\n\t"
      "v_mfma_f32_16x16x32_bf16 v[220:223], v[108:111], v[84:87], v[220:223]\n\t"
      "buffer_load_dwordx4 %[vL16], s[52:55], s45 offen offset:3072 sc1 lds\n\t"
      "v_mfma_f32_16x16x32_bf16 v[204:207], v[108:111], v[80:83], v[204:207]\n\t"
      "v_mfma_f32_16x16x32_bf16 v[200:203], v[104:107], v[80:83], v[200:203]\n\t"
      "s_waitcnt lgkmcnt(13)\n\t"
      "v_mfma_f32_16x16x32_bf16 v[232:235], v[104:107], v[88:91], v[232:235]\n\t"
      "ds_read_b128 v[80:83], %[vXr] offset:43008\n\t"
      "v_mfma_f32_16x16x32_bf16 v[236:239], v[108:111], v[88:91], v[236:239]\n\t"
      "s_waitcnt lgkmcnt(11)\n\t"
      "v_mfma_f32_16x16x32_bf16 v[252:255], v[108:111], v[92:95], v[252:255]\n\t"
      "v_mfma_f32_16x16x32_bf16 v[248:251], v[104:107], v[92:95], v[248:251]\n\t"
      "v_mfma_f32_16x16x32_bf16 v[240:243], v[96:99], v[92:95], v[240:243]\n\t"
      "ds_read_b128 v[84:87], %[vXr] offset:43264\n\t"
      "v_mfma_f32_16x16x32_bf16 v[244:247], v[100:103], v[92:95], v[244:247]\n\t"
      "v_mfma_f32_16x16x32_bf16 v[228:231], v[100:103], v[88:91], v[228:231]\n\t"
      "v_mfma_f32_16x16x32_bf16 v[224:227], v[96:99], v[88:91], v[224:227]\n\t"
      "s_waitcnt lgkmcnt(9)\n\t"
      "v_mfma_f32_16x16x32_bf16 v[128:131], v[112:115], v[64:67], v[128:131]\n\t"
      "ds_read_b128 v[88:91], %[vXr] offset:47104\n\t"
      "v_mfma_f32_16x16x32_bf16 v[132:135], v[116:119], v[64:67], v[132:135]\n\t"
      "ds_read_b128 v[92:95], %[vXr] offset:47360\n\t"
      "s_waitcnt lgkmcnt(8)\n\t"
      "v_mfma_f32_16x16x32_bf16 v[148:151], v[116:119], v[68:71], v[148:151]\n\t"
      "v_mfma_f32_16x16x32_bf16 v[144:147], v[112:115], v[68:71], v[144:147]\n\t"
      "v_mfma_f32_16x16x32_bf16 v[152:155], v[120:123], v[68:71], v[152:155]\n\t"
      "s_waitcnt lgkmcnt(6)\n\t"
      "v_mfma_f32_16x16x32_bf16 v[156:159], v[124:127], v[68:71], v[156:159]\n\t"
      "v_mfma_f32_16x16x32_bf16 v[140:143], v[124:127], v[64:67], v[140:143]\n\t"
      "v_mfma_f32_16x16x32_bf16 v[136:139], v[120:123], v[64:67], v[136:139]\n\t"
      "s_waitcnt lgkmcnt(5)\n\t"
      "v_mfma_f32_16x16x32_bf16 v[168:171], v[120:123], v[72:75], v[168:171]\n\t"
      "v_mfma_f32_16x16x32_bf16 v[172:175], v[124:127], v[72:75], v[172:175]\n\t"
      "s_waitcnt lgkmcnt(4)\n\t"
      "v_mfma_f32_16x16x32_bf16 v[188:191], v[124:127], v[76:79], v[188:191]\n\t"
      "v_mfma_f32_16x16x32_bf16 v[184:187], v[120:123], v[76:79], v[184:187]\n\t"
      "v_mfma_f32_16x16x32_bf16 v[176:179], v[112:115], v[76:79], v[176:179]\n\t"
      "v_mfma_f32_16x16x32_bf16 v[180:183], v[116:119], v[76:79], v[180:183]\n\t"
      "v_mfma_f32_16x16x32_bf16 v[164:167], v[116:119], v[72:75], v[164:167]\n\t"
      "v_mfma_f32_16x16x32_bf16 v[160:163], v[112:115], v[72:75], v[160:163]\n\t"
      "s_waitcnt lgkmcnt(3)\n\t"
      "v_mfma_f32_16x16x32_bf16 v[192:195], v[112:115], v[80:83], v[192:195]\n\t"
      "v_mfma_f32_16x16x32_bf16 v[196:199], v[116:119], v[80:83], v[196:199]\n\t"
      "s_waitcnt lgkmcnt(2)\n\t"
      "v_mfma_f32_16x16x32_bf16 v[212:215], v[116:119], v[84:87], v[212:215]\n\t"
      "v_mfma_f32_16x16x32_bf16 v[208:211], v[112:115], v[84:87], v[208:211]\n\t"
      "v_mfma_f32_16x16x32_bf16 v[216:219], v[120:123], v[84:87], v[216:219]\n\t"
      "v_mfma_f32_16x16x32_bf16 v[220:223], v[124:127], v[84:87], v[220:223]\n\t"
      "v_mfma_f32_16x16x32_bf16 v[204:207], v[124:127], v[80:83], v[204:207]\n\t"
      "v_mfma_f32_16x16x32_bf16 v[200:203], v[120:123], v[80:83], v[200:203]\n\t"
      "s_waitcnt lgkmcnt(1)\n\t"
      "v_mfma_f32_16x16x32_bf16 v[232:235], v[120:123], v[88:91], v[232:235]\n\t"
      "v_mfma_f32_16x16x32_bf16 v[236:239], v[124:127], v[88:91], v[236:239]\n\t"
      "s_waitcnt lgkmcnt(0)\n\t"
      "v_mfma_f32_16x16x32_bf16 v[252:255], v[124:127], v[92:95], v[252:255]\n\t"
      "v_mfma_f32_16x16x32_bf16 v[248:251], v[120:123], v[92:95], v[248:251]\n\t"
      "v_mfma_f32_16x16x32_bf16 v[240:243], v[112:115], v[92:95], v[240:243]\n\t"
      "v_mfma_f32_16x16x32_bf16 v[244:247], v[116:119], v[92:95], v[244:247]\n\t"
      "v_mfma_f32_16x16x32_bf16 v[228:231], v[116:119], v[88:91], v[228:231]\n\t"
      "v_mfma_f32_16x16x32_bf16 v[224:227], v[112:115], v[88:91], v[224:227]\n\t"
      "s_waitcnt vmcnt(8) lgkmcnt(0)\n\t"
      "v_max_u32_dpp v51, v51, v51 quad_perm:[1,0,3,2] row_mask:0xf bank_mask:0xf\n\t"
      "s_nop 1\n\t"
      "v_max_u32_dpp v51, v51, v51 quad_perm:[2,3,0,1] row_mask:0xf bank_mask:0xf\n\t"
      "s_nop 1\n\t"
      "v_max_u32_dpp v51, v51, v51 row_half_mirror row_mask:0xf bank_mask:0xf\n\t"
      "s_nop 1\n\t"
      "v_max_u32_dpp v51, v51, v51 row_mirror row_mask:0xf bank_mask:0xf\n\t"
      "s_nop 1\n\t"
      "v_mov_b32 v52, v51\n\t"
      "s_nop 1\n\t"
      "v_permlane16_swap_b32 v51, v52\n\t"
      "s_nop 0\n\t"
      "v_max_u32 v51, v51, v52\n\t"
      "s_cmp_ge_u32 %[w], 2\n\t"
      "s_cbranch_scc1 P1_EPR_%=\n\t"
      "s_lshl_b32 s48, %[w], 13\n\t"
      "s_add_u32 s48, s48, 147456\n\t"
      "v_add_u32 v50, s48, %[vOut]\n\t"
      "s_mov_b32 s56, 0x3e8293ee\n\t"
      "v_mul_f32 v132, s56, v132\n\t"
      "v_mul_f32 v133, s56, v133\n\t"
      "v_mul_f32 v134, s56, v134\n\t"
      "v_mul_f32 v135, s56, v135\n\t"
      "v_mul_f32 v140, s56, v140\n\t"
      "v_mul_f32 v141, s56, v141\n\t"
      "v_mul_f32 v142, s56, v142\n\t"
      "v_mul_f32 v143, s56, v143\n\t"
      "v_cvt_pk_bf16_f32 v64, v132, v133\n\t"
      "v_cvt_pk_bf16_f32 v65, v134, v135\n\t"
      "v_cvt_pk_bf16_f32 v66, v140, v141\n\t"
      "v_cvt_pk_bf16_f32 v67, v142, v143\n\t"
      "ds_write_b128 v50, v[64:67] offset:0\n\t"
      "v_mul_f32 v148, s56, v148\n\t"
      "v_mul_f32 v149, s56, v149\n\t"
      "v_mul_f32 v150, s56, v150\n\t"
      "v_mul_f32 v151, s56, v151\n\t"
      "v_mul_f32 v156, s56, v156\n\t"
      "v_mul_f32 v157, s56, v157\n\t"
      "v_mul_f32 v158, s56, v158\n\t"
      "v_mul_f32 v159, s56, v159\n\t"
      "v_cvt_pk_bf16_f32 v68, v148, v149\n\t"
      "v_cvt_pk_bf16_f32 v69, v150, v151\n\t"
      "v_cvt_pk_bf16_f32 v70, v156, v157\n\t"
      "v_cvt_pk_bf16_f32 v71, v158, v159\n\t"
      "ds_write_b128 v50, v[68:71] offset:256\n\t"
      "v_mul_f32 v164, s56, v164\n\t"
      "v_mul_f32 v165, s56, v165\n\t"
      "v_mul_f32 v166, s56, v166\n\t"
      "v_mul_f32 v167, s56, v167\n\t"
      "v_mul_f32 v172, s56, v172\n\t"
      "v_mul_f32 v173, s56, v173\n\t"
      "v_mul_f32 v174, s56, v174\n\t"
      "v_mul_f32 v175, s56, v175\n\t"
      "v_cvt_pk_bf16_f32 v72, v164, v165\n\t"
      "v_cvt_pk_bf16_f32 v73, v166, v167\n\t"
      "v_cvt_pk_bf16_f32 v74, v172, v173\n\t"
      "v_cvt_pk_bf16_f32 v75, v174, v175\n\t"
      "ds_write_b128 v50, v[72:75] offset:2048\n\t"
      "v_mul_f32 v180, s56, v180\n\t"
      "v_mul_f32 v181, s56, v181\n\t"
      "v_mul_f32 v182, s56, v182\n\t"
      "v_mul_f32 v183, s56, v183\n\t"
      "v_mul_f32 v188, s56, v188\n\t"
      "v_mul_f32 v189, s56, v189\n\t"
      "v_mul_f32 v190, s56, v190\n\t"
      "v_mul_f32 v191, s56, v191\n\t"
      "v_cvt_pk_bf16_f32 v76, v180, v181\n\t"
      "v_cvt_pk_bf16_f32 v77, v182, v183\n\t"
      "v_cvt_pk_bf16_f32 v78, v188, v189\n\t"
      "v_cvt_pk_bf16_f32 v79, v190, v191\n\t"
      "ds_write_b128 v50, v[76:79] offset:2304\n\t"
      "v_mul_f32 v196, s56, v196\n\t"
      "v_mul_f32 v197, s56, v197\n\t"
      "v_mul_f32 v198, s56, v198\n\t"
      "v_mul_f32 v199, s56, v199\n\t"
      "v_mul_f32 v204, s56, v204\n\t"
      "v_mul_f32 v205, s56, v205\n\t"
      "v_mul_f32 v206, s56, v206\n\t"
      "v_mul_f32 v207, s56, v207\n\t"
      "v_cvt_pk_bf16_f32 v80, v196, v197\n\t"
      "v_cvt_pk_bf16_f32 v81, v198, v199\n\t"
      "v_cvt_pk_bf16_f32 v82, v204, v205\n\t"
      "v_cvt_pk_bf16_f32 v83, v206, v207\n\t"
      "ds_write_b128 v50, v[80:83] offset:4096\n\t"
      "v_mul_f32 v212, s56, v212\n\t"
      "v_mul_f32 v213, s56, v213\n\t"
      "v_mul_f32 v214, s56, v214\n\t"
      "v_mul_f32 v215, s56, v215\n\t"
      "v_mul_f32 v220, s56, v220\n\t"
      "v_mul_f32 v221, s56, v221\n\t"
      "v_mul_f32 v222, s56, v222\n\t"
      "v_mul_f32 v223, s56, v223\n\t"
      "v_cvt_pk_bf16_f32 v84, v212, v213\n\t"
      "v_cvt_pk_bf16_f32 v85, v214, v215\n\t"
      "v_cvt_pk_bf16_f32 v86, v220, v221\n\t"
      "v_cvt_pk_bf16_f32 v87, v222, v223\n\t"
      "ds_write_b128 v50, v[84:87] offset:4352\n\t"
      "v_mul_f32 v228, s56, v228\n\t"
      "v_mul_f32 v229, s56, v229\n\t"
      "v_mul_f32 v230, s56, v230\n\t"
      "v_mul_f32 v231, s56, v231\n\t"
      "v_mul_f32 v236, s56, v236\n\t"
      "v_mul_f32 v237, s56, v237\n\t"
      "v_mul_f32 v238, s56, v238\n\t"
      "v_mul_f32 v239, s56, v239\n\t"
      "v_cvt_pk_bf16_f32 v88, v228, v229\n\t"
      "v_cvt_pk_bf16_f32 v89, v230, v231\n\t"
      "v_cvt_pk_bf16_f32 v90, v236, v237\n\t"
      "v_cvt_pk_bf16_f32 v91, v238, v239\n\t"
      "ds_write_b128 v50, v[88:91] offset:6144\n\t"
      "v_mul_f32 v244, s56, v244\n\t"
      "v_mul_f32 v245, s56, v245\n\t"
      "v_mul_f32 v246, s56, v246\n\t"
      "v_mul_f32 v247, s56, v247\n\t"
      "v_mul_f32 v252, s56, v252\n\t"
      "v_mul_f32 v253, s56, v253\n\t"
      "v_mul_f32 v254, s56, v254\n\t"
      "v_mul_f32 v255, s56, v255\n\t"
      "v_cvt_pk_bf16_f32 v92, v244, v245\n\t"
      "v_cvt_pk_bf16_f32 v93, v246, v247\n\t"
      "v_cvt_pk_bf16_f32 v94, v252, v253\n\t"
      "v_cvt_pk_bf16_f32 v95, v254, v255\n\t"
      "ds_write_b128 v50, v[92:95] offset:6400\n\t"
      "ds_write_b32 v56, v51\n\t"
      "v_max_f32 v128, 0, v128\n\t"
      "v_max_f32 v129, 0, v129\n\t"
      "v_max_f32 v130, 0, v130\n\t"
      "v_max_f32 v131, 0, v131\n\t"
      "v_max_f32 v144, 0, v144\n\t"
      "v_max_f32 v145, 0, v145\n\t"
      "v_max_f32 v146, 0, v146\n\t"
      "v_max_f32 v147, 0, v147\n\t"
      "v_mul_f32 v128, v128, v136\n\t"
      "v_mul_f32 v129, v129, v137\n\t"
      "v_mul_f32 v130, v130, v138\n\t"
      "v_mul_f32 v131, v131, v139\n\t"
      "v_mul_f32 v144, v144, v152\n\t"
      "v_mul_f32 v145, v145, v153\n\t"
      "v_mul_f32 v146, v146, v154\n\t"
      "v_mul_f32 v147, v147, v155\n\t"
      "v_cvt_pk_bf16_f32 v96, v128, v129\n\t"
      "v_cvt_pk_bf16_f32 v97, v130, v131\n\t"
      "v_cvt_pk_bf16_f32 v98, v144, v145\n\t"
      "v_cvt_pk_bf16_f32 v99, v146, v147\n\t"
      "s_nop 1\n\t"
      "v_permlane16_swap_b32 v96, v98\n\t"
      "v_permlane16_swap_b32 v97, v99\n\t"
      "s_nop 1\n\t"
      "buffer_store_dwordx4 v[96:99], %[vL16], %[srdWS], s44 offen sc1\n\t"
      "s_add_u32 s44, s44, 294912\n\t"
      "v_max_f32 v160, 0, v160\n\t"
      "v_max_f32 v161, 0, v161\n\t"
      "v_max_f32 v162, 0, v162\n\t"
      "v_max_f32 v163, 0, v163\n\t"
      "v_max_f32 v176, 0, v176\n\t"
      "v_max_f32 v177, 0, v177\n\t"
      "v_max_f32 v178, 0, v178\n\t"
      "v_max_f32 v179, 0, v179\n\t"
      "v_mul_f32 v160, v160, v168\n\t"
      "v_mul_f32 v161, v161, v169\n\t"
      "v_mul_f32 v162, v162, v170\n\t"
      "v_mul_f32 v163, v163, v171\n\t"
      "v_mul_f32 v176, v176, v184\n\t"
      "v_mul_f32 v177, v177, v185\n\t"
      "v_mul_f32 v178, v178, v186\n\t"
      "v_mul_f32 v179, v179, v187\n\t"
      "v_cvt_pk_bf16_f32 v100, v160, v161\n\t"
      "v_cvt_pk_bf16_f32 v101, v162, v163\n\t"
      "v_cvt_pk_bf16_f32 v102, v176, v177\n\t"
      "v_cvt_pk_bf16_f32 v103, v178, v179\n\t"
      "s_nop 1\n\t"
      "v_permlane16_swap_b32 v100, v102\n\t"
      "v_permlane16_swap_b32 v101, v103\n\t"
      "s_nop 1\n\t"
      "buffer_store_dwordx4 v[100:103], %[vL16], %[srdWS], s44 offen sc1\n\t"
      "s_add_u32 s44, s44, 294912\n\t"
      "v_max_f32 v192, 0, v192\n\t"
      "v_max_f32 v193, 0, v193\n\t"
      "v_max_f32 v194, 0, v194\n\t"
      "v_max_f32 v195, 0, v195\n\t"
      "v_max_f32 v208, 0, v208\n\t"
      "v_max_f32 v209, 0, v209\n\t"
      "v_max_f32 v210, 0, v210\n\t"
      "v_max_f32 v211, 0, v211\n\t"
      "v_mul_f32 v192, v192, v200\n\t"
      "v_mul_f32 v193, v193, v201\n\t"
      "v_mul_f32 v194, v194, v202\n\t"
      "v_mul_f32 v195, v195, v203\n\t"
      "v_mul_f32 v208, v208, v216\n\t"
      "v_mul_f32 v209, v209, v217\n\t"
      "v_mul_f32 v210, v210, v218\n\t"
      "v_mul_f32 v211, v211, v219\n\t"
      "v_cvt_pk_bf16_f32 v104, v192, v193\n\t"
      "v_cvt_pk_bf16_f32 v105, v194, v195\n\t"
      "v_cvt_pk_bf16_f32 v106, v208, v209\n\t"
      "v_cvt_pk_bf16_f32 v107, v210, v211\n\t"
      "s_nop 1\n\t"
      "v_permlane16_swap_b32 v104, v106\n\t"
      "v_permlane16_swap_b32 v105, v107\n\t"
      "s_nop 1\n\t"
      "buffer_store_dwordx4 v[104:107], %[vL16], %[srdWS], s44 offen sc1\n\t"
      "s_add_u32 s44, s44, 294912\n\t"
      "v_max_f32 v224, 0, v224\n\t"
      "v_max_f32 v225, 0, v225\n\t"
      "v_max_f32 v226, 0, v226\n\t"
      "v_max_f32 v227, 0, v227\n\t"
      "v_max_f32 v240, 0, v240\n\t"
      "v_max_f32 v241, 0, v241\n\t"
      "v_max_f32 v242, 0, v242\n\t"
      "v_max_f32 v243, 0, v243\n\t"
      "v_mul_f32 v224, v224, v232\n\t"
      "v_mul_f32 v225, v225, v233\n\t"
      "v_mul_f32 v226, v226, v234\n\t"
      "v_mul_f32 v227, v227, v235\n\t"
      "v_mul_f32 v240, v240, v248\n\t"
      "v_mul_f32 v241, v241, v249\n\t"
      "v_mul_f32 v242, v242, v250\n\t"
      "v_mul_f32 v243, v243, v251\n\t"
      "v_cvt_pk_bf16_f32 v108, v224, v225\n\t"
      "v_cvt_pk_bf16_f32 v109, v226, v227\n\t"
      "v_cvt_pk_bf16_f32 v110, v240, v241\n\t"
      "v_cvt_pk_bf16_f32 v111, v242, v243\n\t"
      "s_nop 1\n\t"
      "v_permlane16_swap_b32 v108, v110\n\t"
      "v_permlane16_swap_b32 v109, v111\n\t"
      "s_nop 1\n\t"
      "buffer_store_dwordx4 v[108:111], %[vL16], %[srdWS], s44 offen sc1\n\t"
      "s_branch P1_EPE_%=\n\t"
      "P1_EPR_%=:\n\t"
      "ds_write_b32 v56, v51\n\t"
      "v_max_f32 v128, 0, v128\n\t"
      "v_max_f32 v129, 0, v129\n\t"
      "v_max_f32 v130, 0, v130\n\t"
      "v_max_f32 v131, 0, v131\n\t"
      "v_max_f32 v132, 0, v132\n\t"
      "v_max_f32 v133, 0, v133\n\t"
      "v_max_f32 v134, 0, v134\n\t"
      "v_max_f32 v135, 0, v135\n\t"
      "v_mul_f32 v128, v128, v136\n\t"
      "v_mul_f32 v129, v129, v137\n\t"
      "v_mul_f32 v130, v130, v138\n\t"
      "v_mul_f32 v131, v131, v139\n\t"
      "v_mul_f32 v132, v132, v140\n\t"
      "v_mul_f32 v133, v133, v141\n\t"
      "v_mul_f32 v134, v134, v142\n\t"
      "v_mul_f32 v135, v135, v143\n\t"
      "v_cvt_pk_bf16_f32 v64, v128, v129\n\t"
      "v_cvt_pk_bf16_f32 v65, v130, v131\n\t"
      "v_cvt_pk_bf16_f32 v66, v132, v133\n\t"
      "v_cvt_pk_bf16_f32 v67, v134, v135\n\t"
      "buffer_store_dwordx4 v[64:67], %[vOut], %[srdWS], s44 offen offset:0 sc1\n\t"
      "v_max_f32 v144, 0, v144\n\t"
      "v_max_f32 v145, 0, v145\n\t"
      "v_max_f32 v146, 0, v146\n\t"
      "v_max_f32 v147, 0, v147\n\t"
      "v_max_f32 v148, 0, v148\n\t"
      "v_max_f32 v149, 0, v149\n\t"
      "v_max_f32 v150, 0, v150\n\t"
      "v_max_f32 v151, 0, v151\n\t"
      "v_mul_f32 v144, v144, v152\n\t"
      "v_mul_f32 v145, v145, v153\n\t"
      "v_mul_f32 v146, v146, v154\n\t"
      "v_mul_f32 v147, v147, v155\n\t"
      "v_mul_f32 v148, v148, v156\n\t"
      "v_mul_f32 v149, v149, v157\n\t"
      "v_mul_f32 v150, v150, v158\n\t"
      "v_mul_f32 v151, v151, v159\n\t"
      "v_cvt_pk_bf16_f32 v68, v144, v145\n\t"
      "v_cvt_pk_bf16_f32 v69, v146, v147\n\t"
      "v_cvt_pk_bf16_f32 v70, v148, v149\n\t"
      "v_cvt_pk_bf16_f32 v71, v150, v151\n\t"
      "buffer_store_dwordx4 v[68:71], %[vOut], %[srdWS], s44 offen offset:256 sc1\n\t"
      "s_add_u32 s44, s44, 294912\n\t"
      "v_max_f32 v160, 0, v160\n\t"
      "v_max_f32 v161, 0, v161\n\t"
      "v_max_f32 v162, 0, v162\n\t"
      "v_max_f32 v163, 0, v163\n\t"
      "v_max_f32 v164, 0, v164\n\t"
      "v_max_f32 v165, 0, v165\n\t"
      "v_max_f32 v166, 0, v166\n\t"
      "v_max_f32 v167, 0, v167\n\t"
      "v_mul_f32 v160, v160, v168\n\t"
      "v_mul_f32 v161, v161, v169\n\t"
      "v_mul_f32 v162, v162, v170\n\t"
      "v_mul_f32 v163, v163, v171\n\t"
      "v_mul_f32 v164, v164, v172\n\t"
      "v_mul_f32 v165, v165, v173\n\t"
      "v_mul_f32 v166, v166, v174\n\t"
      "v_mul_f32 v167, v167, v175\n\t"
      "v_cvt_pk_bf16_f32 v72, v160, v161\n\t"
      "v_cvt_pk_bf16_f32 v73, v162, v163\n\t"
      "v_cvt_pk_bf16_f32 v74, v164, v165\n\t"
      "v_cvt_pk_bf16_f32 v75, v166, v167\n\t"
      "buffer_store_dwordx4 v[72:75], %[vOut], %[srdWS], s44 offen offset:0 sc1\n\t"
      "v_max_f32 v176, 0, v176\n\t"
      "v_max_f32 v177, 0, v177\n\t"
      "v_max_f32 v178, 0, v178\n\t"
      "v_max_f32 v179, 0, v179\n\t"
      "v_max_f32 v180, 0, v180\n\t"
      "v_max_f32 v181, 0, v181\n\t"
      "v_max_f32 v182, 0, v182\n\t"
      "v_max_f32 v183, 0, v183\n\t"
      "v_mul_f32 v176, v176, v184\n\t"
      "v_mul_f32 v177, v177, v185\n\t"
      "v_mul_f32 v178, v178, v186\n\t"
      "v_mul_f32 v179, v179, v187\n\t"
      "v_mul_f32 v180, v180, v188\n\t"
      "v_mul_f32 v181, v181, v189\n\t"
      "v_mul_f32 v182, v182, v190\n\t"
      "v_mul_f32 v183, v183, v191\n\t"
      "v_cvt_pk_bf16_f32 v76, v176, v177\n\t"
      "v_cvt_pk_bf16_f32 v77, v178, v179\n\t"
      "v_cvt_pk_bf16_f32 v78, v180, v181\n\t"
      "v_cvt_pk_bf16_f32 v79, v182, v183\n\t"
      "buffer_store_dwordx4 v[76:79], %[vOut], %[srdWS], s44 offen offset:256 sc1\n\t"
      "s_add_u32 s44, s44, 294912\n\t"
      "v_max_f32 v192, 0, v192\n\t"
      "v_max_f32 v193, 0, v193\n\t"
      "v_max_f32 v194, 0, v194\n\t"
      "v_max_f32 v195, 0, v195\n\t"
      "v_max_f32 v196, 0, v196\n\t"
      "v_max_f32 v197, 0, v197\n\t"
      "v_max_f32 v198, 0, v198\n\t"
      "v_max_f32 v199, 0, v199\n\t"
      "v_mul_f32 v192, v192, v200\n\t"
      "v_mul_f32 v193, v193, v201\n\t"
      "v_mul_f32 v194, v194, v202\n\t"
      "v_mul_f32 v195, v195, v203\n\t"
      "v_mul_f32 v196, v196, v204\n\t"
      "v_mul_f32 v197, v197, v205\n\t"
      "v_mul_f32 v198, v198, v206\n\t"
      "v_mul_f32 v199, v199, v207\n\t"
      "v_cvt_pk_bf16_f32 v80, v192, v193\n\t"
      "v_cvt_pk_bf16_f32 v81, v194, v195\n\t"
      "v_cvt_pk_bf16_f32 v82, v196, v197\n\t"
      "v_cvt_pk_bf16_f32 v83, v198, v199\n\t"
      "buffer_store_dwordx4 v[80:83], %[vOut], %[srdWS], s44 offen offset:0 sc1\n\t"
      "v_max_f32 v208, 0, v208\n\t"
      "v_max_f32 v209, 0, v209\n\t"
      "v_max_f32 v210, 0, v210\n\t"
      "v_max_f32 v211, 0, v211\n\t"
      "v_max_f32 v212, 0, v212\n\t"
      "v_max_f32 v213, 0, v213\n\t"
      "v_max_f32 v214, 0, v214\n\t"
      "v_max_f32 v215, 0, v215\n\t"
      "v_mul_f32 v208, v208, v216\n\t"
      "v_mul_f32 v209, v209, v217\n\t"
      "v_mul_f32 v210, v210, v218\n\t"
      "v_mul_f32 v211, v211, v219\n\t"
      "v_mul_f32 v212, v212, v220\n\t"
      "v_mul_f32 v213, v213, v221\n\t"
      "v_mul_f32 v214, v214, v222\n\t"
      "v_mul_f32 v215, v215, v223\n\t"
      "v_cvt_pk_bf16_f32 v84, v208, v209\n\t"
      "v_cvt_pk_bf16_f32 v85, v210, v211\n\t"
      "v_cvt_pk_bf16_f32 v86, v212, v213\n\t"
      "v_cvt_pk_bf16_f32 v87, v214, v215\n\t"
      "buffer_store_dwordx4 v[84:87], %[vOut], %[srdWS], s44 offen offset:256 sc1\n\t"
      "s_add_u32 s44, s44, 294912\n\t"
      "v_max_f32 v224, 0, v224\n\t"
      "v_max_f32 v225, 0, v225\n\t"
      "v_max_f32 v226, 0, v226\n\t"
      "v_max_f32 v227, 0, v227\n\t"
      "v_max_f32 v228, 0, v228\n\t"
      "v_max_f32 v229, 0, v229\n\t"
      "v_max_f32 v230, 0, v230\n\t"
      "v_max_f32 v231, 0, v231\n\t"
      "v_mul_f32 v224, v224, v232\n\t"
      "v_mul_f32 v225, v225, v233\n\t"
      "v_mul_f32 v226, v226, v234\n\t"
      "v_mul_f32 v227, v227, v235\n\t"
      "v_mul_f32 v228, v228, v236\n\t"
      "v_mul_f32 v229, v229, v237\n\t"
      "v_mul_f32 v230, v230, v238\n\t"
      "v_mul_f32 v231, v231, v239\n\t"
      "v_cvt_pk_bf16_f32 v88, v224, v225\n\t"
      "v_cvt_pk_bf16_f32 v89, v226, v227\n\t"
      "v_cvt_pk_bf16_f32 v90, v228, v229\n\t"
      "v_cvt_pk_bf16_f32 v91, v230, v231\n\t"
      "buffer_store_dwordx4 v[88:91], %[vOut], %[srdWS], s44 offen offset:0 sc1\n\t"
      "v_max_f32 v240, 0, v240\n\t"
      "v_max_f32 v241, 0, v241\n\t"
      "v_max_f32 v242, 0, v242\n\t"
      "v_max_f32 v243, 0, v243\n\t"
      "v_max_f32 v244, 0, v244\n\t"
      "v_max_f32 v245, 0, v245\n\t"
      "v_max_f32 v246, 0, v246\n\t"
      "v_max_f32 v247, 0, v247\n\t"
      "v_mul_f32 v240, v240, v248\n\t"
      "v_mul_f32 v241, v241, v249\n\t"
      "v_mul_f32 v242, v242, v250\n\t"
      "v_mul_f32 v243, v243, v251\n\t"
      "v_mul_f32 v244, v244, v252\n\t"
      "v_mul_f32 v245, v245, v253\n\t"
      "v_mul_f32 v246, v246, v254\n\t"
      "v_mul_f32 v247, v247, v255\n\t"
      "v_cvt_pk_bf16_f32 v92, v240, v241\n\t"
      "v_cvt_pk_bf16_f32 v93, v242, v243\n\t"
      "v_cvt_pk_bf16_f32 v94, v244, v245\n\t"
      "v_cvt_pk_bf16_f32 v95, v246, v247\n\t"
      "buffer_store_dwordx4 v[92:95], %[vOut], %[srdWS], s44 offen offset:256 sc1\n\t"
      "P1_EPE_%=:\n\t"
      "v_add_u32 v236, 0x10000, %[vL16]\n\t"
      "s_lshl_b32 s48, %[w], 12\n\t"
      "s_add_u32 s48, s48, 147456\n\t"
      "v_mov_b32 v104, 0\n\t"
      "v_mov_b32 v105, 0\n\t"
      "v_mov_b32 v106, 0\n\t"
      "v_mov_b32 v107, 0\n\t"
      "v_mov_b32 v108, 0\n\t"
      "v_mov_b32 v109, 0\n\t"
      "v_mov_b32 v110, 0\n\t"
      "v_mov_b32 v111, 0\n\t"
      "v_add_u32 v253, s48, %[vL16]\n\t"
      "v_mfma_f32_32x32x16_bf16 v[128:143], v[104:107], v[104:107], 0\n\t"
      "v_mfma_f32_32x32x16_bf16 v[144:159], v[104:107], v[104:107], 0\n\t"
      "v_mfma_f32_32x32x16_bf16 v[200:215], v[104:107], v[104:107], 0\n\t"
      "v_mov_b32 v252, 131072\n\t"
      "s_mov_b32 s49, 0\n\t"
      "PA_SYNC_%=:\n\t"
      "ds_read_b128 v[240:243], v252\n\t"
      "ds_read_b128 v[244:247], v252 offset:16\n\t"
      "ds_read_b128 v[112:115], %[vL16] offset:0\n\t"
      "ds_read_b128 v[116:119], %[vL16] offset:1024\n\t"
      "ds_read_b128 v[220:223], v253 offset:0\n\t"
      "ds_read_b128 v[224:227], v253 offset:1024\n\t"
      "ds_read_b128 v[228:231], v253 offset:2048\n\t"
      "ds_read_b128 v[232:235], v253 offset:3072\n\t"
      "s_waitcnt lgkmcnt(6)\n\t"
      "v_max3_u32 v240, v240, v241, v242\n\t"
      "v_max3_u32 v244, v244, v245, v246\n\t"
      "v_max_u32 v240, v240, v243\n\t"
      "v_max_u32 v244, v244, v247\n\t"
      "v_cmp_ne_u32 vcc, -1, v240\n\t"
      "s_add_u32 s49, s49, 1\n\t"
      "s_waitcnt lgkmcnt(0)\n\t"
      "s_cbranch_vccnz PA_GO_%=\n\t"
      "s_cmp_lt_u32 s49, 0x400000\n\t"
      "s_cbranch_scc1 PA_SYNC_%=\n\t"
      "PA_GO_%=:\n\t"
      "v_mfma_f32_32x32x16_bf16 v[64:79], v[112:115], v[220:223], 0\n\t"
      "v_mfma_f32_32x32x16_bf16 v[80:95], v[112:115], v[228:231], 0\n\t"
      "v_mfma_f32_32x32x16_bf16 v[64:79], v[116:119], v[224:227], v[64:79]\n\t"
      "v_mfma_f32_32x32x16_bf16 v[80:95], v[116:119], v[232:235], v[80:95]\n\t"
      "v_dot2_f32_bf16 v248, v220, v220, 0\n\t"
      "v_dot2_f32_bf16 v249, v228, v228, 0\n\t"
      "v_dot2_f32_bf16 v248, v221, v221, v248\n\t"
      "v_dot2_f32_bf16 v249, v229, v229, v249\n\t"
      "v_dot2_f32_bf16 v248, v222, v222, v248\n\t"
      "v_dot2_f32_bf16 v249, v230, v230, v249\n\t"
      "v_dot2_f32_bf16 v248, v223, v223, v248\n\t"
      "v_dot2_f32_bf16 v249, v231, v231, v249\n\t"
      "v_dot2_f32_bf16 v248, v224, v224, v248\n\t"
      "v_dot2_f32_bf16 v249, v232, v232, v249\n\t"
      "v_dot2_f32_bf16 v248, v225, v225, v248\n\t"
      "v_dot2_f32_bf16 v249, v233, v233, v249\n\t"
      "v_dot2_f32_bf16 v248, v226, v226, v248\n\t"
      "v_dot2_f32_bf16 v249, v234, v234, v249\n\t"
      "v_dot2_f32_bf16 v248, v227, v227, v248\n\t"
      "v_dot2_f32_bf16 v249, v235, v235, v249\n\t"
      "s_cmp_lg_u32 %[sE], 0\n\t"
      "s_cbranch_scc1 PA_NOE_%=\n\t"
      "s_mov_b32 s48, 9441280\n\t"
      "v_mov_b32 v30, %[taglo]\n\t"
      "v_mov_b32 v31, %[taghi]\n\t"
      "s_mov_b64 exec, 1\n\t"
      "buffer_store_dwordx2 v[30:31], %[vL16], %[srdWS], s48 offen sc1\n\t"
      "s_mov_b64 exec, -1\n\t"
      "PA_NOE_%=:\n\t"
      "v_bfe_i32 v218, %[vL16], 6, 1\n\t"
      "v_bfe_u32 v237, %[vL16], 4, 1\n\t"
      "v_mov_b32 v250, v248\n\t"
      "v_mov_b32 v251, v249\n\t"
      "s_nop 0\n\t"
      "v_permlane32_swap_b32 v248, v250\n\t"
      "v_permlane32_swap_b32 v249, v251\n\t"
      "s_nop 0\n\t"
      "v_add_f32 v248, v248, v250\n\t"
      "v_add_f32 v249, v249, v251\n\t"
      "v_max_f32 v238, v64, v65\n\t"
      "v_max_f32 v239, v80, v81\n\t"
      "v_max3_f32 v238, v66, v67, v238\n\t"
      "v_max3_f32 v239, v82, v83, v239\n\t"
      "v_max3_f32 v238, v68, v69, v238\n\t"
      "v_max3_f32 v239, v84, v85, v239\n\t"
      "v_max3_f32 v238, v70, v71, v238\n\t"
      "v_max3_f32 v239, v86, v87, v239\n\t"
      "v_max3_f32 v238, v72, v73, v238\n\t"
      "v_max3_f32 v239, v88, v89, v239\n\t"
      "v_max3_f32 v238, v74, v75, v238\n\t"
      "v_max3_f32 v239, v90, v91, v239\n\t"
      "v_max3_f32 v238, v76, v77, v238\n\t"
      "v_max3_f32 v239, v92, v93, v239\n\t"
      "v_max3_f32 v238, v78, v79, v238\n\t"
      "v_max3_f32 v239, v94, v95, v239\n\t"
      "v_mov_b32 v250, v238\n\t"
      "v_mov_b32 v251, v239\n\t"
      "s_nop 0\n\t"
      "v_permlane32_swap_b32 v238, v250\n\t"
      "v_permlane32_swap_b32 v239, v251\n\t"
      "s_nop 0\n\t"
      "v_max_f32 v238, v238, v250\n\t"
      "v_max_f32 v239, v239, v251\n\t"
      "v_mul_f32 v248, v248, v240\n\t"
      "v_mul_f32 v249, v249, v240\n\t"
      "v_sqrt_f32 v248, v248\n\t"
      "v_sqrt_f32 v249, v249\n\t"
      "v_and_b32 v218, 0x3f803f80, v218\n\t"
      "s_mov_b32 s48, 0x3f8020c5\n\t"
      "v_fma_f32 v248, v248, s48, -v238\n\t"
      "v_fma_f32 v249, v249, s48, -v239\n\t"
      "v_cmp_ge_f32 vcc, 0x5d5e0b6b, v244\n\t"
      "s_mov_b64 s[56:57], vcc\n\t"
      "v_cmp_ge_f32 vcc, 0x42a00000, v248\n\t"
      "s_and_b64 s[56:57], s[56:57], vcc\n\t"
      "v_cmp_ge_f32 vcc, 0x42a00000, v249\n\t"
      "s_and_b64 s[56:57], s[56:57], vcc\n\t"
      "s_cmp_eq_u64 s[56:57], -1\n\t"
      "s_cbranch_scc1 PA_SAFE_%=\n\t"
      "s_mov_b32 s48, 0\n\t"
      "PA_FB_%=:\n\t"
      "buffer_load_dwordx4 v[120:123], %[vL16], s[52:55], s48 offen offset:0 sc1\n\t"
      "buffer_load_dwordx4 v[124:127], %[vL16], s[52:55], s48 offen offset:1024 sc1\n\t"
      "s_waitcnt vmcnt(0)\n\t"
      "v_mfma_f32_32x32x16_bf16 v[64:79], v[120:123], v[220:223], 0\n\t"
      "v_mfma_f32_32x32x16_bf16 v[80:95], v[120:123], v[228:231], 0\n\t"
      "v_mfma_f32_32x32x16_bf16 v[64:79], v[124:127], v[224:227], v[64:79]\n\t"
      "v_mfma_f32_32x32x16_bf16 v[80:95], v[124:127], v[232:235], v[80:95]\n\t"
      "s_nop 15\n\t"
      "v_max3_f32 v238, v64, v65, v238\n\t"
      "v_max3_f32 v239, v80, v81, v239\n\t"
      "v_max3_f32 v238, v66, v67, v238\n\t"
      "v_max3_f32 v239, v82, v83, v239\n\t"
      "v_max3_f32 v238, v68, v69, v238\n\t"
      "v_max3_f32 v239, v84, v85, v239\n\t"
      "v_max3_f32 v238, v70, v71, v238\n\t"
      "v_max3_f32 v239, v86, v87, v239\n\t"
      "v_max3_f32 v238, v72, v73, v238\n\t"
      "v_max3_f32 v239, v88, v89, v239\n\t"
      "v_max3_f32 v238, v74, v75, v238\n\t"
      "v_max3_f32 v239, v90, v91, v239\n\t"
      "v_max3_f32 v238, v76, v77, v238\n\t"
      "v_max3_f32 v239, v92, v93, v239\n\t"
      "v_max3_f32 v238, v78, v79, v238\n\t"
      "v_max3_f32 v239, v94, v95, v239\n\t"
      "s_add_u32 s48, s48, 4096\n\t"
      "s_cmp_lt_u32 s48, 262144\n\t"
      "s_cbranch_scc1 PA_FB_%=\n\t"
      "v_mov_b32 v250, v238\n\t"
      "v_mov_b32 v251, v239\n\t"
      "s_nop 0\n\t"
      "v_permlane32_swap_b32 v238, v250\n\t"
      "v_permlane32_swap_b32 v239, v251\n\t"
      "s_nop 0\n\t"
      "v_max_f32 v238, v238, v250\n\t"
      "v_max_f32 v239, v239, v251\n\t"
      "PA_SAFE_%=:\n\t"
      "v_sub_f32 v160, 0, v238\n\t"
      "v_sub_f32 v161, 0, v238\n\t"
      "v_sub_f32 v162, 0, v238\n\t"
      "v_sub_f32 v163, 0, v238\n\t"
      "v_sub_f32 v164, 0, v238\n\t"
      "v_sub_f32 v165, 0, v238\n\t"
      "v_sub_f32 v166, 0, v238\n\t"
      "v_sub_f32 v167, 0, v238\n\t"
      "v_sub_f32 v168, 0, v238\n\t"
      "v_sub_f32 v169, 0, v238\n\t"
      "v_sub_f32 v170, 0, v238\n\t"
      "v_sub_f32 v171, 0, v238\n\t"
      "v_sub_f32 v172, 0, v238\n\t"
      "v_sub_f32 v173, 0, v238\n\t"
      "v_sub_f32 v174, 0, v238\n\t"
      "v_sub_f32 v175, 0, v238\n\t"
      "v_mul_u32_u24 v237, 0xaaaa, v237\n\t"
      "v_xor_b32 v216, 0x3f803f80, v218\n\t"
      "v_mfma_f32_32x32x16_bf16 v[64:79], v[112:115], v[220:223], v[160:175]\n\t"
      "v_mfma_f32_32x32x16_bf16 v[64:79], v[116:119], v[224:227], v[64:79]\n\t"
      "v_sub_f32 v176, 0, v239\n\t"
      "v_sub_f32 v177, 0, v239\n\t"
      "v_sub_f32 v178, 0, v239\n\t"
      "v_sub_f32 v179, 0, v239\n\t"
      "v_sub_f32 v180, 0, v239\n\t"
      "v_sub_f32 v181, 0, v239\n\t"
      "v_sub_f32 v182, 0, v239\n\t"
      "v_sub_f32 v183, 0, v239\n\t"
      "v_sub_f32 v184, 0, v239\n\t"
      "v_sub_f32 v185, 0, v239\n\t"
      "v_sub_f32 v186, 0, v239\n\t"
      "v_sub_f32 v187, 0, v239\n\t"
      "v_sub_f32 v188, 0, v239\n\t"
      "v_sub_f32 v189, 0, v239\n\t"
      "v_sub_f32 v190, 0, v239\n\t"
      "v_sub_f32 v191, 0, v239\n\t"
      "v_and_b32 v217, 160, %[vL16]\n\t"
      "v_cmp_eq_u32 vcc, 0, v217\n\t"
      "s_nop 1\n\t"
      "v_cndmask_b32 v216, 0, v216, vcc\n\t"
      "v_cndmask_b32 v218, 0, v218, vcc\n\t"
      "v_add_u32 v237, 0x4444, v237\n\t"
      "v_mov_b32 v217, v216\n\t"
      "v_mov_b32 v219, v218\n\t"
      "s_mov_b32 s43, 4\n\t"
      "PA_LOOP_%=:\n\t"
      "v_mfma_f32_32x32x16_bf16 v[80:95], v[112:115], v[228:231], v[176:191]\n\t"
      "ds_read_b128 v[120:123], %[vL16] offset:4096\n\t"
      "v_exp_f32 v64, v64\n\t"
      "v_exp_f32 v65, v65\n\t"
      "v_exp_f32 v66, v66\n\t"
      "v_exp_f32 v67, v67\n\t"
      "v_mfma_f32_32x32x16_bf16 v[80:95], v[116:119], v[232:235], v[80:95]\n\t"
      "ds_read_b128 v[124:127], %[vL16] offset:5120\n\t"
      "v_exp_f32 v68, v68\n\t"
      "v_exp_f32 v69, v69\n\t"
      "v_exp_f32 v70, v70\n\t"
      "v_exp_f32 v71, v71\n\t"
      "v_mfma_f32_32x32x16_bf16 v[144:159], v[200:203], v[104:107], v[144:159]\n\t"
      "ds_read_b128 v[192:195], %[vL16] offset:2048\n\t"
      "ds_read_b128 v[196:199], %[vL16] offset:3072\n\t"
      "v_exp_f32 v72, v72\n\t"
      "v_exp_f32 v73, v73\n\t"
      "v_exp_f32 v74, v74\n\t"
      "v_exp_f32 v75, v75\n\t"
      "v_mfma_f32_32x32x16_bf16 v[144:159], v[204:207], v[108:111], v[144:159]\n\t"
      "v_exp_f32 v76, v76\n\t"
      "v_exp_f32 v77, v77\n\t"
      "v_exp_f32 v78, v78\n\t"
      "v_exp_f32 v79, v79\n\t"
      "v_smfmac_f32_16x16x64_bf16 v[212:215], v[216:219], v[104:111], v237\n\t"
      "s_waitcnt lgkmcnt(0)\n\t"
      "v_cvt_pk_bf16_f32 v96, v64, v65\n\t"
      "v_cvt_pk_bf16_f32 v97, v66, v67\n\t"
      "v_cvt_pk_bf16_f32 v98, v68, v69\n\t"
      "v_cvt_pk_bf16_f32 v99, v70, v71\n\t"
      "v_cvt_pk_bf16_f32 v100, v72, v73\n\t"
      "v_cvt_pk_bf16_f32 v101, v74, v75\n\t"
      "v_cvt_pk_bf16_f32 v102, v76, v77\n\t"
      "v_cvt_pk_bf16_f32 v103, v78, v79\n\t"
      "v_mfma_f32_32x32x16_bf16 v[64:79], v[120:123], v[220:223], v[160:175]\n\t"
      "v_exp_f32 v80, v80\n\t"
      "v_exp_f32 v81, v81\n\t"
      "v_exp_f32 v82, v82\n\t"
      "v_exp_f32 v83, v83\n\t"
      "v_mfma_f32_32x32x16_bf16 v[64:79], v[124:127], v[224:227], v[64:79]\n\t"
      "v_exp_f32 v84, v84\n\t"
      "v_exp_f32 v85, v85\n\t"
      "v_exp_f32 v86, v86\n\t"
      "v_exp_f32 v87, v87\n\t"
      "v_mfma_f32_32x32x16_bf16 v[128:143], v[192:195], v[96:99], v[128:143]\n\t"
      "v_exp_f32 v88, v88\n\t"
      "v_exp_f32 v89, v89\n\t"
      "v_exp_f32 v90, v90\n\t"
      "v_exp_f32 v91, v91\n\t"
      "v_mfma_f32_32x32x16_bf16 v[128:143], v[196:199], v[100:103], v[128:143]\n\t"
      "v_exp_f32 v92, v92\n\t"
      "v_exp_f32 v93, v93\n\t"
      "v_exp_f32 v94, v94\n\t"
      "v_exp_f32 v95, v95\n\t"
      "v_smfmac_f32_16x16x64_bf16 v[208:211], v[216:219], v[96:103], v237\n\t"
      "v_cvt_pk_bf16_f32 v104, v80, v81\n\t"
      "v_cvt_pk_bf16_f32 v105, v82, v83\n\t"
      "v_cvt_pk_bf16_f32 v106, v84, v85\n\t"
      "v_cvt_pk_bf16_f32 v107, v86, v87\n\t"
      "v_cvt_pk_bf16_f32 v108, v88, v89\n\t"
      "v_cvt_pk_bf16_f32 v109, v90, v91\n\t"
      "v_cvt_pk_bf16_f32 v110, v92, v93\n\t"
      "v_cvt_pk_bf16_f32 v111, v94, v95\n\t"
      "v_mfma_f32_32x32x16_bf16 v[80:95], v[120:123], v[228:231], v[176:191]\n\t"
      "ds_read_b128 v[112:115], %[vL16] offset:8192\n\t"
      "v_exp_f32 v64, v64\n\t"
      "v_exp_f32 v65, v65\n\t"
      "v_exp_f32 v66, v66\n\t"
      "v_exp_f32 v67, v67\n\t"
      "v_mfma_f32_32x32x16_bf16 v[80:95], v[124:127], v[232:235], v[80:95]\n\t"
      "ds_read_b128 v[116:119], %[vL16] offset:9216\n\t"
      "v_exp_f32 v68, v68\n\t"
      "v_exp_f32 v69, v69\n\t"
      "v_exp_f32 v70, v70\n\t"
      "v_exp_f32 v71, v71\n\t"
      "v_mfma_f32_32x32x16_bf16 v[144:159], v[192:195], v[104:107], v[144:159]\n\t"
      "ds_read_b128 v[200:203], %[vL16] offset:6144\n\t"
      "ds_read_b128 v[204:207], %[vL16] offset:7168\n\t"
      "v_exp_f32 v72, v72\n\t"
      "v_exp_f32 v73, v73\n\t"
      "v_exp_f32 v74, v74\n\t"
      "v_exp_f32 v75, v75\n\t"
      "v_mfma_f32_32x32x16_bf16 v[144:159], v[196:199], v[108:111], v[144:159]\n\t"
      "v_exp_f32 v76, v76\n\t"
      "v_exp_f32 v77, v77\n\t"
      "v_exp_f32 v78, v78\n\t"
      "v_exp_f32 v79, v79\n\t"
      "v_smfmac_f32_16x16x64_bf16 v[212:215], v[216:219], v[104:111], v237\n\t"
      "s_waitcnt lgkmcnt(0)\n\t"
      "v_cvt_pk_bf16_f32 v96, v64, v65\n\t"
      "v_cvt_pk_bf16_f32 v97, v66, v67\n\t"
      "v_cvt_pk_bf16_f32 v98, v68, v69\n\t"
      "v_cvt_pk_bf16_f32 v99, v70, v71\n\t"
      "v_cvt_pk_bf16_f32 v100, v72, v73\n\t"
      "v_cvt_pk_bf16_f32 v101, v74, v75\n\t"
      "v_cvt_pk_bf16_f32 v102, v76, v77\n\t"
      "v_cvt_pk_bf16_f32 v103, v78, v79\n\t"
      "v_mfma_f32_32x32x16_bf16 v[64:79], v[112:115], v[220:223], v[160:175]\n\t"
      "v_exp_f32 v80, v80\n\t"
      "v_exp_f32 v81, v81\n\t"
      "v_exp_f32 v82, v82\n\t"
      "v_exp_f32 v83, v83\n\t"
      "v_mfma_f32_32x32x16_bf16 v[64:79], v[116:119], v[224:227], v[64:79]\n\t"
      "v_exp_f32 v84, v84\n\t"
      "v_exp_f32 v85, v85\n\t"
      "v_exp_f32 v86, v86\n\t"
      "v_exp_f32 v87, v87\n\t"
      "v_mfma_f32_32x32x16_bf16 v[128:143], v[200:203], v[96:99], v[128:143]\n\t"
      "v_exp_f32 v88, v88\n\t"
      "v_exp_f32 v89, v89\n\t"
      "v_exp_f32 v90, v90\n\t"
      "v_exp_f32 v91, v91\n\t"
      "v_mfma_f32_32x32x16_bf16 v[128:143], v[204:207], v[100:103], v[128:143]\n\t"
      "v_exp_f32 v92, v92\n\t"
      "v_exp_f32 v93, v93\n\t"
      "v_exp_f32 v94, v94\n\t"
      "v_exp_f32 v95, v95\n\t"
      "v_smfmac_f32_16x16x64_bf16 v[208:211], v[216:219], v[96:103], v237\n\t"
      "v_cvt_pk_bf16_f32 v104, v80, v81\n\t"
      "v_cvt_pk_bf16_f32 v105, v82, v83\n\t"
      "v_cvt_pk_bf16_f32 v106, v84, v85\n\t"
      "v_cvt_pk_bf16_f32 v107, v86, v87\n\t"
      "v_cvt_pk_bf16_f32 v108, v88, v89\n\t"
      "v_cvt_pk_bf16_f32 v109, v90, v91\n\t"
      "v_cvt_pk_bf16_f32 v110, v92, v93\n\t"
      "v_cvt_pk_bf16_f32 v111, v94, v95\n\t"
      "v_mfma_f32_32x32x16_bf16 v[80:95], v[112:115], v[228:231], v[176:191]\n\t"
      "ds_read_b128 v[120:123], %[vL16] offset:12288\n\t"
      "v_exp_f32 v64, v64\n\t"
      "v_exp_f32 v65, v65\n\t"
      "v_exp_f32 v66, v66\n\t"
      "v_exp_f32 v67, v67\n\t"
      "v_mfma_f32_32x32x16_bf16 v[80:95], v[116:119], v[232:235], v[80:95]\n\t"
      "ds_read_b128 v[124:127], %[vL16] offset:13312\n\t"
      "v_exp_f32 v68, v68\n\t"
      "v_exp_f32 v69, v69\n\t"
      "v_exp_f32 v70, v70\n\t"
      "v_exp_f32 v71, v71\n\t"
      "v_mfma_f32_32x32x16_bf16 v[144:159], v[200:203], v[104:107], v[144:159]\n\t"
      "ds_read_b128 v[192:195], %[vL16] offset:10240\n\t"
      "ds_read_b128 v[196:199], %[vL16] offset:11264\n\t"
      "v_exp_f32 v72, v72\n\t"
      "v_exp_f32 v73, v73\n\t"
      "v_exp_f32 v74, v74\n\t"
      "v_exp_f32 v75, v75\n\t"
      "v_mfma_f32_32x32x16_bf16 v[144:159], v[204:207], v[108:111], v[144:159]\n\t"
      "v_exp_f32 v76, v76\n\t"
      "v_exp_f32 v77, v77\n\t"
      "v_exp_f32 v78, v78\n\t"
      "v_exp_f32 v79, v79\n\t"
      "v_smfmac_f32_16x16x64_bf16 v[212:215], v[216:219], v[104:111], v237\n\t"
      "s_waitcnt lgkmcnt(0)\n\t"
      "v_cvt_pk_bf16_f32 v96, v64, v65\n\t"
      "v_cvt_pk_bf16_f32 v97, v66, v67\n\t"
      "v_cvt_pk_bf16_f32 v98, v68, v69\n\t"
      "v_cvt_pk_bf16_f32 v99, v70, v71\n\t"
      "v_cvt_pk_bf16_f32 v100, v72, v73\n\t"
      "v_cvt_pk_bf16_f32 v101, v74, v75\n\t"
      "v_cvt_pk_bf16_f32 v102, v76, v77\n\t"
      "v_cvt_pk_bf16_f32 v103, v78, v79\n\t"
      "v_mfma_f32_32x32x16_bf16 v[64:79], v[120:123], v[220:223], v[160:175]\n\t"
      "v_exp_f32 v80, v80\n\t"
      "v_exp_f32 v81, v81\n\t"
      "v_exp_f32 v82, v82\n\t"
      "v_exp_f32 v83, v83\n\t"
      "v_mfma_f32_32x32x16_bf16 v[64:79], v[124:127], v[224:227], v[64:79]\n\t"
      "v_exp_f32 v84, v84\n\t"
      "v_exp_f32 v85, v85\n\t"
      "v_exp_f32 v86, v86\n\t"
      "v_exp_f32 v87, v87\n\t"
      "v_mfma_f32_32x32x16_bf16 v[128:143], v[192:195], v[96:99], v[128:143]\n\t"
      "v_exp_f32 v88, v88\n\t"
      "v_exp_f32 v89, v89\n\t"
      "v_exp_f32 v90, v90\n\t"
      "v_exp_f32 v91, v91\n\t"
      "v_mfma_f32_32x32x16_bf16 v[128:143], v[196:199], v[100:103], v[128:143]\n\t"
      "v_exp_f32 v92, v92\n\t"
      "v_exp_f32 v93, v93\n\t"
      "v_exp_f32 v94, v94\n\t"
      "v_exp_f32 v95, v95\n\t"
      "v_smfmac_f32_16x16x64_bf16 v[208:211], v[216:219], v[96:103], v237\n\t"
      "v_cvt_pk_bf16_f32 v104, v80, v81\n\t"
      "v_cvt_pk_bf16_f32 v105, v82, v83\n\t"
      "v_cvt_pk_bf16_f32 v106, v84, v85\n\t"
      "v_cvt_pk_bf16_f32 v107, v86, v87\n\t"
      "v_cvt_pk_bf16_f32 v108, v88, v89\n\t"
      "v_cvt_pk_bf16_f32 v109, v90, v91\n\t"
      "v_cvt_pk_bf16_f32 v110, v92, v93\n\t"
      "v_cvt_pk_bf16_f32 v111, v94, v95\n\t"
      "v_mfma_f32_32x32x16_bf16 v[80:95], v[120:123], v[228:231], v[176:191]\n\t"
      "s_waitcnt vmcnt(0)\n\t"
      "s_barrier\n\t"
      "ds_read_b128 v[112:115], %[vL16] offset:16384\n\t"
      "v_exp_f32 v64, v64\n\t"
      "v_exp_f32 v65, v65\n\t"
      "v_exp_f32 v66, v66\n\t"
      "v_exp_f32 v67, v67\n\t"
      "v_mfma_f32_32x32x16_bf16 v[80:95], v[124:127], v[232:235], v[80:95]\n\t"
      "ds_read_b128 v[116:119], %[vL16] offset:17408\n\t"
      "v_exp_f32 v68, v68\n\t"
      "v_exp_f32 v69, v69\n\t"
      "v_exp_f32 v70, v70\n\t"
      "v_exp_f32 v71, v71\n\t"
      "v_mfma_f32_32x32x16_bf16 v[144:159], v[192:195], v[104:107], v[144:159]\n\t"
      "ds_read_b128 v[200:203], %[vL16] offset:14336\n\t"
      "ds_read_b128 v[204:207], %[vL16] offset:15360\n\t"
      "v_exp_f32 v72, v72\n\t"
      "v_exp_f32 v73, v73\n\t"
      "v_exp_f32 v74, v74\n\t"
      "v_exp_f32 v75, v75\n\t"
      "v_mfma_f32_32x32x16_bf16 v[144:159], v[196:199], v[108:111], v[144:159]\n\t"
      "v_exp_f32 v76, v76\n\t"
      "v_exp_f32 v77, v77\n\t"
      "v_exp_f32 v78, v78\n\t"
      "v_exp_f32 v79, v79\n\t"
      "v_smfmac_f32_16x16x64_bf16 v[212:215], v[216:219], v[104:111], v237\n\t"
      "s_waitcnt lgkmcnt(0)\n\t"
      "v_cvt_pk_bf16_f32 v96, v64, v65\n\t"
      "v_cvt_pk_bf16_f32 v97, v66, v67\n\t"
      "v_cvt_pk_bf16_f32 v98, v68, v69\n\t"
      "v_cvt_pk_bf16_f32 v99, v70, v71\n\t"
      "v_cvt_pk_bf16_f32 v100, v72, v73\n\t"
      "v_cvt_pk_bf16_f32 v101, v74, v75\n\t"
      "v_cvt_pk_bf16_f32 v102, v76, v77\n\t"
      "v_cvt_pk_bf16_f32 v103, v78, v79\n\t"
      "v_mfma_f32_32x32x16_bf16 v[64:79], v[112:115], v[220:223], v[160:175]\n\t"
      "v_exp_f32 v80, v80\n\t"
      "v_exp_f32 v81, v81\n\t"
      "v_exp_f32 v82, v82\n\t"
      "v_exp_f32 v83, v83\n\t"
      "v_mfma_f32_32x32x16_bf16 v[64:79], v[116:119], v[224:227], v[64:79]\n\t"
      "v_exp_f32 v84, v84\n\t"
      "v_exp_f32 v85, v85\n\t"
      "v_exp_f32 v86, v86\n\t"
      "v_exp_f32 v87, v87\n\t"
      "v_mfma_f32_32x32x16_bf16 v[128:143], v[200:203], v[96:99], v[128:143]\n\t"
      "v_exp_f32 v88, v88\n\t"
      "v_exp_f32 v89, v89\n\t"
      "v_exp_f32 v90, v90\n\t"
      "v_exp_f32 v91, v91\n\t"
      "v_mfma_f32_32x32x16_bf16 v[128:143], v[204:207], v[100:103], v[128:143]\n\t"
      "v_exp_f32 v92, v92\n\t"
      "v_exp_f32 v93, v93\n\t"
      "v_exp_f32 v94, v94\n\t"
      "v_exp_f32 v95, v95\n\t"
      "v_smfmac_f32_16x16x64_bf16 v[208:211], v[216:219], v[96:103], v237\n\t"
      "v_cvt_pk_bf16_f32 v104, v80, v81\n\t"
      "v_cvt_pk_bf16_f32 v105, v82, v83\n\t"
      "v_cvt_pk_bf16_f32 v106, v84, v85\n\t"
      "v_cvt_pk_bf16_f32 v107, v86, v87\n\t"
      "v_cvt_pk_bf16_f32 v108, v88, v89\n\t"
      "v_cvt_pk_bf16_f32 v109, v90, v91\n\t"
      "v_cvt_pk_bf16_f32 v110, v92, v93\n\t"
      "v_cvt_pk_bf16_f32 v111, v94, v95\n\t"
      "v_mfma_f32_32x32x16_bf16 v[80:95], v[112:115], v[228:231], v[176:191]\n\t"
      "ds_read_b128 v[120:123], %[vL16] offset:20480\n\t"
      "v_exp_f32 v64, v64\n\t"
      "v_exp_f32 v65, v65\n\t"
      "v_exp_f32 v66, v66\n\t"
      "v_exp_f32 v67, v67\n\t"
      "v_mfma_f32_32x32x16_bf16 v[80:95], v[116:119], v[232:235], v[80:95]\n\t"
      "ds_read_b128 v[124:127], %[vL16] offset:21504\n\t"
      "v_exp_f32 v68, v68\n\t"
      "v_exp_f32 v69, v69\n\t"
      "v_exp_f32 v70, v70\n\t"
      "v_exp_f32 v71, v71\n\t"
      "v_mfma_f32_32x32x16_bf16 v[144:159], v[200:203], v[104:107], v[144:159]\n\t"
      "ds_read_b128 v[192:195], %[vL16] offset:18432\n\t"
      "ds_read_b128 v[196:199], %[vL16] offset:19456\n\t"
      "v_exp_f32 v72, v72\n\t"
      "v_exp_f32 v73, v73\n\t"
      "v_exp_f32 v74, v74\n\t"
      "v_exp_f32 v75, v75\n\t"
      "v_mfma_f32_32x32x16_bf16 v[144:159], v[204:207], v[108:111], v[144:159]\n\t"
      "v_exp_f32 v76, v76\n\t"
      "v_exp_f32 v77, v77\n\t"
      "v_exp_f32 v78, v78\n\t"
      "v_exp_f32 v79, v79\n\t"
      "v_smfmac_f32_16x16x64_bf16 v[212:215], v[216:219], v[104:111], v237\n\t"
      "s_waitcnt lgkmcnt(0)\n\t"
      "v_cvt_pk_bf16_f32 v96, v64, v65\n\t"
      "v_cvt_pk_bf16_f32 v97, v66, v67\n\t"
      "v_cvt_pk_bf16_f32 v98, v68, v69\n\t"
      "v_cvt_pk_bf16_f32 v99, v70, v71\n\t"
      "v_cvt_pk_bf16_f32 v100, v72, v73\n\t"
      "v_cvt_pk_bf16_f32 v101, v74, v75\n\t"
      "v_cvt_pk_bf16_f32 v102, v76, v77\n\t"
      "v_cvt_pk_bf16_f32 v103, v78, v79\n\t"
      "v_mfma_f32_32x32x16_bf16 v[64:79], v[120:123], v[220:223], v[160:175]\n\t"
      "v_exp_f32 v80, v80\n\t"
      "v_exp_f32 v81, v81\n\t"
      "v_exp_f32 v82, v82\n\t"
      "v_exp_f32 v83, v83\n\t"
      "v_mfma_f32_32x32x16_bf16 v[64:79], v[124:127], v[224:227], v[64:79]\n\t"
      "v_exp_f32 v84, v84\n\t"
      "v_exp_f32 v85, v85\n\t"
      "v_exp_f32 v86, v86\n\t"
      "v_exp_f32 v87, v87\n\t"
      "v_mfma_f32_32x32x16_bf16 v[128:143], v[192:195], v[96:99], v[128:143]\n\t"
      "v_exp_f32 v88, v88\n\t"
      "v_exp_f32 v89, v89\n\t"
      "v_exp_f32 v90, v90\n\t"
      "v_exp_f32 v91, v91\n\t"
      "v_mfma_f32_32x32x16_bf16 v[128:143], v[196:199], v[100:103], v[128:143]\n\t"
      "v_exp_f32 v92, v92\n\t"
      "v_exp_f32 v93, v93\n\t"
      "v_exp_f32 v94, v94\n\t"
      "v_exp_f32 v95, v95\n\t"
      "v_smfmac_f32_16x16x64_bf16 v[208:211], v[216:219], v[96:103], v237\n\t"
      "v_cvt_pk_bf16_f32 v104, v80, v81\n\t"
      "v_cvt_pk_bf16_f32 v105, v82, v83\n\t"
      "v_cvt_pk_bf16_f32 v106, v84, v85\n\t"
      "v_cvt_pk_bf16_f32 v107, v86, v87\n\t"
      "v_cvt_pk_bf16_f32 v108, v88, v89\n\t"
      "v_cvt_pk_bf16_f32 v109, v90, v91\n\t"
      "v_cvt_pk_bf16_f32 v110, v92, v93\n\t"
      "v_cvt_pk_bf16_f32 v111, v94, v95\n\t"
      "v_mfma_f32_32x32x16_bf16 v[80:95], v[120:123], v[228:231], v[176:191]\n\t"
      "ds_read_b128 v[112:115], %[vL16] offset:24576\n\t"
      "v_exp_f32 v64, v64\n\t"
      "v_exp_f32 v65, v65\n\t"
      "v_exp_f32 v66, v66\n\t"
      "v_exp_f32 v67, v67\n\t"
      "v_mfma_f32_32x32x16_bf16 v[80:95], v[124:127], v[232:235], v[80:95]\n\t"
      "ds_read_b128 v[116:119], %[vL16] offset:25600\n\t"
      "v_exp_f32 v68, v68\n\t"
      "v_exp_f32 v69, v69\n\t"
      "v_exp_f32 v70, v70\n\t"
      "v_exp_f32 v71, v71\n\t"
      "v_mfma_f32_32x32x16_bf16 v[144:159], v[192:195], v[104:107], v[144:159]\n\t"
      "ds_read_b128 v[200:203], %[vL16] offset:22528\n\t"
      "ds_read_b128 v[204:207], %[vL16] offset:23552\n\t"
      "v_exp_f32 v72, v72\n\t"
      "v_exp_f32 v73, v73\n\t"
      "v_exp_f32 v74, v74\n\t"
      "v_exp_f32 v75, v75\n\t"
      "v_mfma_f32_32x32x16_bf16 v[144:159], v[196:199], v[108:111], v[144:159]\n\t"
      "v_exp_f32 v76, v76\n\t"
      "v_exp_f32 v77, v77\n\t"
      "v_exp_f32 v78, v78\n\t"
      "v_exp_f32 v79, v79\n\t"
      "v_smfmac_f32_16x16x64_bf16 v[212:215], v[216:219], v[104:111], v237\n\t"
      "s_waitcnt lgkmcnt(0)\n\t"
      "v_cvt_pk_bf16_f32 v96, v64, v65\n\t"
      "v_cvt_pk_bf16_f32 v97, v66, v67\n\t"
      "v_cvt_pk_bf16_f32 v98, v68, v69\n\t"
      "v_cvt_pk_bf16_f32 v99, v70, v71\n\t"
      "v_cvt_pk_bf16_f32 v100, v72, v73\n\t"
      "v_cvt_pk_bf16_f32 v101, v74, v75\n\t"
      "v_cvt_pk_bf16_f32 v102, v76, v77\n\t"
      "v_cvt_pk_bf16_f32 v103, v78, v79\n\t"
      "v_mfma_f32_32x32x16_bf16 v[64:79], v[112:115], v[220:223], v[160:175]\n\t"
      "v_exp_f32 v80, v80\n\t"
      "v_exp_f32 v81, v81\n\t"
      "v_exp_f32 v82, v82\n\t"
      "v_exp_f32 v83, v83\n\t"
      "v_mfma_f32_32x32x16_bf16 v[64:79], v[116:119], v[224:227], v[64:79]\n\t"
      "v_exp_f32 v84, v84\n\t"
      "v_exp_f32 v85, v85\n\t"
      "v_exp_f32 v86, v86\n\t"
      "v_exp_f32 v87, v87\n\t"
      "v_mfma_f32_32x32x16_bf16 v[128:143], v[200:203], v[96:99], v[128:143]\n\t"
      "v_exp_f32 v88, v88\n\t"
      "v_exp_f32 v89, v89\n\t"
      "v_exp_f32 v90, v90\n\t"
      "v_exp_f32 v91, v91\n\t"
      "v_mfma_f32_32x32x16_bf16 v[128:143], v[204:207], v[100:103], v[128:143]\n\t"
      "v_exp_f32 v92, v92\n\t"
      "v_exp_f32 v93, v93\n\t"
      "v_exp_f32 v94, v94\n\t"
      "v_exp_f32 v95, v95\n\t"
      "v_smfmac_f32_16x16x64_bf16 v[208:211], v[216:219], v[96:103], v237\n\t"
      "v_cvt_pk_bf16_f32 v104, v80, v81\n\t"
      "v_cvt_pk_bf16_f32 v105, v82, v83\n\t"
      "v_cvt_pk_bf16_f32 v106, v84, v85\n\t"
      "v_cvt_pk_bf16_f32 v107, v86, v87\n\t"
      "v_cvt_pk_bf16_f32 v108, v88, v89\n\t"
      "v_cvt_pk_bf16_f32 v109, v90, v91\n\t"
      "v_cvt_pk_bf16_f32 v110, v92, v93\n\t"
      "v_cvt_pk_bf16_f32 v111, v94, v95\n\t"
      "v_mfma_f32_32x32x16_bf16 v[80:95], v[112:115], v[228:231], v[176:191]\n\t"
      "ds_read_b128 v[120:123], %[vL16] offset:28672\n\t"
      "v_exp_f32 v64, v64\n\t"
      "v_exp_f32 v65, v65\n\t"
      "v_exp_f32 v66, v66\n\t"
      "v_exp_f32 v67, v67\n\t"
      "v_mfma_f32_32x32x16_bf16 v[80:95], v[116:119], v[232:235], v[80:95]\n\t"
      "ds_read_b128 v[124:127], %[vL16] offset:29696\n\t"
      "v_exp_f32 v68, v68\n\t"
      "v_exp_f32 v69, v69\n\t"
      "v_exp_f32 v70, v70\n\t"
      "v_exp_f32 v71, v71\n\t"
      "v_mfma_f32_32x32x16_bf16 v[144:159], v[200:203], v[104:107], v[144:159]\n\t"
      "ds_read_b128 v[192:195], %[vL16] offset:26624\n\t"
      "ds_read_b128 v[196:199], %[vL16] offset:27648\n\t"
      "v_exp_f32 v72, v72\n\t"
      "v_exp_f32 v73, v73\n\t"
      "v_exp_f32 v74, v74\n\t"
      "v_exp_f32 v75, v75\n\t"
      "v_mfma_f32_32x32x16_bf16 v[144:159], v[204:207], v[108:111], v[144:159]\n\t"
      "v_exp_f32 v76, v76\n\t"
      "v_exp_f32 v77, v77\n\t"
      "v_exp_f32 v78, v78\n\t"
      "v_exp_f32 v79, v79\n\t"
      "v_smfmac_f32_16x16x64_bf16 v[212:215], v[216:219], v[104:111], v237\n\t"
      "s_waitcnt lgkmcnt(0)\n\t"
      "v_cvt_pk_bf16_f32 v96, v64, v65\n\t"
      "v_cvt_pk_bf16_f32 v97, v66, v67\n\t"
      "v_cvt_pk_bf16_f32 v98, v68, v69\n\t"
      "v_cvt_pk_bf16_f32 v99, v70, v71\n\t"
      "v_cvt_pk_bf16_f32 v100, v72, v73\n\t"
      "v_cvt_pk_bf16_f32 v101, v74, v75\n\t"
      "v_cvt_pk_bf16_f32 v102, v76, v77\n\t"
      "v_cvt_pk_bf16_f32 v103, v78, v79\n\t"
      "v_mfma_f32_32x32x16_bf16 v[64:79], v[120:123], v[220:223], v[160:175]\n\t"
      "v_exp_f32 v80, v80\n\t"
      "v_exp_f32 v81, v81\n\t"
      "v_exp_f32 v82, v82\n\t"
      "v_exp_f32 v83, v83\n\t"
      "v_mfma_f32_32x32x16_bf16 v[64:79], v[124:127], v[224:227], v[64:79]\n\t"
      "v_exp_f32 v84, v84\n\t"
      "v_exp_f32 v85, v85\n\t"
      "v_exp_f32 v86, v86\n\t"
      "v_exp_f32 v87, v87\n\t"
      "v_mfma_f32_32x32x16_bf16 v[128:143], v[192:195], v[96:99], v[128:143]\n\t"
      "v_exp_f32 v88, v88\n\t"
      "v_exp_f32 v89, v89\n\t"
      "v_exp_f32 v90, v90\n\t"
      "v_exp_f32 v91, v91\n\t"
      "v_mfma_f32_32x32x16_bf16 v[128:143], v[196:199], v[100:103], v[128:143]\n\t"
      "v_exp_f32 v92, v92\n\t"
      "v_exp_f32 v93, v93\n\t"
      "v_exp_f32 v94, v94\n\t"
      "v_exp_f32 v95, v95\n\t"
      "v_smfmac_f32_16x16x64_bf16 v[208:211], v[216:219], v[96:103], v237\n\t"
      "v_cvt_pk_bf16_f32 v104, v80, v81\n\t"
      "v_cvt_pk_bf16_f32 v105, v82, v83\n\t"
      "v_cvt_pk_bf16_f32 v106, v84, v85\n\t"
      "v_cvt_pk_bf16_f32 v107, v86, v87\n\t"
      "v_cvt_pk_bf16_f32 v108, v88, v89\n\t"
      "v_cvt_pk_bf16_f32 v109, v90, v91\n\t"
      "v_cvt_pk_bf16_f32 v110, v92, v93\n\t"
      "v_cvt_pk_bf16_f32 v111, v94, v95\n\t"
      "v_mfma_f32_32x32x16_bf16 v[80:95], v[120:123], v[228:231], v[176:191]\n\t"
      "s_barrier\n\t"
      "ds_read_b128 v[112:115], v236 offset:0\n\t"
      "v_exp_f32 v64, v64\n\t"
      "v_exp_f32 v65, v65\n\t"
      "v_exp_f32 v66, v66\n\t"
      "v_exp_f32 v67, v67\n\t"
      "v_mfma_f32_32x32x16_bf16 v[80:95], v[124:127], v[232:235], v[80:95]\n\t"
      "ds_read_b128 v[116:119], v236 offset:1024\n\t"
      "v_exp_f32 v68, v68\n\t"
      "v_exp_f32 v69, v69\n\t"
      "v_exp_f32 v70, v70\n\t"
      "v_exp_f32 v71, v71\n\t"
      "v_mfma_f32_32x32x16_bf16 v[144:159], v[192:195], v[104:107], v[144:159]\n\t"
      "ds_read_b128 v[200:203], %[vL16] offset:30720\n\t"
      "ds_read_b128 v[204:207], %[vL16] offset:31744\n\t"
      "v_exp_f32 v72, v72\n\t"
      "v_exp_f32 v73, v73\n\t"
      "v_exp_f32 v74, v74\n\t"
      "v_exp_f32 v75, v75\n\t"
      "v_mfma_f32_32x32x16_bf16 v[144:159], v[196:199], v[108:111], v[144:159]\n\t"
      "v_exp_f32 v76, v76\n\t"
      "v_exp_f32 v77, v77\n\t"
      "v_exp_f32 v78, v78\n\t"
      "v_exp_f32 v79, v79\n\t"
      "v_smfmac_f32_16x16x64_bf16 v[212:215], v[216:219], v[104:111], v237\n\t"
      "s_waitcnt lgkmcnt(0)\n\t"
      "v_cvt_pk_bf16_f32 v96, v64, v65\n\t"
      "v_cvt_pk_bf16_f32 v97, v66, v67\n\t"
      "v_cvt_pk_bf16_f32 v98, v68, v69\n\t"
      "v_cvt_pk_bf16_f32 v99, v70, v71\n\t"
      "v_cvt_pk_bf16_f32 v100, v72, v73\n\t"
      "v_cvt_pk_bf16_f32 v101, v74, v75\n\t"
      "v_cvt_pk_bf16_f32 v102, v76, v77\n\t"
      "v_cvt_pk_bf16_f32 v103, v78, v79\n\t"
      "v_mfma_f32_32x32x16_bf16 v[64:79], v[112:115], v[220:223], v[160:175]\n\t"
      "v_exp_f32 v80, v80\n\t"
      "v_exp_f32 v81, v81\n\t"
      "v_exp_f32 v82, v82\n\t"
      "v_exp_f32 v83, v83\n\t"
      "v_mfma_f32_32x32x16_bf16 v[64:79], v[116:119], v[224:227], v[64:79]\n\t"
      "v_exp_f32 v84, v84\n\t"
      "v_exp_f32 v85, v85\n\t"
      "v_exp_f32 v86, v86\n\t"
      "v_exp_f32 v87, v87\n\t"
      "v_mfma_f32_32x32x16_bf16 v[128:143], v[200:203], v[96:99], v[128:143]\n\t"
      "v_exp_f32 v88, v88\n\t"
      "v_exp_f32 v89, v89\n\t"
      "v_exp_f32 v90, v90\n\t"
      "v_exp_f32 v91, v91\n\t"
      "v_mfma_f32_32x32x16_bf16 v[128:143], v[204:207], v[100:103], v[128:143]\n\t"
      "v_exp_f32 v92, v92\n\t"
      "v_exp_f32 v93, v93\n\t"
      "v_exp_f32 v94, v94\n\t"
      "v_exp_f32 v95, v95\n\t"
      "v_smfmac_f32_16x16x64_bf16 v[208:211], v[216:219], v[96:103], v237\n\t"
      "v_cvt_pk_bf16_f32 v104, v80, v81\n\t"
      "v_cvt_pk_bf16_f32 v105, v82, v83\n\t"
      "v_cvt_pk_bf16_f32 v106, v84, v85\n\t"
      "v_cvt_pk_bf16_f32 v107, v86, v87\n\t"
      "v_cvt_pk_bf16_f32 v108, v88, v89\n\t"
      "v_cvt_pk_bf16_f32 v109, v90, v91\n\t"
      "v_cvt_pk_bf16_f32 v110, v92, v93\n\t"
      "v_cvt_pk_bf16_f32 v111, v94, v95\n\t"
      "v_mfma_f32_32x32x16_bf16 v[80:95], v[112:115], v[228:231], v[176:191]\n\t"
      "ds_read_b128 v[120:123], v236 offset:4096\n\t"
      "v_exp_f32 v64, v64\n\t"
      "v_exp_f32 v65, v65\n\t"
      "v_exp_f32 v66, v66\n\t"
      "v_exp_f32 v67, v67\n\t"
      "v_mfma_f32_32x32x16_bf16 v[80:95], v[116:119], v[232:235], v[80:95]\n\t"
      "ds_read_b128 v[124:127], v236 offset:5120\n\t"
      "v_exp_f32 v68, v68\n\t"
      "v_exp_f32 v69, v69\n\t"
      "v_exp_f32 v70, v70\n\t"
      "v_exp_f32 v71, v71\n\t"
      "v_mfma_f32_32x32x16_bf16 v[144:159], v[200:203], v[104:107], v[144:159]\n\t"
      "ds_read_b128 v[192:195], v236 offset:2048\n\t"
      "ds_read_b128 v[196:199], v236 offset:3072\n\t"
      "v_exp_f32 v72, v72\n\t"
      "v_exp_f32 v73, v73\n\t"
      "v_exp_f32 v74, v74\n\t"
      "v_exp_f32 v75, v75\n\t"
      "v_mfma_f32_32x32x16_bf16 v[144:159], v[204:207], v[108:111], v[144:159]\n\t"
      "v_exp_f32 v76, v76\n\t"
      "v_exp_f32 v77, v77\n\t"
      "v_exp_f32 v78, v78\n\t"
      "v_exp_f32 v79, v79\n\t"
      "v_smfmac_f32_16x16x64_bf16 v[212:215], v[216:219], v[104:111], v237\n\t"
      "s_waitcnt lgkmcnt(0)\n\t"
      "v_cvt_pk_bf16_f32 v96, v64, v65\n\t"
      "v_cvt_pk_bf16_f32 v97, v66, v67\n\t"
      "v_cvt_pk_bf16_f32 v98, v68, v69\n\t"
      "v_cvt_pk_bf16_f32 v99, v70, v71\n\t"
      "v_cvt_pk_bf16_f32 v100, v72, v73\n\t"
      "v_cvt_pk_bf16_f32 v101, v74, v75\n\t"
      "v_cvt_pk_bf16_f32 v102, v76, v77\n\t"
      "v_cvt_pk_bf16_f32 v103, v78, v79\n\t"
      "v_mfma_f32_32x32x16_bf16 v[64:79], v[120:123], v[220:223], v[160:175]\n\t"
      "v_exp_f32 v80, v80\n\t"
      "v_exp_f32 v81, v81\n\t"
      "v_exp_f32 v82, v82\n\t"
      "v_exp_f32 v83, v83\n\t"
      "v_mfma_f32_32x32x16_bf16 v[64:79], v[124:127], v[224:227], v[64:79]\n\t"
      "v_exp_f32 v84, v84\n\t"
      "v_exp_f32 v85, v85\n\t"
      "v_exp_f32 v86, v86\n\t"
      "v_exp_f32 v87, v87\n\t"
      "v_mfma_f32_32x32x16_bf16 v[128:143], v[192:195], v[96:99], v[128:143]\n\t"
      "v_exp_f32 v88, v88\n\t"
      "v_exp_f32 v89, v89\n\t"
      "v_exp_f32 v90, v90\n\t"
      "v_exp_f32 v91, v91\n\t"
      "v_mfma_f32_32x32x16_bf16 v[128:143], v[196:199], v[100:103], v[128:143]\n\t"
      "v_exp_f32 v92, v92\n\t"
      "v_exp_f32 v93, v93\n\t"
      "v_exp_f32 v94, v94\n\t"
      "v_exp_f32 v95, v95\n\t"
      "v_smfmac_f32_16x16x64_bf16 v[208:211], v[216:219], v[96:103], v237\n\t"
      "v_cvt_pk_bf16_f32 v104, v80, v81\n\t"
      "v_cvt_pk_bf16_f32 v105, v82, v83\n\t"
      "v_cvt_pk_bf16_f32 v106, v84, v85\n\t"
      "v_cvt_pk_bf16_f32 v107, v86, v87\n\t"
      "v_cvt_pk_bf16_f32 v108, v88, v89\n\t"
      "v_cvt_pk_bf16_f32 v109, v90, v91\n\t"
      "v_cvt_pk_bf16_f32 v110, v92, v93\n\t"
      "v_cvt_pk_bf16_f32 v111, v94, v95\n\t"
      "v_mfma_f32_32x32x16_bf16 v[80:95], v[120:123], v[228:231], v[176:191]\n\t"
      "ds_read_b128 v[112:115], v236 offset:8192\n\t"
      "v_exp_f32 v64, v64\n\t"
      "v_exp_f32 v65, v65\n\t"
      "v_exp_f32 v66, v66\n\t"
      "v_exp_f32 v67, v67\n\t"
      "v_mfma_f32_32x32x16_bf16 v[80:95], v[124:127], v[232:235], v[80:95]\n\t"
      "ds_read_b128 v[116:119], v236 offset:9216\n\t"
      "v_exp_f32 v68, v68\n\t"
      "v_exp_f32 v69, v69\n\t"
      "v_exp_f32 v70, v70\n\t"
      "v_exp_f32 v71, v71\n\t"
      "v_mfma_f32_32x32x16_bf16 v[144:159], v[192:195], v[104:107], v[144:159]\n\t"
      "ds_read_b128 v[200:203], v236 offset:6144\n\t"
      "ds_read_b128 v[204:207], v236 offset:7168\n\t"
      "v_exp_f32 v72, v72\n\t"
      "v_exp_f32 v73, v73\n\t"
      "v_exp_f32 v74, v74\n\t"
      "v_exp_f32 v75, v75\n\t"
      "v_mfma_f32_32x32x16_bf16 v[144:159], v[196:199], v[108:111], v[144:159]\n\t"
      "v_exp_f32 v76, v76\n\t"
      "v_exp_f32 v77, v77\n\t"
      "v_exp_f32 v78, v78\n\t"
      "v_exp_f32 v79, v79\n\t"
      "v_smfmac_f32_16x16x64_bf16 v[212:215], v[216:219], v[104:111], v237\n\t"
      "s_waitcnt lgkmcnt(0)\n\t"
      "v_cvt_pk_bf16_f32 v96, v64, v65\n\t"
      "v_cvt_pk_bf16_f32 v97, v66, v67\n\t"
      "v_cvt_pk_bf16_f32 v98, v68, v69\n\t"
      "v_cvt_pk_bf16_f32 v99, v70, v71\n\t"
      "v_cvt_pk_bf16_f32 v100, v72, v73\n\t"
      "v_cvt_pk_bf16_f32 v101, v74, v75\n\t"
      "v_cvt_pk_bf16_f32 v102, v76, v77\n\t"
      "v_cvt_pk_bf16_f32 v103, v78, v79\n\t"
      "v_mfma_f32_32x32x16_bf16 v[64:79], v[112:115], v[220:223], v[160:175]\n\t"
      "v_exp_f32 v80, v80\n\t"
      "v_exp_f32 v81, v81\n\t"
      "v_exp_f32 v82, v82\n\t"
      "v_exp_f32 v83, v83\n\t"
      "v_mfma_f32_32x32x16_bf16 v[64:79], v[116:119], v[224:227], v[64:79]\n\t"
      "v_exp_f32 v84, v84\n\t"
      "v_exp_f32 v85, v85\n\t"
      "v_exp_f32 v86, v86\n\t"
      "v_exp_f32 v87, v87\n\t"
      "v_mfma_f32_32x32x16_bf16 v[128:143], v[200:203], v[96:99], v[128:143]\n\t"
      "v_exp_f32 v88, v88\n\t"
      "v_exp_f32 v89, v89\n\t"
      "v_exp_f32 v90, v90\n\t"
      "v_exp_f32 v91, v91\n\t"
      "v_mfma_f32_32x32x16_bf16 v[128:143], v[204:207], v[100:103], v[128:143]\n\t"
      "v_exp_f32 v92, v92\n\t"
      "v_exp_f32 v93, v93\n\t"
      "v_exp_f32 v94, v94\n\t"
      "v_exp_f32 v95, v95\n\t"
      "v_smfmac_f32_16x16x64_bf16 v[208:211], v[216:219], v[96:103], v237\n\t"
      "v_cvt_pk_bf16_f32 v104, v80, v81\n\t"
      "v_cvt_pk_bf16_f32 v105, v82, v83\n\t"
      "v_cvt_pk_bf16_f32 v106, v84, v85\n\t"
      "v_cvt_pk_bf16_f32 v107, v86, v87\n\t"
      "v_cvt_pk_bf16_f32 v108, v88, v89\n\t"
      "v_cvt_pk_bf16_f32 v109, v90, v91\n\t"
      "v_cvt_pk_bf16_f32 v110, v92, v93\n\t"
      "v_cvt_pk_bf16_f32 v111, v94, v95\n\t"
      "v_mfma_f32_32x32x16_bf16 v[80:95], v[112:115], v[228:231], v[176:191]\n\t"
      "ds_read_b128 v[120:123], v236 offset:12288\n\t"
      "v_exp_f32 v64, v64\n\t"
      "v_exp_f32 v65, v65\n\t"
      "v_exp_f32 v66, v66\n\t"
      "v_exp_f32 v67, v67\n\t"
      "v_mfma_f32_32x32x16_bf16 v[80:95], v[116:119], v[232:235], v[80:95]\n\t"
      "ds_read_b128 v[124:127], v236 offset:13312\n\t"
      "v_exp_f32 v68, v68\n\t"
      "v_exp_f32 v69, v69\n\t"
      "v_exp_f32 v70, v70\n\t"
      "v_exp_f32 v71, v71\n\t"
      "v_mfma_f32_32x32x16_bf16 v[144:159], v[200:203], v[104:107], v[144:159]\n\t"
      "ds_read_b128 v[192:195], v236 offset:10240\n\t"
      "ds_read_b128 v[196:199], v236 offset:11264\n\t"
      "v_exp_f32 v72, v72\n\t"
      "v_exp_f32 v73, v73\n\t"
      "v_exp_f32 v74, v74\n\t"
      "v_exp_f32 v75, v75\n\t"
      "v_mfma_f32_32x32x16_bf16 v[144:159], v[204:207], v[108:111], v[144:159]\n\t"
      "v_exp_f32 v76, v76\n\t"
      "v_exp_f32 v77, v77\n\t"
      "v_exp_f32 v78, v78\n\t"
      "v_exp_f32 v79, v79\n\t"
      "v_smfmac_f32_16x16x64_bf16 v[212:215], v[216:219], v[104:111], v237\n\t"
      "s_waitcnt lgkmcnt(0)\n\t"
      "v_cvt_pk_bf16_f32 v96, v64, v65\n\t"
      "v_cvt_pk_bf16_f32 v97, v66, v67\n\t"
      "v_cvt_pk_bf16_f32 v98, v68, v69\n\t"
      "v_cvt_pk_bf16_f32 v99, v70, v71\n\t"
      "v_cvt_pk_bf16_f32 v100, v72, v73\n\t"
      "v_cvt_pk_bf16_f32 v101, v74, v75\n\t"
      "v_cvt_pk_bf16_f32 v102, v76, v77\n\t"
      "v_cvt_pk_bf16_f32 v103, v78, v79\n\t"
      "v_mfma_f32_32x32x16_bf16 v[64:79], v[120:123], v[220:223], v[160:175]\n\t"
      "v_exp_f32 v80, v80\n\t"
      "v_exp_f32 v81, v81\n\t"
      "v_exp_f32 v82, v82\n\t"
      "v_exp_f32 v83, v83\n\t"
      "v_mfma_f32_32x32x16_bf16 v[64:79], v[124:127], v[224:227], v[64:79]\n\t"
      "v_exp_f32 v84, v84\n\t"
      "v_exp_f32 v85, v85\n\t"
      "v_exp_f32 v86, v86\n\t"
      "v_exp_f32 v87, v87\n\t"
      "v_mfma_f32_32x32x16_bf16 v[128:143], v[192:195], v[96:99], v[128:143]\n\t"
      "v_exp_f32 v88, v88\n\t"
      "v_exp_f32 v89, v89\n\t"
      "v_exp_f32 v90, v90\n\t"
      "v_exp_f32 v91, v91\n\t"
      "v_mfma_f32_32x32x16_bf16 v[128:143], v[196:199], v[100:103], v[128:143]\n\t"
      "v_exp_f32 v92, v92\n\t"
      "v_exp_f32 v93, v93\n\t"
      "v_exp_f32 v94, v94\n\t"
      "v_exp_f32 v95, v95\n\t"
      "v_smfmac_f32_16x16x64_bf16 v[208:211], v[216:219], v[96:103], v237\n\t"
      "v_cvt_pk_bf16_f32 v104, v80, v81\n\t"
      "v_cvt_pk_bf16_f32 v105, v82, v83\n\t"
      "v_cvt_pk_bf16_f32 v106, v84, v85\n\t"
      "v_cvt_pk_bf16_f32 v107, v86, v87\n\t"
      "v_cvt_pk_bf16_f32 v108, v88, v89\n\t"
      "v_cvt_pk_bf16_f32 v109, v90, v91\n\t"
      "v_cvt_pk_bf16_f32 v110, v92, v93\n\t"
      "v_cvt_pk_bf16_f32 v111, v94, v95\n\t"
      "v_mfma_f32_32x32x16_bf16 v[80:95], v[120:123], v[228:231], v[176:191]\n\t"
      "s_barrier\n\t"
      "ds_read_b128 v[112:115], v236 offset:16384\n\t"
      "v_exp_f32 v64, v64\n\t"
      "v_exp_f32 v65, v65\n\t"
      "v_exp_f32 v66, v66\n\t"
      "v_exp_f32 v67, v67\n\t"
      "v_mfma_f32_32x32x16_bf16 v[80:95], v[124:127], v[232:235], v[80:95]\n\t"
      "ds_read_b128 v[116:119], v236 offset:17408\n\t"
      "v_exp_f32 v68, v68\n\t"
      "v_exp_f32 v69, v69\n\t"
      "v_exp_f32 v70, v70\n\t"
      "v_exp_f32 v71, v71\n\t"
      "v_mfma_f32_32x32x16_bf16 v[144:159], v[192:195], v[104:107], v[144:159]\n\t"
      "ds_read_b128 v[200:203], v236 offset:14336\n\t"
      "ds_read_b128 v[204:207], v236 offset:15360\n\t"
      "v_exp_f32 v72, v72\n\t"
      "v_exp_f32 v73, v73\n\t"
      "v_exp_f32 v74, v74\n\t"
      "v_exp_f32 v75, v75\n\t"
      "v_mfma_f32_32x32x16_bf16 v[144:159], v[196:199], v[108:111], v[144:159]\n\t"
      "v_exp_f32 v76, v76\n\t"
      "v_exp_f32 v77, v77\n\t"
      "v_exp_f32 v78, v78\n\t"
      "v_exp_f32 v79, v79\n\t"
      "v_smfmac_f32_16x16x64_bf16 v[212:215], v[216:219], v[104:111], v237\n\t"
      "s_waitcnt lgkmcnt(0)\n\t"
      "v_cvt_pk_bf16_f32 v96, v64, v65\n\t"
      "v_cvt_pk_bf16_f32 v97, v66, v67\n\t"
      "v_cvt_pk_bf16_f32 v98, v68, v69\n\t"
      "v_cvt_pk_bf16_f32 v99, v70, v71\n\t"
      "v_cvt_pk_bf16_f32 v100, v72, v73\n\t"
      "v_cvt_pk_bf16_f32 v101, v74, v75\n\t"
      "v_cvt_pk_bf16_f32 v102, v76, v77\n\t"
      "v_cvt_pk_bf16_f32 v103, v78, v79\n\t"
      "v_mfma_f32_32x32x16_bf16 v[64:79], v[112:115], v[220:223], v[160:175]\n\t"
      "v_exp_f32 v80, v80\n\t"
      "v_exp_f32 v81, v81\n\t"
      "v_exp_f32 v82, v82\n\t"
      "v_exp_f32 v83, v83\n\t"
      "v_mfma_f32_32x32x16_bf16 v[64:79], v[116:119], v[224:227], v[64:79]\n\t"
      "v_exp_f32 v84, v84\n\t"
      "v_exp_f32 v85, v85\n\t"
      "v_exp_f32 v86, v86\n\t"
      "v_exp_f32 v87, v87\n\t"
      "v_mfma_f32_32x32x16_bf16 v[128:143], v[200:203], v[96:99], v[128:143]\n\t"
      "v_exp_f32 v88, v88\n\t"
      "v_exp_f32 v89, v89\n\t"
      "v_exp_f32 v90, v90\n\t"
      "v_exp_f32 v91, v91\n\t"
      "v_mfma_f32_32x32x16_bf16 v[128:143], v[204:207], v[100:103], v[128:143]\n\t"
      "v_exp_f32 v92, v92\n\t"
      "v_exp_f32 v93, v93\n\t"
      "v_exp_f32 v94, v94\n\t"
      "v_exp_f32 v95, v95\n\t"
      "v_smfmac_f32_16x16x64_bf16 v[208:211], v[216:219], v[96:103], v237\n\t"
      "v_cvt_pk_bf16_f32 v104, v80, v81\n\t"
      "v_cvt_pk_bf16_f32 v105, v82, v83\n\t"
      "v_cvt_pk_bf16_f32 v106, v84, v85\n\t"
      "v_cvt_pk_bf16_f32 v107, v86, v87\n\t"
      "v_cvt_pk_bf16_f32 v108, v88, v89\n\t"
      "v_cvt_pk_bf16_f32 v109, v90, v91\n\t"
      "v_cvt_pk_bf16_f32 v110, v92, v93\n\t"
      "v_cvt_pk_bf16_f32 v111, v94, v95\n\t"
      "v_mfma_f32_32x32x16_bf16 v[80:95], v[112:115], v[228:231], v[176:191]\n\t"
      "ds_read_b128 v[120:123], v236 offset:20480\n\t"
      "v_exp_f32 v64, v64\n\t"
      "v_exp_f32 v65, v65\n\t"
      "v_exp_f32 v66, v66\n\t"
      "v_exp_f32 v67, v67\n\t"
      "v_mfma_f32_32x32x16_bf16 v[80:95], v[116:119], v[232:235], v[80:95]\n\t"
      "ds_read_b128 v[124:127], v236 offset:21504\n\t"
      "v_exp_f32 v68, v68\n\t"
      "v_exp_f32 v69, v69\n\t"
      "v_exp_f32 v70, v70\n\t"
      "v_exp_f32 v71, v71\n\t"
      "v_mfma_f32_32x32x16_bf16 v[144:159], v[200:203], v[104:107], v[144:159]\n\t"
      "ds_read_b128 v[192:195], v236 offset:18432\n\t"
      "ds_read_b128 v[196:199], v236 offset:19456\n\t"
      "v_exp_f32 v72, v72\n\t"
      "v_exp_f32 v73, v73\n\t"
      "v_exp_f32 v74, v74\n\t"
      "v_exp_f32 v75, v75\n\t"
      "v_mfma_f32_32x32x16_bf16 v[144:159], v[204:207], v[108:111], v[144:159]\n\t"
      "v_exp_f32 v76, v76\n\t"
      "v_exp_f32 v77, v77\n\t"
      "v_exp_f32 v78, v78\n\t"
      "v_exp_f32 v79, v79\n\t"
      "v_smfmac_f32_16x16x64_bf16 v[212:215], v[216:219], v[104:111], v237\n\t"
      "s_waitcnt lgkmcnt(0)\n\t"
      "v_cvt_pk_bf16_f32 v96, v64, v65\n\t"
      "v_cvt_pk_bf16_f32 v97, v66, v67\n\t"
      "v_cvt_pk_bf16_f32 v98, v68, v69\n\t"
      "v_cvt_pk_bf16_f32 v99, v70, v71\n\t"
      "v_cvt_pk_bf16_f32 v100, v72, v73\n\t"
      "v_cvt_pk_bf16_f32 v101, v74, v75\n\t"
      "v_cvt_pk_bf16_f32 v102, v76, v77\n\t"
      "v_cvt_pk_bf16_f32 v103, v78, v79\n\t"
      "v_mfma_f32_32x32x16_bf16 v[64:79], v[120:123], v[220:223], v[160:175]\n\t"
      "v_exp_f32 v80, v80\n\t"
      "v_exp_f32 v81, v81\n\t"
      "v_exp_f32 v82, v82\n\t"
      "v_exp_f32 v83, v83\n\t"
      "v_mfma_f32_32x32x16_bf16 v[64:79], v[124:127], v[224:227], v[64:79]\n\t"
      "v_exp_f32 v84, v84\n\t"
      "v_exp_f32 v85, v85\n\t"
      "v_exp_f32 v86, v86\n\t"
      "v_exp_f32 v87, v87\n\t"
      "v_mfma_f32_32x32x16_bf16 v[128:143], v[192:195], v[96:99], v[128:143]\n\t"
      "v_exp_f32 v88, v88\n\t"
      "v_exp_f32 v89, v89\n\t"
      "v_exp_f32 v90, v90\n\t"
      "v_exp_f32 v91, v91\n\t"
      "v_mfma_f32_32x32x16_bf16 v[128:143], v[196:199], v[100:103], v[128:143]\n\t"
      "v_exp_f32 v92, v92\n\t"
      "v_exp_f32 v93, v93\n\t"
      "v_exp_f32 v94, v94\n\t"
      "v_exp_f32 v95, v95\n\t"
      "v_smfmac_f32_16x16x64_bf16 v[208:211], v[216:219], v[96:103], v237\n\t"
      "v_cvt_pk_bf16_f32 v104, v80, v81\n\t"
      "v_cvt_pk_bf16_f32 v105, v82, v83\n\t"
      "v_cvt_pk_bf16_f32 v106, v84, v85\n\t"
      "v_cvt_pk_bf16_f32 v107, v86, v87\n\t"
      "v_cvt_pk_bf16_f32 v108, v88, v89\n\t"
      "v_cvt_pk_bf16_f32 v109, v90, v91\n\t"
      "v_cvt_pk_bf16_f32 v110, v92, v93\n\t"
      "v_cvt_pk_bf16_f32 v111, v94, v95\n\t"
      "v_mfma_f32_32x32x16_bf16 v[80:95], v[120:123], v[228:231], v[176:191]\n\t"
      "ds_read_b128 v[112:115], v236 offset:24576\n\t"
      "v_exp_f32 v64, v64\n\t"
      "v_exp_f32 v65, v65\n\t"
      "v_exp_f32 v66, v66\n\t"
      "v_exp_f32 v67, v67\n\t"
      "v_mfma_f32_32x32x16_bf16 v[80:95], v[124:127], v[232:235], v[80:95]\n\t"
      "ds_read_b128 v[116:119], v236 offset:25600\n\t"
      "v_exp_f32 v68, v68\n\t"
      "v_exp_f32 v69, v69\n\t"
      "v_exp_f32 v70, v70\n\t"
      "v_exp_f32 v71, v71\n\t"
      "v_mfma_f32_32x32x16_bf16 v[144:159], v[192:195], v[104:107], v[144:159]\n\t"
      "ds_read_b128 v[200:203], v236 offset:22528\n\t"
      "ds_read_b128 v[204:207], v236 offset:23552\n\t"
      "v_exp_f32 v72, v72\n\t"
      "v_exp_f32 v73, v73\n\t"
      "v_exp_f32 v74, v74\n\t"
      "v_exp_f32 v75, v75\n\t"
      "v_mfma_f32_32x32x16_bf16 v[144:159], v[196:199], v[108:111], v[144:159]\n\t"
      "v_exp_f32 v76, v76\n\t"
      "v_exp_f32 v77, v77\n\t"
      "v_exp_f32 v78, v78\n\t"
      "v_exp_f32 v79, v79\n\t"
      "v_smfmac_f32_16x16x64_bf16 v[212:215], v[216:219], v[104:111], v237\n\t"
      "s_waitcnt lgkmcnt(0)\n\t"
      "v_cvt_pk_bf16_f32 v96, v64, v65\n\t"
      "v_cvt_pk_bf16_f32 v97, v66, v67\n\t"
      "v_cvt_pk_bf16_f32 v98, v68, v69\n\t"
      "v_cvt_pk_bf16_f32 v99, v70, v71\n\t"
      "v_cvt_pk_bf16_f32 v100, v72, v73\n\t"
      "v_cvt_pk_bf16_f32 v101, v74, v75\n\t"
      "v_cvt_pk_bf16_f32 v102, v76, v77\n\t"
      "v_cvt_pk_bf16_f32 v103, v78, v79\n\t"
      "v_mfma_f32_32x32x16_bf16 v[64:79], v[112:115], v[220:223], v[160:175]\n\t"
      "v_exp_f32 v80, v80\n\t"
      "v_exp_f32 v81, v81\n\t"
      "v_exp_f32 v82, v82\n\t"
      "v_exp_f32 v83, v83\n\t"
      "v_mfma_f32_32x32x16_bf16 v[64:79], v[116:119], v[224:227], v[64:79]\n\t"
      "v_exp_f32 v84, v84\n\t"
      "v_exp_f32 v85, v85\n\t"
      "v_exp_f32 v86, v86\n\t"
      "v_exp_f32 v87, v87\n\t"
      "v_mfma_f32_32x32x16_bf16 v[128:143], v[200:203], v[96:99], v[128:143]\n\t"
      "v_exp_f32 v88, v88\n\t"
      "v_exp_f32 v89, v89\n\t"
      "v_exp_f32 v90, v90\n\t"
      "v_exp_f32 v91, v91\n\t"
      "v_mfma_f32_32x32x16_bf16 v[128:143], v[204:207], v[100:103], v[128:143]\n\t"
      "v_exp_f32 v92, v92\n\t"
      "v_exp_f32 v93, v93\n\t"
      "v_exp_f32 v94, v94\n\t"
      "v_exp_f32 v95, v95\n\t"
      "v_smfmac_f32_16x16x64_bf16 v[208:211], v[216:219], v[96:103], v237\n\t"
      "v_cvt_pk_bf16_f32 v104, v80, v81\n\t"
      "v_cvt_pk_bf16_f32 v105, v82, v83\n\t"
      "v_cvt_pk_bf16_f32 v106, v84, v85\n\t"
      "v_cvt_pk_bf16_f32 v107, v86, v87\n\t"
      "v_cvt_pk_bf16_f32 v108, v88, v89\n\t"
      "v_cvt_pk_bf16_f32 v109, v90, v91\n\t"
      "v_cvt_pk_bf16_f32 v110, v92, v93\n\t"
      "v_cvt_pk_bf16_f32 v111, v94, v95\n\t"
      "v_mfma_f32_32x32x16_bf16 v[80:95], v[112:115], v[228:231], v[176:191]\n\t"
      "ds_read_b128 v[120:123], v236 offset:28672\n\t"
      "v_exp_f32 v64, v64\n\t"
      "v_exp_f32 v65, v65\n\t"
      "v_exp_f32 v66, v66\n\t"
      "v_exp_f32 v67, v67\n\t"
      "v_mfma_f32_32x32x16_bf16 v[80:95], v[116:119], v[232:235], v[80:95]\n\t"
      "ds_read_b128 v[124:127], v236 offset:29696\n\t"
      "v_exp_f32 v68, v68\n\t"
      "v_exp_f32 v69, v69\n\t"
      "v_exp_f32 v70, v70\n\t"
      "v_exp_f32 v71, v71\n\t"
      "v_mfma_f32_32x32x16_bf16 v[144:159], v[200:203], v[104:107], v[144:159]\n\t"
      "ds_read_b128 v[192:195], v236 offset:26624\n\t"
      "ds_read_b128 v[196:199], v236 offset:27648\n\t"
      "v_exp_f32 v72, v72\n\t"
      "v_exp_f32 v73, v73\n\t"
      "v_exp_f32 v74, v74\n\t"
      "v_exp_f32 v75, v75\n\t"
      "v_mfma_f32_32x32x16_bf16 v[144:159], v[204:207], v[108:111], v[144:159]\n\t"
      "v_exp_f32 v76, v76\n\t"
      "v_exp_f32 v77, v77\n\t"
      "v_exp_f32 v78, v78\n\t"
      "v_exp_f32 v79, v79\n\t"
      "v_smfmac_f32_16x16x64_bf16 v[212:215], v[216:219], v[104:111], v237\n\t"
      "s_waitcnt lgkmcnt(0)\n\t"
      "v_cvt_pk_bf16_f32 v96, v64, v65\n\t"
      "v_cvt_pk_bf16_f32 v97, v66, v67\n\t"
      "v_cvt_pk_bf16_f32 v98, v68, v69\n\t"
      "v_cvt_pk_bf16_f32 v99, v70, v71\n\t"
      "v_cvt_pk_bf16_f32 v100, v72, v73\n\t"
      "v_cvt_pk_bf16_f32 v101, v74, v75\n\t"
      "v_cvt_pk_bf16_f32 v102, v76, v77\n\t"
      "v_cvt_pk_bf16_f32 v103, v78, v79\n\t"
      "v_mfma_f32_32x32x16_bf16 v[64:79], v[120:123], v[220:223], v[160:175]\n\t"
      "v_exp_f32 v80, v80\n\t"
      "v_exp_f32 v81, v81\n\t"
      "v_exp_f32 v82, v82\n\t"
      "v_exp_f32 v83, v83\n\t"
      "v_mfma_f32_32x32x16_bf16 v[64:79], v[124:127], v[224:227], v[64:79]\n\t"
      "v_exp_f32 v84, v84\n\t"
      "v_exp_f32 v85, v85\n\t"
      "v_exp_f32 v86, v86\n\t"
      "v_exp_f32 v87, v87\n\t"
      "v_mfma_f32_32x32x16_bf16 v[128:143], v[192:195], v[96:99], v[128:143]\n\t"
      "v_exp_f32 v88, v88\n\t"
      "v_exp_f32 v89, v89\n\t"
      "v_exp_f32 v90, v90\n\t"
      "v_exp_f32 v91, v91\n\t"
      "v_mfma_f32_32x32x16_bf16 v[128:143], v[196:199], v[100:103], v[128:143]\n\t"
      "v_exp_f32 v92, v92\n\t"
      "v_exp_f32 v93, v93\n\t"
      "v_exp_f32 v94, v94\n\t"
      "v_exp_f32 v95, v95\n\t"
      "v_smfmac_f32_16x16x64_bf16 v[208:211], v[216:219], v[96:103], v237\n\t"
      "v_cvt_pk_bf16_f32 v104, v80, v81\n\t"
      "v_cvt_pk_bf16_f32 v105, v82, v83\n\t"
      "v_cvt_pk_bf16_f32 v106, v84, v85\n\t"
      "v_cvt_pk_bf16_f32 v107, v86, v87\n\t"
      "v_cvt_pk_bf16_f32 v108, v88, v89\n\t"
      "v_cvt_pk_bf16_f32 v109, v90, v91\n\t"
      "v_cvt_pk_bf16_f32 v110, v92, v93\n\t"
      "v_cvt_pk_bf16_f32 v111, v94, v95\n\t"
      "v_mfma_f32_32x32x16_bf16 v[80:95], v[120:123], v[228:231], v[176:191]\n\t"
      "s_barrier\n\t"
      "ds_read_b128 v[112:115], %[vL16] offset:0\n\t"
      "v_exp_f32 v64, v64\n\t"
      "v_exp_f32 v65, v65\n\t"
      "v_exp_f32 v66, v66\n\t"
      "v_exp_f32 v67, v67\n\t"
      "v_mfma_f32_32x32x16_bf16 v[80:95], v[124:127], v[232:235], v[80:95]\n\t"
      "ds_read_b128 v[116:119], %[vL16] offset:1024\n\t"
      "v_exp_f32 v68, v68\n\t"
      "v_exp_f32 v69, v69\n\t"
      "v_exp_f32 v70, v70\n\t"
      "v_exp_f32 v71, v71\n\t"
      "v_mfma_f32_32x32x16_bf16 v[144:159], v[192:195], v[104:107], v[144:159]\n\t"
      "ds_read_b128 v[200:203], v236 offset:30720\n\t"
      "ds_read_b128 v[204:207], v236 offset:31744\n\t"
      "v_exp_f32 v72, v72\n\t"
      "v_exp_f32 v73, v73\n\t"
      "v_exp_f32 v74, v74\n\t"
      "v_exp_f32 v75, v75\n\t"
      "v_mfma_f32_32x32x16_bf16 v[144:159], v[196:199], v[108:111], v[144:159]\n\t"
      "v_exp_f32 v76, v76\n\t"
      "v_exp_f32 v77, v77\n\t"
      "v_exp_f32 v78, v78\n\t"
      "v_exp_f32 v79, v79\n\t"
      "v_smfmac_f32_16x16x64_bf16 v[212:215], v[216:219], v[104:111], v237\n\t"
      "s_waitcnt lgkmcnt(0)\n\t"
      "v_cvt_pk_bf16_f32 v96, v64, v65\n\t"
      "v_cvt_pk_bf16_f32 v97, v66, v67\n\t"
      "v_cvt_pk_bf16_f32 v98, v68, v69\n\t"
      "v_cvt_pk_bf16_f32 v99, v70, v71\n\t"
      "v_cvt_pk_bf16_f32 v100, v72, v73\n\t"
      "v_cvt_pk_bf16_f32 v101, v74, v75\n\t"
      "v_cvt_pk_bf16_f32 v102, v76, v77\n\t"
      "v_cvt_pk_bf16_f32 v103, v78, v79\n\t"
      "v_mfma_f32_32x32x16_bf16 v[64:79], v[112:115], v[220:223], v[160:175]\n\t"
      "s_sub_u32 s43, s43, 1\n\t"
      "v_exp_f32 v80, v80\n\t"
      "v_exp_f32 v81, v81\n\t"
      "v_exp_f32 v82, v82\n\t"
      "v_exp_f32 v83, v83\n\t"
      "v_mfma_f32_32x32x16_bf16 v[64:79], v[116:119], v[224:227], v[64:79]\n\t"
      "s_cmp_lg_u32 s43, 0\n\t"
      "v_exp_f32 v84, v84\n\t"
      "v_exp_f32 v85, v85\n\t"
      "v_exp_f32 v86, v86\n\t"
      "v_exp_f32 v87, v87\n\t"
      "v_mfma_f32_32x32x16_bf16 v[128:143], v[200:203], v[96:99], v[128:143]\n\t"
      "v_exp_f32 v88, v88\n\t"
      "v_exp_f32 v89, v89\n\t"
      "v_exp_f32 v90, v90\n\t"
      "v_exp_f32 v91, v91\n\t"
      "v_mfma_f32_32x32x16_bf16 v[128:143], v[204:207], v[100:103], v[128:143]\n\t"
      "v_exp_f32 v92, v92\n\t"
      "v_exp_f32 v93, v93\n\t"
      "v_exp_f32 v94, v94\n\t"
      "v_exp_f32 v95, v95\n\t"
      "v_smfmac_f32_16x16x64_bf16 v[208:211], v[216:219], v[96:103], v237\n\t"
      "v_cvt_pk_bf16_f32 v104, v80, v81\n\t"
      "v_cvt_pk_bf16_f32 v105, v82, v83\n\t"
      "v_cvt_pk_bf16_f32 v106, v84, v85\n\t"
      "v_cvt_pk_bf16_f32 v107, v86, v87\n\t"
      "v_cvt_pk_bf16_f32 v108, v88, v89\n\t"
      "v_cvt_pk_bf16_f32 v109, v90, v91\n\t"
      "v_cvt_pk_bf16_f32 v110, v92, v93\n\t"
      "v_cvt_pk_bf16_f32 v111, v94, v95\n\t"
      "s_cbranch_scc1 PA_LOOP_%=\n\t"
      "s_add_u32 s48, %[sOa], 294912\n\t"
      "v_mfma_f32_32x32x16_bf16 v[144:159], v[200:203], v[104:107], v[144:159]\n\t"
      "v_mfma_f32_32x32x16_bf16 v[144:159], v[204:207], v[108:111], v[144:159]\n\t"
      "v_smfmac_f32_16x16x64_bf16 v[212:215], v[216:219], v[104:111], v237\n\t"
      "v_add_f32 v208, v208, v209\n\t"
      "v_mov_b32 v209, v208\n\t"
      "s_nop 1\n\t"
      "v_permlane32_swap_b32 v208, v209\n\t"
      "s_nop 0\n\t"
      "v_rcp_f32 v208, v208\n\t"
      "s_nop 0\n\t"
      "v_mul_f32 v128, v128, v208\n\t"
      "v_mul_f32 v129, v129, v208\n\t"
      "v_mul_f32 v130, v130, v208\n\t"
      "v_mul_f32 v131, v131, v208\n\t"
      "v_mul_f32 v132, v132, v208\n\t"
      "v_mul_f32 v133, v133, v208\n\t"
      "v_mul_f32 v134, v134, v208\n\t"
      "v_mul_f32 v135, v135, v208\n\t"
      "v_mul_f32 v136, v136, v208\n\t"
      "v_mul_f32 v137, v137, v208\n\t"
      "v_mul_f32 v138, v138, v208\n\t"
      "v_mul_f32 v139, v139, v208\n\t"
      "v_mul_f32 v140, v140, v208\n\t"
      "v_mul_f32 v141, v141, v208\n\t"
      "v_mul_f32 v142, v142, v208\n\t"
      "v_mul_f32 v143, v143, v208\n\t"
      "v_cvt_pk_bf16_f32 v80, v128, v129\n\t"
      "v_cvt_pk_bf16_f32 v81, v130, v131\n\t"
      "v_cvt_pk_bf16_f32 v82, v132, v133\n\t"
      "v_cvt_pk_bf16_f32 v83, v134, v135\n\t"
      "v_cvt_pk_bf16_f32 v84, v136, v137\n\t"
      "v_cvt_pk_bf16_f32 v85, v138, v139\n\t"
      "v_cvt_pk_bf16_f32 v86, v140, v141\n\t"
      "v_cvt_pk_bf16_f32 v87, v142, v143\n\t"
      "buffer_store_dwordx4 v[80:83], %[vL16], %[srdWS], %[sOa] offen sc1\n\t"
      "buffer_store_dwordx4 v[84:87], %[vL16], %[srdWS], %[sOa] offen offset:1024 sc1\n\t"
      "v_add_f32 v212, v212, v213\n\t"
      "v_mov_b32 v213, v212\n\t"
      "s_nop 1\n\t"
      "v_permlane32_swap_b32 v212, v213\n\t"
      "s_nop 0\n\t"
      "v_rcp_f32 v212, v212\n\t"
      "s_nop 0\n\t"
      "v_mul_f32 v144, v144, v212\n\t"
      "v_mul_f32 v145, v145, v212\n\t"
      "v_mul_f32 v146, v146, v212\n\t"
      "v_mul_f32 v147, v147, v212\n\t"
      "v_mul_f32 v148, v148, v212\n\t"
      "v_mul_f32 v149, v149, v212\n\t"
      "v_mul_f32 v150, v150, v212\n\t"
      "v_mul_f32 v151, v151, v212\n\t"
      "v_mul_f32 v152, v152, v212\n\t"
      "v_mul_f32 v153, v153, v212\n\t"
      "v_mul_f32 v154, v154, v212\n\t"
      "v_mul_f32 v155, v155, v212\n\t"
      "v_mul_f32 v156, v156, v212\n\t"
      "v_mul_f32 v157, v157, v212\n\t"
      "v_mul_f32 v158, v158, v212\n\t"
      "v_mul_f32 v159, v159, v212\n\t"
      "v_cvt_pk_bf16_f32 v88, v144, v145\n\t"
      "v_cvt_pk_bf16_f32 v89, v146, v147\n\t"
      "v_cvt_pk_bf16_f32 v90, v148, v149\n\t"
      "v_cvt_pk_bf16_f32 v91, v150, v151\n\t"
      "v_cvt_pk_bf16_f32 v92, v152, v153\n\t"
      "v_cvt_pk_bf16_f32 v93, v154, v155\n\t"
      "v_cvt_pk_bf16_f32 v94, v156, v157\n\t"
      "v_cvt_pk_bf16_f32 v95, v158, v159\n\t"
      "buffer_store_dwordx4 v[88:91], %[vL16], %[srdWS], s48 offen sc1\n\t"
      "buffer_store_dwordx4 v[92:95], %[vL16], %[srdWS], s48 offen offset:1024 sc1\n\t"
      "v_lshrrev_b32 v236, 1, %[vL16]\n\t"
      "v_mov_b32 v30, %[taglo]\n\t"
      "v_mov_b32 v31, %[taghi]\n\t"
      "s_mov_b64 exec, 0xffff\n\t"
      "s_waitcnt vmcnt(0)\n\t"
      "buffer_store_dwordx2 v[30:31], v236, %[srdWS], %[sFO] offen sc1\n\t"
      "s_mov_b64 exec, -1\n\t"
      "s_endpgm\n\t"
      "P1_YOUNG_%=:\n\t"
      "s_add_u32 m0, s42, 32768\n\t"
      "v_lshl_add_u64 v[48:49], %[vWst], 0, v[48:49]\n\t"
      "global_load_lds_dwordx4 v[48:49], off\n\t"
      "s_add_u32 m0, s42, 40960\n\t"
      "v_lshl_add_u64 v[48:49], %[vWst], 0, v[48:49]\n\t"
      "global_load_lds_dwordx4 v[48:49], off\n\t"
      "s_add_u32 m0, s42, 49152\n\t"
      "v_lshl_add_u64 v[48:49], %[vWst], 0, v[48:49]\n\t"
      "global_load_lds_dwordx4 v[48:49], off\n\t"
      "s_add_u32 m0, s42, 57344\n\t"
      "v_lshl_add_u64 v[48:49], %[vWst], 0, v[48:49]\n\t"
      "global_load_lds_dwordx4 v[48:49], off\n\t"
      "s_add_u32 m0, s41, 32768\n\t"
      "s_add_u32 s40, s40, 4096\n\t"
      "buffer_load_dwordx4 %[vL16], %[srdWS], s40 offen offset:0 sc1 lds\n\t"
      "buffer_load_dwordx4 %[vL16], %[srdWS], s40 offen offset:1024 sc1 lds\n\t"
      "buffer_load_dwordx4 %[vL16], %[srdWS], s40 offen offset:2048 sc1 lds\n\t"
      "buffer_load_dwordx4 %[vL16], %[srdWS], s40 offen offset:3072 sc1 lds\n\t"
      "s_waitcnt vmcnt(8)\n\t"
      "s_barrier\n\t"
      "ds_read_b128 v[64:67], %[vXr] offset:0\n\t"
      "ds_read_b128 v[68:71], %[vXr] offset:256\n\t"
      "ds_read_b128 v[72:75], %[vXr] offset:4096\n\t"
      "ds_read_b64_tr_b16 v[96:97], %[vWr0] offset:0\n\t"
      "ds_read_b64_tr_b16 v[98:99], %[vWr0] offset:8192\n\t"
      "ds_read_b128 v[76:79], %[vXr] offset:4352\n\t"
      "ds_read_b64_tr_b16 v[100:101], %[vWr1] offset:0\n\t"
      "ds_read_b64_tr_b16 v[102:103], %[vWr1] offset:8192\n\t"
      "ds_read_b128 v[80:83], %[vXr] offset:8192\n\t"
      "ds_read_b64_tr_b16 v[104:105], %[vWr0] offset:256\n\t"
      "ds_read_b64_tr_b16 v[106:107], %[vWr0] offset:8448\n\t"
      "ds_read_b128 v[84:87], %[vXr] offset:8448\n\t"
      "ds_read_b64_tr_b16 v[108:109], %[vWr1] offset:256\n\t"
      "ds_read_b64_tr_b16 v[110:111], %[vWr1] offset:8448\n\t"
      "s_waitcnt lgkmcnt(0)\n\t"
      "s_cmp_eq_u32 %[light], 0\n\t"
      "s_cbranch_scc1 P1_HEAVYY_%=\n\t"
      "s_mov_b32 s43, 7\n\t"
      "P1_LIGHTY_%=:\n\t"
      "s_waitcnt lgkmcnt(10)\n\t"
      "v_mfma_f32_16x16x32_bf16 v[128:131], v[96:99], v[64:67], v[128:131]\n\t"
      "ds_read_b128 v[88:91], %[vXr] offset:12288\n\t"
      "s_waitcnt lgkmcnt(8)\n\t"
      "v_mfma_f32_16x16x32_bf16 v[132:135], v[100:103], v[64:67], v[132:135]\n\t"
      "ds_read_b64_tr_b16 v[112:113], %[vWr0] offset:16384\n\t"
      "v_mfma_f32_16x16x32_bf16 v[148:151], v[100:103], v[68:71], v[148:151]\n\t"
      "ds_read_b64_tr_b16 v[114:115], %[vWr0] offset:24576\n\t"
      "v_mfma_f32_16x16x32_bf16 v[144:147], v[96:99], v[68:71], v[144:147]\n\t"
      "s_waitcnt lgkmcnt(7)\n\t"
      "v_mfma_f32_16x16x32_bf16 v[152:155], v[104:107], v[68:71], v[152:155]\n\t"
      "ds_read_b128 v[92:95], %[vXr] offset:12544\n\t"
      "s_waitcnt lgkmcnt(5)\n\t"
      "v_mfma_f32_16x16x32_bf16 v[156:159], v[108:111], v[68:71], v[156:159]\n\t"
      "ds_read_b64_tr_b16 v[116:117], %[vWr1] offset:16384\n\t"
      "v_mfma_f32_16x16x32_bf16 v[140:143], v[108:111], v[64:67], v[140:143]\n\t"
      "ds_read_b64_tr_b16 v[118:119], %[vWr1] offset:24576\n\t"
      "v_mfma_f32_16x16x32_bf16 v[136:139], v[104:107], v[64:67], v[136:139]\n\t"
      "v_mfma_f32_16x16x32_bf16 v[168:171], v[104:107], v[72:75], v[168:171]\n\t"
      "ds_read_b128 v[64:67], %[vXr] offset:2048\n\t"
      "v_mfma_f32_16x16x32_bf16 v[172:175], v[108:111], v[72:75], v[172:175]\n\t"
      "ds_read_b64_tr_b16 v[120:121], %[vWr0] offset:16640\n\t"
      "v_mfma_f32_16x16x32_bf16 v[188:191], v[108:111], v[76:79], v[188:191]\n\t"
      "ds_read_b64_tr_b16 v[122:123], %[vWr0] offset:24832\n\t"
      "v_mfma_f32_16x16x32_bf16 v[184:187], v[104:107], v[76:79], v[184:187]\n\t"
      "buffer_load_dword v51, %[vPF], %[srdP], s47 offen\n\t"
      "s_add_u32 s47, s47, %[sPFs]\n\t"
      "v_mfma_f32_16x16x32_bf16 v[176:179], v[96:99], v[76:79], v[176:179]\n\t"
      "ds_read_b128 v[68:71], %[vXr] offset:2304\n\t"
      "v_mfma_f32_16x16x32_bf16 v[180:183], v[100:103], v[76:79], v[180:183]\n\t"
      "ds_read_b64_tr_b16 v[124:125], %[vWr1] offset:16640\n\t"
      "v_mfma_f32_16x16x32_bf16 v[164:167], v[100:103], v[72:75], v[164:167]\n\t"
      "ds_read_b64_tr_b16 v[126:127], %[vWr1] offset:24832\n\t"
      "v_mfma_f32_16x16x32_bf16 v[160:163], v[96:99], v[72:75], v[160:163]\n\t"
      "v_mfma_f32_16x16x32_bf16 v[192:195], v[96:99], v[80:83], v[192:195]\n\t"
      "ds_read_b128 v[72:75], %[vXr] offset:6144\n\t"
      "v_mfma_f32_16x16x32_bf16 v[196:199], v[100:103], v[80:83], v[196:199]\n\t"
      "s_waitcnt lgkmcnt(13)\n\t"
      "v_mfma_f32_16x16x32_bf16 v[212:215], v[100:103], v[84:87], v[212:215]\n\t"
      "v_mfma_f32_16x16x32_bf16 v[208:211], v[96:99], v[84:87], v[208:211]\n\t"
      "v_mfma_f32_16x16x32_bf16 v[216:219], v[104:107], v[84:87], v[216:219]\n\t"
      "ds_read_b128 v[76:79], %[vXr] offset:6400\n\t"
      "v_mfma_f32_16x16x32_bf16 v[220:223], v[108:111], v[84:87], v[220:223]\n\t"
      "v_mfma_f32_16x16x32_bf16 v[204:207], v[108:111], v[80:83], v[204:207]\n\t"
      "v_mfma_f32_16x16x32_bf16 v[200:203], v[104:107], v[80:83], v[200:203]\n\t"
      "s_waitcnt lgkmcnt(13)\n\t"
      "v_mfma_f32_16x16x32_bf16 v[232:235], v[104:107], v[88:91], v[232:235]\n\t"
      "ds_read_b128 v[80:83], %[vXr] offset:10240\n\t"
      "v_mfma_f32_16x16x32_bf16 v[236:239], v[108:111], v[88:91], v[236:239]\n\t"
      "s_waitcnt lgkmcnt(11)\n\t"
      "v_mfma_f32_16x16x32_bf16 v[252:255], v[108:111], v[92:95], v[252:255]\n\t"
      "v_mfma_f32_16x16x32_bf16 v[248:251], v[104:107], v[92:95], v[248:251]\n\t"
      "v_mfma_f32_16x16x32_bf16 v[240:243], v[96:99], v[92:95], v[240:243]\n\t"
      "ds_read_b128 v[84:87], %[vXr] offset:10496\n\t"
      "v_mfma_f32_16x16x32_bf16 v[244:247], v[100:103], v[92:95], v[244:247]\n\t"
      "v_mfma_f32_16x16x32_bf16 v[228:231], v[100:103], v[88:91], v[228:231]\n\t"
      "v_mfma_f32_16x16x32_bf16 v[224:227], v[96:99], v[88:91], v[224:227]\n\t"
      "s_waitcnt lgkmcnt(9)\n\t"
      "v_mfma_f32_16x16x32_bf16 v[128:131], v[112:115], v[64:67], v[128:131]\n\t"
      "ds_read_b128 v[88:91], %[vXr] offset:14336\n\t"
      "v_mfma_f32_16x16x32_bf16 v[132:135], v[116:119], v[64:67], v[132:135]\n\t"
      "ds_read_b128 v[92:95], %[vXr] offset:14592\n\t"
      "s_waitcnt lgkmcnt(8)\n\t"
      "v_mfma_f32_16x16x32_bf16 v[148:151], v[116:119], v[68:71], v[148:151]\n\t"
      "v_mfma_f32_16x16x32_bf16 v[144:147], v[112:115], v[68:71], v[144:147]\n\t"
      "v_mfma_f32_16x16x32_bf16 v[152:155], v[120:123], v[68:71], v[152:155]\n\t"
      "s_waitcnt lgkmcnt(6)\n\t"
      "v_mfma_f32_16x16x32_bf16 v[156:159], v[124:127], v[68:71], v[156:159]\n\t"
      "v_mfma_f32_16x16x32_bf16 v[140:143], v[124:127], v[64:67], v[140:143]\n\t"
      "v_mfma_f32_16x16x32_bf16 v[136:139], v[120:123], v[64:67], v[136:139]\n\t"
      "s_waitcnt vmcnt(1) lgkmcnt(0)\n\t"
      "s_barrier\n\t"
      "v_mfma_f32_16x16x32_bf16 v[168:171], v[120:123], v[72:75], v[168:171]\n\t"
      "ds_read_b128 v[64:67], %[vXr] offset:32768\n\t"
      "v_mfma_f32_16x16x32_bf16 v[172:175], v[124:127], v[72:75], v[172:175]\n\t"
      "s_add_u32 m0, s42, 0\n\t"
      "v_lshl_add_u64 v[48:49], %[vWst], 0, v[48:49]\n\t"
      "global_load_lds_dwordx4 v[48:49], off\n\t"
      "v_mfma_f32_16x16x32_bf16 v[188:191], v[124:127], v[76:79], v[188:191]\n\t"
      "s_add_u32 m0, s42, 8192\n\t"
      "v_lshl_add_u64 v[48:49], %[vWst], 0, v[48:49]\n\t"
      "global_load_lds_dwordx4 v[48:49], off\n\t"
      "v_mfma_f32_16x16x32_bf16 v[184:187], v[120:123], v[76:79], v[184:187]\n\t"
      "v_mfma_f32_16x16x32_bf16 v[176:179], v[112:115], v[76:79], v[176:179]\n\t"
      "ds_read_b128 v[68:71], %[vXr] offset:33024\n\t"
      "v_mfma_f32_16x16x32_bf16 v[180:183], v[116:119], v[76:79], v[180:183]\n\t"
      "ds_read_b64_tr_b16 v[96:97], %[vWr0] offset:32768\n\t"
      "v_mfma_f32_16x16x32_bf16 v[164:167], v[116:119], v[72:75], v[164:167]\n\t"
      "ds_read_b64_tr_b16 v[98:99], %[vWr0] offset:40960\n\t"
      "v_mfma_f32_16x16x32_bf16 v[160:163], v[112:115], v[72:75], v[160:163]\n\t"
      "s_add_u32 m0, s42, 16384\n\t"
      "v_lshl_add_u64 v[48:49], %[vWst], 0, v[48:49]\n\t"
      "global_load_lds_dwordx4 v[48:49], off\n\t"
      "s_add_u32 m0, s42, 24576\n\t"
      "v_lshl_add_u64 v[48:49], %[vWst], 0, v[48:49]\n\t"
      "global_load_lds_dwordx4 v[48:49], off\n\t"
      "v_mfma_f32_16x16x32_bf16 v[192:195], v[112:115], v[80:83], v[192:195]\n\t"
      "ds_read_b128 v[72:75], %[vXr] offset:36864\n\t"
      "v_mfma_f32_16x16x32_bf16 v[196:199], v[116:119], v[80:83], v[196:199]\n\t"
      "ds_read_b64_tr_b16 v[100:101], %[vWr1] offset:32768\n\t"
      "v_mfma_f32_16x16x32_bf16 v[212:215], v[116:119], v[84:87], v[212:215]\n\t"
      "ds_read_b64_tr_b16 v[102:103], %[vWr1] offset:40960\n\t"
      "v_mfma_f32_16x16x32_bf16 v[208:211], v[112:115], v[84:87], v[208:211]\n\t"
      "s_add_u32 m0, s41, 0\n\t"
      "s_add_u32 s40, s40, 4096\n\t"
      "buffer_load_dwordx4 %[vL16], %[srdWS], s40 offen offset:0 sc1 lds\n\t"
      "buffer_load_dwordx4 %[vL16], %[srdWS], s40 offen offset:1024 sc1 lds\n\t"
      "v_mfma_f32_16x16x32_bf16 v[216:219], v[120:123], v[84:87], v[216:219]\n\t"
      "ds_read_b128 v[76:79], %[vXr] offset:37120\n\t"
      "v_mfma_f32_16x16x32_bf16 v[220:223], v[124:127], v[84:87], v[220:223]\n\t"
      "ds_read_b64_tr_b16 v[104:105], %[vWr0] offset:33024\n\t"
      "v_mfma_f32_16x16x32_bf16 v[204:207], v[124:127], v[80:83], v[204:207]\n\t"
      "ds_read_b64_tr_b16 v[106:107], %[vWr0] offset:41216\n\t"
      "v_mfma_f32_16x16x32_bf16 v[200:203], v[120:123], v[80:83], v[200:203]\n\t"
      "buffer_load_dwordx4 %[vL16], %[srdWS], s40 offen offset:2048 sc1 lds\n\t"
      "buffer_load_dwordx4 %[vL16], %[srdWS], s40 offen offset:3072 sc1 lds\n\t"
      "v_mfma_f32_16x16x32_bf16 v[232:235], v[120:123], v[88:91], v[232:235]\n\t"
      "ds_read_b128 v[80:83], %[vXr] offset:40960\n\t"
      "v_mfma_f32_16x16x32_bf16 v[236:239], v[124:127], v[88:91], v[236:239]\n\t"
      "ds_read_b64_tr_b16 v[108:109], %[vWr1] offset:33024\n\t"
      "v_mfma_f32_16x16x32_bf16 v[252:255], v[124:127], v[92:95], v[252:255]\n\t"
      "ds_read_b64_tr_b16 v[110:111], %[vWr1] offset:41216\n\t"
      "v_mfma_f32_16x16x32_bf16 v[248:251], v[120:123], v[92:95], v[248:251]\n\t"
      "v_mfma_f32_16x16x32_bf16 v[240:243], v[112:115], v[92:95], v[240:243]\n\t"
      "ds_read_b128 v[84:87], %[vXr] offset:41216\n\t"
      "v_mfma_f32_16x16x32_bf16 v[244:247], v[116:119], v[92:95], v[244:247]\n\t"
      "v_mfma_f32_16x16x32_bf16 v[228:231], v[116:119], v[88:91], v[228:231]\n\t"
      "v_mfma_f32_16x16x32_bf16 v[224:227], v[112:115], v[88:91], v[224:227]\n\t"
      "s_waitcnt lgkmcnt(10)\n\t"
      "v_mfma_f32_16x16x32_bf16 v[128:131], v[96:99], v[64:67], v[128:131]\n\t"
      "ds_read_b128 v[88:91], %[vXr] offset:45056\n\t"
      "s_waitcnt lgkmcnt(8)\n\t"
      "v_mfma_f32_16x16x32_bf16 v[132:135], v[100:103], v[64:67], v[132:135]\n\t"
      "ds_read_b64_tr_b16 v[112:113], %[vWr0] offset:49152\n\t"
      "v_mfma_f32_16x16x32_bf16 v[148:151], v[100:103], v[68:71], v[148:151]\n\t"
      "ds_read_b64_tr_b16 v[114:115], %[vWr0] offset:57344\n\t"
      "v_mfma_f32_16x16x32_bf16 v[144:147], v[96:99], v[68:71], v[144:147]\n\t"
      "s_waitcnt lgkmcnt(7)\n\t"
      "v_mfma_f32_16x16x32_bf16 v[152:155], v[104:107], v[68:71], v[152:155]\n\t"
      "ds_read_b128 v[92:95], %[vXr] offset:45312\n\t"
      "s_waitcnt lgkmcnt(5)\n\t"
      "v_mfma_f32_16x16x32_bf16 v[156:159], v[108:111], v[68:71], v[156:159]\n\t"
      "ds_read_b64_tr_b16 v[116:117], %[vWr1] offset:49152\n\t"
      "v_mfma_f32_16x16x32_bf16 v[140:143], v[108:111], v[64:67], v[140:143]\n\t"
      "ds_read_b64_tr_b16 v[118:119], %[vWr1] offset:57344\n\t"
      "v_mfma_f32_16x16x32_bf16 v[136:139], v[104:107], v[64:67], v[136:139]\n\t"
      "v_mfma_f32_16x16x32_bf16 v[168:171], v[104:107], v[72:75], v[168:171]\n\t"
      "ds_read_b128 v[64:67], %[vXr] offset:34816\n\t"
      "v_mfma_f32_16x16x32_bf16 v[172:175], v[108:111], v[72:75], v[172:175]\n\t"
      "ds_read_b64_tr_b16 v[120:121], %[vWr0] offset:49408\n\t"
      "v_mfma_f32_16x16x32_bf16 v[188:191], v[108:111], v[76:79], v[188:191]\n\t"
      "ds_read_b64_tr_b16 v[122:123], %[vWr0] offset:57600\n\t"
      "v_mfma_f32_16x16x32_bf16 v[184:187], v[104:107], v[76:79], v[184:187]\n\t"
      "buffer_load_dword v51, %[vPF], %[srdP], s47 offen\n\t"
      "s_add_u32 s47, s47, %[sPFs]\n\t"
      "v_mfma_f32_16x16x32_bf16 v[176:179], v[96:99], v[76:79], v[176:179]\n\t"
      "ds_read_b128 v[68:71], %[vXr] offset:35072\n\t"
      "v_mfma_f32_16x16x32_bf16 v[180:183], v[100:103], v[76:79], v[180:183]\n\t"
      "ds_read_b64_tr_b16 v[124:125], %[vWr1] offset:49408\n\t"
      "v_mfma_f32_16x16x32_bf16 v[164:167], v[100:103], v[72:75], v[164:167]\n\t"
      "ds_read_b64_tr_b16 v[126:127], %[vWr1] offset:57600\n\t"
      "v_mfma_f32_16x16x32_bf16 v[160:163], v[96:99], v[72:75], v[160:163]\n\t"
      "v_mfma_f32_16x16x32_bf16 v[192:195], v[96:99], v[80:83], v[192:195]\n\t"
      "ds_read_b128 v[72:75], %[vXr] offset:38912\n\t"
      "v_mfma_f32_16x16x32_bf16 v[196:199], v[100:103], v[80:83], v[196:199]\n\t"
      "s_waitcnt lgkmcnt(13)\n\t"
      "v_mfma_f32_16x16x32_bf16 v[212:215], v[100:103], v[84:87], v[212:215]\n\t"
      "v_mfma_f32_16x16x32_bf16 v[208:211], v[96:99], v[84:87], v[208:211]\n\t"
      "v_mfma_f32_16x16x32_bf16 v[216:219], v[104:107], v[84:87], v[216:219]\n\t"
      "ds_read_b128 v[76:79], %[vXr] offset:39168\n\t"
      "v_mfma_f32_16x16x32_bf16 v[220:223], v[108:111], v[84:87], v[220:223]\n\t"
      "v_mfma_f32_16x16x32_bf16 v[204:207], v[108:111], v[80:83], v[204:207]\n\t"
      "v_mfma_f32_16x16x32_bf16 v[200:203], v[104:107], v[80:83], v[200:203]\n\t"
      "s_waitcnt lgkmcnt(13)\n\t"
      "v_mfma_f32_16x16x32_bf16 v[232:235], v[104:107], v[88:91], v[232:235]\n\t"
      "ds_read_b128 v[80:83], %[vXr] offset:43008\n\t"
      "v_mfma_f32_16x16x32_bf16 v[236:239], v[108:111], v[88:91], v[236:239]\n\t"
      "s_waitcnt lgkmcnt(11)\n\t"
      "v_mfma_f32_16x16x32_bf16 v[252:255], v[108:111], v[92:95], v[252:255]\n\t"
      "v_mfma_f32_16x16x32_bf16 v[248:251], v[104:107], v[92:95], v[248:251]\n\t"
      "v_mfma_f32_16x16x32_bf16 v[240:243], v[96:99], v[92:95], v[240:243]\n\t"
      "ds_read_b128 v[84:87], %[vXr] offset:43264\n\t"
      "v_mfma_f32_16x16x32_bf16 v[244:247], v[100:103], v[92:95], v[244:247]\n\t"
      "v_mfma_f32_16x16x32_bf16 v[228:231], v[100:103], v[88:91], v[228:231]\n\t"
      "v_mfma_f32_16x16x32_bf16 v[224:227], v[96:99], v[88:91], v[224:227]\n\t"
      "s_waitcnt lgkmcnt(9)\n\t"
      "v_mfma_f32_16x16x32_bf16 v[128:131], v[112:115], v[64:67], v[128:131]\n\t"
      "ds_read_b128 v[88:91], %[vXr] offset:47104\n\t"
      "v_mfma_f32_16x16x32_bf16 v[132:135], v[116:119], v[64:67], v[132:135]\n\t"
      "ds_read_b128 v[92:95], %[vXr] offset:47360\n\t"
      "s_waitcnt lgkmcnt(8)\n\t"
      "v_mfma_f32_16x16x32_bf16 v[148:151], v[116:119], v[68:71], v[148:151]\n\t"
      "v_mfma_f32_16x16x32_bf16 v[144:147], v[112:115], v[68:71], v[144:147]\n\t"
      "v_mfma_f32_16x16x32_bf16 v[152:155], v[120:123], v[68:71], v[152:155]\n\t"
      "s_waitcnt lgkmcnt(6)\n\t"
      "v_mfma_f32_16x16x32_bf16 v[156:159], v[124:127], v[68:71], v[156:159]\n\t"
      "v_mfma_f32_16x16x32_bf16 v[140:143], v[124:127], v[64:67], v[140:143]\n\t"
      "v_mfma_f32_16x16x32_bf16 v[136:139], v[120:123], v[64:67], v[136:139]\n\t"
      "s_waitcnt vmcnt(1) lgkmcnt(0)\n\t"
      "s_barrier\n\t"
      "v_mfma_f32_16x16x32_bf16 v[168:171], v[120:123], v[72:75], v[168:171]\n\t"
      "ds_read_b128 v[64:67], %[vXr] offset:0\n\t"
      "v_mfma_f32_16x16x32_bf16 v[172:175], v[124:127], v[72:75], v[172:175]\n\t"
      "s_add_u32 m0, s42, 32768\n\t"
      "v_lshl_add_u64 v[48:49], %[vWst], 0, v[48:49]\n\t"
      "global_load_lds_dwordx4 v[48:49], off\n\t"
      "v_mfma_f32_16x16x32_bf16 v[188:191], v[124:127], v[76:79], v[188:191]\n\t"
      "s_add_u32 m0, s42, 40960\n\t"
      "v_lshl_add_u64 v[48:49], %[vWst], 0, v[48:49]\n\t"
      "global_load_lds_dwordx4 v[48:49], off\n\t"
      "v_mfma_f32_16x16x32_bf16 v[184:187], v[120:123], v[76:79], v[184:187]\n\t"
      "v_mfma_f32_16x16x32_bf16 v[176:179], v[112:115], v[76:79], v[176:179]\n\t"
      "ds_read_b128 v[68:71], %[vXr] offset:256\n\t"
      "v_mfma_f32_16x16x32_bf16 v[180:183], v[116:119], v[76:79], v[180:183]\n\t"
      "ds_read_b64_tr_b16 v[96:97], %[vWr0] offset:0\n\t"
      "v_mfma_f32_16x16x32_bf16 v[164:167], v[116:119], v[72:75], v[164:167]\n\t"
      "ds_read_b64_tr_b16 v[98:99], %[vWr0] offset:8192\n\t"
      "v_mfma_f32_16x16x32_bf16 v[160:163], v[112:115], v[72:75], v[160:163]\n\t"
      "s_add_u32 m0, s42, 49152\n\t"
      "v_lshl_add_u64 v[48:49], %[vWst], 0, v[48:49]\n\t"
      "global_load_lds_dwordx4 v[48:49], off\n\t"
      "s_add_u32 m0, s42, 57344\n\t"
      "v_lshl_add_u64 v[48:49], %[vWst], 0, v[48:49]\n\t"
      "global_load_lds_dwordx4 v[48:49], off\n\t"
      "v_mfma_f32_16x16x32_bf16 v[192:195], v[112:115], v[80:83], v[192:195]\n\t"
      "ds_read_b128 v[72:75], %[vXr] offset:4096\n\t"
      "v_mfma_f32_16x16x32_bf16 v[196:199], v[116:119], v[80:83], v[196:199]\n\t"
      "ds_read_b64_tr_b16 v[100:101], %[vWr1] offset:0\n\t"
      "v_mfma_f32_16x16x32_bf16 v[212:215], v[116:119], v[84:87], v[212:215]\n\t"
      "ds_read_b64_tr_b16 v[102:103], %[vWr1] offset:8192\n\t"
      "v_mfma_f32_16x16x32_bf16 v[208:211], v[112:115], v[84:87], v[208:211]\n\t"
      "s_add_u32 m0, s41, 32768\n\t"
      "s_add_u32 s40, s40, 4096\n\t"
      "buffer_load_dwordx4 %[vL16], %[srdWS], s40 offen offset:0 sc1 lds\n\t"
      "buffer_load_dwordx4 %[vL16], %[srdWS], s40 offen offset:1024 sc1 lds\n\t"
      "v_mfma_f32_16x16x32_bf16 v[216:219], v[120:123], v[84:87], v[216:219]\n\t"
      "ds_read_b128 v[76:79], %[vXr] offset:4352\n\t"
      "v_mfma_f32_16x16x32_bf16 v[220:223], v[124:127], v[84:87], v[220:223]\n\t"
      "ds_read_b64_tr_b16 v[104:105], %[vWr0] offset:256\n\t"
      "v_mfma_f32_16x16x32_bf16 v[204:207], v[124:127], v[80:83], v[204:207]\n\t"
      "ds_read_b64_tr_b16 v[106:107], %[vWr0] offset:8448\n\t"
      "v_mfma_f32_16x16x32_bf16 v[200:203], v[120:123], v[80:83], v[200:203]\n\t"
      "buffer_load_dwordx4 %[vL16], %[srdWS], s40 offen offset:2048 sc1 lds\n\t"
      "buffer_load_dwordx4 %[vL16], %[srdWS], s40 offen offset:3072 sc1 lds\n\t"
      "v_mfma_f32_16x16x32_bf16 v[232:235], v[120:123], v[88:91], v[232:235]\n\t"
      "ds_read_b128 v[80:83], %[vXr] offset:8192\n\t"
      "v_mfma_f32_16x16x32_bf16 v[236:239], v[124:127], v[88:91], v[236:239]\n\t"
      "ds_read_b64_tr_b16 v[108:109], %[vWr1] offset:256\n\t"
      "v_mfma_f32_16x16x32_bf16 v[252:255], v[124:127], v[92:95], v[252:255]\n\t"
      "ds_read_b64_tr_b16 v[110:111], %[vWr1] offset:8448\n\t"
      "v_mfma_f32_16x16x32_bf16 v[248:251], v[120:123], v[92:95], v[248:251]\n\t"
      "v_mfma_f32_16x16x32_bf16 v[240:243], v[112:115], v[92:95], v[240:243]\n\t"
      "ds_read_b128 v[84:87], %[vXr] offset:8448\n\t"
      "v_mfma_f32_16x16x32_bf16 v[244:247], v[116:119], v[92:95], v[244:247]\n\t"
      "v_mfma_f32_16x16x32_bf16 v[228:231], v[116:119], v[88:91], v[228:231]\n\t"
      "v_mfma_f32_16x16x32_bf16 v[224:227], v[112:115], v[88:91], v[224:227]\n\t"
      "s_sub_u32 s43, s43, 1\n\t"
      "s_cmp_lg_u32 s43, 0\n\t"
      "s_cbranch_scc1 P1_LIGHTY_%=\n\t"
      "s_branch P1_T14Y_%=\n\t"
      "P1_HEAVYY_%=:\n\t"
      "s_add_u32 m0, s42, 147456\n\t"
      "s_add_u32 s48, s42, 57344\n\t"
      "buffer_load_dwordx4 %[vKVsrc], %[srdKW], s48 offen lds\n\t"
      "s_mov_b32 s43, 5\n\t"
      "P1_LOOPY_%=:\n\t"
      "s_waitcnt lgkmcnt(10)\n\t"
      "v_mfma_f32_16x16x32_bf16 v[128:131], v[96:99], v[64:67], v[128:131]\n\t"
      "ds_read_b128 v[88:91], %[vXr] offset:12288\n\t"
      "s_waitcnt lgkmcnt(8)\n\t"
      "v_mfma_f32_16x16x32_bf16 v[132:135], v[100:103], v[64:67], v[132:135]\n\t"
      "ds_read_b64_tr_b16 v[112:113], %[vWr0] offset:16384\n\t"
      "v_mfma_f32_16x16x32_bf16 v[148:151], v[100:103], v[68:71], v[148:151]\n\t"
      "ds_read_b64_tr_b16 v[114:115], %[vWr0] offset:24576\n\t"
      "v_mfma_f32_16x16x32_bf16 v[144:147], v[96:99], v[68:71], v[144:147]\n\t"
      "s_waitcnt lgkmcnt(7)\n\t"
      "v_mfma_f32_16x16x32_bf16 v[152:155], v[104:107], v[68:71], v[152:155]\n\t"
      "ds_read_b128 v[92:95], %[vXr] offset:12544\n\t"
      "s_waitcnt lgkmcnt(5)\n\t"
      "v_mfma_f32_16x16x32_bf16 v[156:159], v[108:111], v[68:71], v[156:159]\n\t"
      "ds_read_b64_tr_b16 v[116:117], %[vWr1] offset:16384\n\t"
      "v_mfma_f32_16x16x32_bf16 v[140:143], v[108:111], v[64:67], v[140:143]\n\t"
      "ds_read_b64_tr_b16 v[118:119], %[vWr1] offset:24576\n\t"
      "v_mfma_f32_16x16x32_bf16 v[136:139], v[104:107], v[64:67], v[136:139]\n\t"
      "v_mfma_f32_16x16x32_bf16 v[168:171], v[104:107], v[72:75], v[168:171]\n\t"
      "ds_read_b128 v[64:67], %[vXr] offset:2048\n\t"
      "v_mfma_f32_16x16x32_bf16 v[172:175], v[108:111], v[72:75], v[172:175]\n\t"
      "ds_read_b64_tr_b16 v[120:121], %[vWr0] offset:16640\n\t"
      "v_mfma_f32_16x16x32_bf16 v[188:191], v[108:111], v[76:79], v[188:191]\n\t"
      "ds_read_b64_tr_b16 v[122:123], %[vWr0] offset:24832\n\t"
      "v_mfma_f32_16x16x32_bf16 v[184:187], v[104:107], v[76:79], v[184:187]\n\t"
      "buffer_load_dword v51, %[vPF], %[srdP], s47 offen\n\t"
      "s_add_u32 s47, s47, %[sPFs]\n\t"
      "v_mfma_f32_16x16x32_bf16 v[176:179], v[96:99], v[76:79], v[176:179]\n\t"
      "ds_read_b128 v[68:71], %[vXr] offset:2304\n\t"
      "v_mfma_f32_16x16x32_bf16 v[180:183], v[100:103], v[76:79], v[180:183]\n\t"
      "ds_read_b64_tr_b16 v[124:125], %[vWr1] offset:16640\n\t"
      "v_mfma_f32_16x16x32_bf16 v[164:167], v[100:103], v[72:75], v[164:167]\n\t"
      "ds_read_b64_tr_b16 v[126:127], %[vWr1] offset:24832\n\t"
      "v_mfma_f32_16x16x32_bf16 v[160:163], v[96:99], v[72:75], v[160:163]\n\t"
      "v_mfma_f32_16x16x32_bf16 v[192:195], v[96:99], v[80:83], v[192:195]\n\t"
      "ds_read_b128 v[72:75], %[vXr] offset:6144\n\t"
      "v_mfma_f32_16x16x32_bf16 v[196:199], v[100:103], v[80:83], v[196:199]\n\t"
      "ds_read_b64_tr_b16 v[52:53], v50 offset:0\n\t"
      "s_waitcnt lgkmcnt(14)\n\t"
      "v_mfma_f32_16x16x32_bf16 v[212:215], v[100:103], v[84:87], v[212:215]\n\t"
      "ds_read_b64_tr_b16 v[54:55], v50 offset:1024\n\t"
      "v_mfma_f32_16x16x32_bf16 v[208:211], v[96:99], v[84:87], v[208:211]\n\t"
      "v_mfma_f32_16x16x32_bf16 v[216:219], v[104:107], v[84:87], v[216:219]\n\t"
      "ds_read_b128 v[76:79], %[vXr] offset:6400\n\t"
      "v_mfma_f32_16x16x32_bf16 v[220:223], v[108:111], v[84:87], v[220:223]\n\t"
      "ds_read_b128 v[56:59], %[vXkv] offset:0\n\t"
      "v_mfma_f32_16x16x32_bf16 v[204:207], v[108:111], v[80:83], v[204:207]\n\t"
      "v_mfma_f32_16x16x32_bf16 v[200:203], v[104:107], v[80:83], v[200:203]\n\t"
      "s_waitcnt lgkmcnt(14)\n\t"
      "v_mfma_f32_16x16x32_bf16 v[232:235], v[104:107], v[88:91], v[232:235]\n\t"
      "ds_read_b128 v[80:83], %[vXr] offset:10240\n\t"
      "v_mfma_f32_16x16x32_bf16 v[236:239], v[108:111], v[88:91], v[236:239]\n\t"
      "s_waitcnt lgkmcnt(14)\n\t"
      "v_mfma_f32_16x16x32_bf16 v[252:255], v[108:111], v[92:95], v[252:255]\n\t"
      "v_mfma_f32_16x16x32_bf16 v[248:251], v[104:107], v[92:95], v[248:251]\n\t"
      "v_mfma_f32_16x16x32_bf16 v[240:243], v[96:99], v[92:95], v[240:243]\n\t"
      "ds_read_b128 v[84:87], %[vXr] offset:10496\n\t"
      "v_mfma_f32_16x16x32_bf16 v[244:247], v[100:103], v[92:95], v[244:247]\n\t"
      "v_mfma_f32_16x16x32_bf16 v[228:231], v[100:103], v[88:91], v[228:231]\n\t"
      "v_mfma_f32_16x16x32_bf16 v[224:227], v[96:99], v[88:91], v[224:227]\n\t"
      "s_waitcnt lgkmcnt(12)\n\t"
      "v_mfma_f32_16x16x32_bf16 v[128:131], v[112:115], v[64:67], v[128:131]\n\t"
      "ds_read_b128 v[88:91], %[vXr] offset:14336\n\t"
      "v_mfma_f32_16x16x32_bf16 v[132:135], v[116:119], v[64:67], v[132:135]\n\t"
      "ds_read_b128 v[92:95], %[vXr] offset:14592\n\t"
      "s_waitcnt lgkmcnt(11)\n\t"
      "v_mfma_f32_16x16x32_bf16 v[148:151], v[116:119], v[68:71], v[148:151]\n\t"
      "v_mfma_f32_16x16x32_bf16 v[144:147], v[112:115], v[68:71], v[144:147]\n\t"
      "v_mfma_f32_16x16x32_bf16 v[152:155], v[120:123], v[68:71], v[152:155]\n\t"
      "s_waitcnt lgkmcnt(9)\n\t"
      "v_mfma_f32_16x16x32_bf16 v[156:159], v[124:127], v[68:71], v[156:159]\n\t"
      "v_mfma_f32_16x16x32_bf16 v[140:143], v[124:127], v[64:67], v[140:143]\n\t"
      "v_mfma_f32_16x16x32_bf16 v[136:139], v[120:123], v[64:67], v[136:139]\n\t"
      "s_waitcnt vmcnt(1) lgkmcnt(0)\n\t"
      "s_barrier\n\t"
      "v_mfma_f32_16x16x32_bf16 v[60:63], v[52:55], v[56:59], v[60:63]\n\t"
      "v_mfma_f32_16x16x32_bf16 v[168:171], v[120:123], v[72:75], v[168:171]\n\t"
      "ds_read_b128 v[64:67], %[vXr] offset:32768\n\t"
      "v_mfma_f32_16x16x32_bf16 v[172:175], v[124:127], v[72:75], v[172:175]\n\t"
      "s_add_u32 m0, s42, 0\n\t"
      "v_lshl_add_u64 v[48:49], %[vWst], 0, v[48:49]\n\t"
      "global_load_lds_dwordx4 v[48:49], off\n\t"
      "v_mfma_f32_16x16x32_bf16 v[188:191], v[124:127], v[76:79], v[188:191]\n\t"
      "s_mov_b32 m0, s46\n\t"
      "s_xor_b32 s46, s46, 8192\n\t"
      "buffer_load_dwordx4 %[vKVsrc], %[srdKW], s45 offen lds\n\t"
      "s_add_u32 s45, s45, 8192\n\t"
      "v_mfma_f32_16x16x32_bf16 v[184:187], v[120:123], v[76:79], v[184:187]\n\t"
      "s_add_u32 m0, s42, 8192\n\t"
      "v_lshl_add_u64 v[48:49], %[vWst], 0, v[48:49]\n\t"
      "global_load_lds_dwordx4 v[48:49], off\n\t"
      "v_mfma_f32_16x16x32_bf16 v[176:179], v[112:115], v[76:79], v[176:179]\n\t"
      "ds_read_b128 v[68:71], %[vXr] offset:33024\n\t"
      "v_mfma_f32_16x16x32_bf16 v[180:183], v[116:119], v[76:79], v[180:183]\n\t"
      "ds_read_b64_tr_b16 v[96:97], %[vWr0] offset:32768\n\t"
      "v_mfma_f32_16x16x32_bf16 v[164:167], v[116:119], v[72:75], v[164:167]\n\t"
      "ds_read_b64_tr_b16 v[98:99], %[vWr0] offset:40960\n\t"
      "v_mfma_f32_16x16x32_bf16 v[160:163], v[112:115], v[72:75], v[160:163]\n\t"
      "s_add_u32 m0, s42, 16384\n\t"
      "v_lshl_add_u64 v[48:49], %[vWst], 0, v[48:49]\n\t"
      "global_load_lds_dwordx4 v[48:49], off\n\t"
      "s_add_u32 m0, s42, 24576\n\t"
      "v_lshl_add_u64 v[48:49], %[vWst], 0, v[48:49]\n\t"
      "global_load_lds_dwordx4 v[48:49], off\n\t"
      "v_mfma_f32_16x16x32_bf16 v[192:195], v[112:115], v[80:83], v[192:195]\n\t"
      "ds_read_b128 v[72:75], %[vXr] offset:36864\n\t"
      "v_mfma_f32_16x16x32_bf16 v[196:199], v[116:119], v[80:83], v[196:199]\n\t"
      "ds_read_b64_tr_b16 v[100:101], %[vWr1] offset:32768\n\t"
      "v_mfma_f32_16x16x32_bf16 v[212:215], v[116:119], v[84:87], v[212:215]\n\t"
      "ds_read_b64_tr_b16 v[102:103], %[vWr1] offset:40960\n\t"
      "v_mfma_f32_16x16x32_bf16 v[208:211], v[112:115], v[84:87], v[208:211]\n\t"
      "s_add_u32 m0, s41, 0\n\t"
      "s_add_u32 s40, s40, 4096\n\t"
      "buffer_load_dwordx4 %[vL16], %[srdWS], s40 offen offset:0 sc1 lds\n\t"
      "buffer_load_dwordx4 %[vL16], %[srdWS], s40 offen offset:1024 sc1 lds\n\t"
      "v_mfma_f32_16x16x32_bf16 v[216:219], v[120:123], v[84:87], v[216:219]\n\t"
      "ds_read_b128 v[76:79], %[vXr] offset:37120\n\t"
      "v_mfma_f32_16x16x32_bf16 v[220:223], v[124:127], v[84:87], v[220:223]\n\t"
      "ds_read_b64_tr_b16 v[104:105], %[vWr0] offset:33024\n\t"
      "v_mfma_f32_16x16x32_bf16 v[204:207], v[124:127], v[80:83], v[204:207]\n\t"
      "ds_read_b64_tr_b16 v[106:107], %[vWr0] offset:41216\n\t"
      "v_mfma_f32_16x16x32_bf16 v[200:203], v[120:123], v[80:83], v[200:203]\n\t"
      "buffer_load_dwordx4 %[vL16], %[srdWS], s40 offen offset:2048 sc1 lds\n\t"
      "buffer_load_dwordx4 %[vL16], %[srdWS], s40 offen offset:3072 sc1 lds\n\t"
      "v_mfma_f32_16x16x32_bf16 v[232:235], v[120:123], v[88:91], v[232:235]\n\t"
      "ds_read_b128 v[80:83], %[vXr] offset:40960\n\t"
      "v_mfma_f32_16x16x32_bf16 v[236:239], v[124:127], v[88:91], v[236:239]\n\t"
      "ds_read_b64_tr_b16 v[108:109], %[vWr1] offset:33024\n\t"
      "v_mfma_f32_16x16x32_bf16 v[252:255], v[124:127], v[92:95], v[252:255]\n\t"
      "ds_read_b64_tr_b16 v[110:111], %[vWr1] offset:41216\n\t"
      "v_mfma_f32_16x16x32_bf16 v[248:251], v[120:123], v[92:95], v[248:251]\n\t"
      "v_mfma_f32_16x16x32_bf16 v[240:243], v[112:115], v[92:95], v[240:243]\n\t"
      "ds_read_b128 v[84:87], %[vXr] offset:41216\n\t"
      "v_mfma_f32_16x16x32_bf16 v[244:247], v[116:119], v[92:95], v[244:247]\n\t"
      "v_mfma_f32_16x16x32_bf16 v[228:231], v[116:119], v[88:91], v[228:231]\n\t"
      "v_mfma_f32_16x16x32_bf16 v[224:227], v[112:115], v[88:91], v[224:227]\n\t"
      "s_waitcnt lgkmcnt(10)\n\t"
      "v_mfma_f32_16x16x32_bf16 v[128:131], v[96:99], v[64:67], v[128:131]\n\t"
      "ds_read_b128 v[88:91], %[vXr] offset:45056\n\t"
      "s_waitcnt lgkmcnt(8)\n\t"
      "v_mfma_f32_16x16x32_bf16 v[132:135], v[100:103], v[64:67], v[132:135]\n\t"
      "ds_read_b64_tr_b16 v[112:113], %[vWr0] offset:49152\n\t"
      "v_mfma_f32_16x16x32_bf16 v[148:151], v[100:103], v[68:71], v[148:151]\n\t"
      "ds_read_b64_tr_b16 v[114:115], %[vWr0] offset:57344\n\t"
      "v_mfma_f32_16x16x32_bf16 v[144:147], v[96:99], v[68:71], v[144:147]\n\t"
      "s_waitcnt lgkmcnt(7)\n\t"
      "v_mfma_f32_16x16x32_bf16 v[152:155], v[104:107], v[68:71], v[152:155]\n\t"
      "ds_read_b128 v[92:95], %[vXr] offset:45312\n\t"
      "s_waitcnt lgkmcnt(5)\n\t"
      "v_mfma_f32_16x16x32_bf16 v[156:159], v[108:111], v[68:71], v[156:159]\n\t"
      "ds_read_b64_tr_b16 v[116:117], %[vWr1] offset:49152\n\t"
      "v_mfma_f32_16x16x32_bf16 v[140:143], v[108:111], v[64:67], v[140:143]\n\t"
      "ds_read_b64_tr_b16 v[118:119], %[vWr1] offset:57344\n\t"
      "v_mfma_f32_16x16x32_bf16 v[136:139], v[104:107], v[64:67], v[136:139]\n\t"
      "v_mfma_f32_16x16x32_bf16 v[168:171], v[104:107], v[72:75], v[168:171]\n\t"
      "ds_read_b128 v[64:67], %[vXr] offset:34816\n\t"
      "v_mfma_f32_16x16x32_bf16 v[172:175], v[108:111], v[72:75], v[172:175]\n\t"
      "ds_read_b64_tr_b16 v[120:121], %[vWr0] offset:49408\n\t"
      "v_mfma_f32_16x16x32_bf16 v[188:191], v[108:111], v[76:79], v[188:191]\n\t"
      "ds_read_b64_tr_b16 v[122:123], %[vWr0] offset:57600\n\t"
      "v_mfma_f32_16x16x32_bf16 v[184:187], v[104:107], v[76:79], v[184:187]\n\t"
      "buffer_load_dword v51, %[vPF], %[srdP], s47 offen\n\t"
      "s_add_u32 s47, s47, %[sPFs]\n\t"
      "v_mfma_f32_16x16x32_bf16 v[176:179], v[96:99], v[76:79], v[176:179]\n\t"
      "ds_read_b128 v[68:71], %[vXr] offset:35072\n\t"
      "v_mfma_f32_16x16x32_bf16 v[180:183], v[100:103], v[76:79], v[180:183]\n\t"
      "ds_read_b64_tr_b16 v[124:125], %[vWr1] offset:49408\n\t"
      "v_mfma_f32_16x16x32_bf16 v[164:167], v[100:103], v[72:75], v[164:167]\n\t"
      "ds_read_b64_tr_b16 v[126:127], %[vWr1] offset:57600\n\t"
      "v_mfma_f32_16x16x32_bf16 v[160:163], v[96:99], v[72:75], v[160:163]\n\t"
      "v_mfma_f32_16x16x32_bf16 v[192:195], v[96:99], v[80:83], v[192:195]\n\t"
      "ds_read_b128 v[72:75], %[vXr] offset:38912\n\t"
      "v_mfma_f32_16x16x32_bf16 v[196:199], v[100:103], v[80:83], v[196:199]\n\t"
      "ds_read_b64_tr_b16 v[52:53], v50 offset:4096\n\t"
      "s_waitcnt lgkmcnt(14)\n\t"
      "v_mfma_f32_16x16x32_bf16 v[212:215], v[100:103], v[84:87], v[212:215]\n\t"
      "ds_read_b64_tr_b16 v[54:55], v50 offset:5120\n\t"
      "v_mfma_f32_16x16x32_bf16 v[208:211], v[96:99], v[84:87], v[208:211]\n\t"
      "v_mfma_f32_16x16x32_bf16 v[216:219], v[104:107], v[84:87], v[216:219]\n\t"
      "ds_read_b128 v[76:79], %[vXr] offset:39168\n\t"
      "v_mfma_f32_16x16x32_bf16 v[220:223], v[108:111], v[84:87], v[220:223]\n\t"
      "ds_read_b128 v[56:59], %[vXkv] offset:32768\n\t"
      "v_mfma_f32_16x16x32_bf16 v[204:207], v[108:111], v[80:83], v[204:207]\n\t"
      "v_mfma_f32_16x16x32_bf16 v[200:203], v[104:107], v[80:83], v[200:203]\n\t"
      "s_waitcnt lgkmcnt(14)\n\t"
      "v_mfma_f32_16x16x32_bf16 v[232:235], v[104:107], v[88:91], v[232:235]\n\t"
      "ds_read_b128 v[80:83], %[vXr] offset:43008\n\t"
      "v_mfma_f32_16x16x32_bf16 v[236:239], v[108:111], v[88:91], v[236:239]\n\t"
      "s_waitcnt lgkmcnt(14)\n\t"
      "v_mfma_f32_16x16x32_bf16 v[252:255], v[108:111], v[92:95], v[252:255]\n\t"
      "v_mfma_f32_16x16x32_bf16 v[248:251], v[104:107], v[92:95], v[248:251]\n\t"
      "v_mfma_f32_16x16x32_bf16 v[240:243], v[96:99], v[92:95], v[240:243]\n\t"
      "ds_read_b128 v[84:87], %[vXr] offset:43264\n\t"
      "v_mfma_f32_16x16x32_bf16 v[244:247], v[100:103], v[92:95], v[244:247]\n\t"
      "v_mfma_f32_16x16x32_bf16 v[228:231], v[100:103], v[88:91], v[228:231]\n\t"
      "v_mfma_f32_16x16x32_bf16 v[224:227], v[96:99], v[88:91], v[224:227]\n\t"
      "s_waitcnt lgkmcnt(12)\n\t"
      "v_mfma_f32_16x16x32_bf16 v[128:131], v[112:115], v[64:67], v[128:131]\n\t"
      "ds_read_b128 v[88:91], %[vXr] offset:47104\n\t"
      "v_mfma_f32_16x16x32_bf16 v[132:135], v[116:119], v[64:67], v[132:135]\n\t"
      "ds_read_b128 v[92:95], %[vXr] offset:47360\n\t"
      "s_waitcnt lgkmcnt(11)\n\t"
      "v_mfma_f32_16x16x32_bf16 v[148:151], v[116:119], v[68:71], v[148:151]\n\t"
      "v_mfma_f32_16x16x32_bf16 v[144:147], v[112:115], v[68:71], v[144:147]\n\t"
      "v_mfma_f32_16x16x32_bf16 v[152:155], v[120:123], v[68:71], v[152:155]\n\t"
      "s_waitcnt lgkmcnt(9)\n\t"
      "v_mfma_f32_16x16x32_bf16 v[156:159], v[124:127], v[68:71], v[156:159]\n\t"
      "v_mfma_f32_16x16x32_bf16 v[140:143], v[124:127], v[64:67], v[140:143]\n\t"
      "v_mfma_f32_16x16x32_bf16 v[136:139], v[120:123], v[64:67], v[136:139]\n\t"
      "s_waitcnt vmcnt(1) lgkmcnt(0)\n\t"
      "s_barrier\n\t"
      "v_mfma_f32_16x16x32_bf16 v[60:63], v[52:55], v[56:59], v[60:63]\n\t"
      "v_mfma_f32_16x16x32_bf16 v[168:171], v[120:123], v[72:75], v[168:171]\n\t"
      "ds_read_b128 v[64:67], %[vXr] offset:0\n\t"
      "v_mfma_f32_16x16x32_bf16 v[172:175], v[124:127], v[72:75], v[172:175]\n\t"
      "s_add_u32 m0, s42, 32768\n\t"
      "v_lshl_add_u64 v[48:49], %[vWst], 0, v[48:49]\n\t"
      "global_load_lds_dwordx4 v[48:49], off\n\t"
      "v_mfma_f32_16x16x32_bf16 v[188:191], v[124:127], v[76:79], v[188:191]\n\t"
      "s_add_u32 m0, s42, 40960\n\t"
      "v_lshl_add_u64 v[48:49], %[vWst], 0, v[48:49]\n\t"
      "global_load_lds_dwordx4 v[48:49], off\n\t"
      "v_mfma_f32_16x16x32_bf16 v[184:187], v[120:123], v[76:79], v[184:187]\n\t"
      "v_mfma_f32_16x16x32_bf16 v[176:179], v[112:115], v[76:79], v[176:179]\n\t"
      "ds_read_b128 v[68:71], %[vXr] offset:256\n\t"
      "v_mfma_f32_16x16x32_bf16 v[180:183], v[116:119], v[76:79], v[180:183]\n\t"
      "ds_read_b64_tr_b16 v[96:97], %[vWr0] offset:0\n\t"
      "v_mfma_f32_16x16x32_bf16 v[164:167], v[116:119], v[72:75], v[164:167]\n\t"
      "ds_read_b64_tr_b16 v[98:99], %[vWr0] offset:8192\n\t"
      "v_mfma_f32_16x16x32_bf16 v[160:163], v[112:115], v[72:75], v[160:163]\n\t"
      "s_add_u32 m0, s42, 49152\n\t"
      "v_lshl_add_u64 v[48:49], %[vWst], 0, v[48:49]\n\t"
      "global_load_lds_dwordx4 v[48:49], off\n\t"
      "s_add_u32 m0, s42, 57344\n\t"
      "v_lshl_add_u64 v[48:49], %[vWst], 0, v[48:49]\n\t"
      "global_load_lds_dwordx4 v[48:49], off\n\t"
      "v_mfma_f32_16x16x32_bf16 v[192:195], v[112:115], v[80:83], v[192:195]\n\t"
      "ds_read_b128 v[72:75], %[vXr] offset:4096\n\t"
      "v_mfma_f32_16x16x32_bf16 v[196:199], v[116:119], v[80:83], v[196:199]\n\t"
      "ds_read_b64_tr_b16 v[100:101], %[vWr1] offset:0\n\t"
      "v_mfma_f32_16x16x32_bf16 v[212:215], v[116:119], v[84:87], v[212:215]\n\t"
      "ds_read_b64_tr_b16 v[102:103], %[vWr1] offset:8192\n\t"
      "v_mfma_f32_16x16x32_bf16 v[208:211], v[112:115], v[84:87], v[208:211]\n\t"
      "s_add_u32 m0, s41, 32768\n\t"
      "s_add_u32 s40, s40, 4096\n\t"
      "buffer_load_dwordx4 %[vL16], %[srdWS], s40 offen offset:0 sc1 lds\n\t"
      "buffer_load_dwordx4 %[vL16], %[srdWS], s40 offen offset:1024 sc1 lds\n\t"
      "v_mfma_f32_16x16x32_bf16 v[216:219], v[120:123], v[84:87], v[216:219]\n\t"
      "ds_read_b128 v[76:79], %[vXr] offset:4352\n\t"
      "v_mfma_f32_16x16x32_bf16 v[220:223], v[124:127], v[84:87], v[220:223]\n\t"
      "ds_read_b64_tr_b16 v[104:105], %[vWr0] offset:256\n\t"
      "v_mfma_f32_16x16x32_bf16 v[204:207], v[124:127], v[80:83], v[204:207]\n\t"
      "ds_read_b64_tr_b16 v[106:107], %[vWr0] offset:8448\n\t"
      "v_mfma_f32_16x16x32_bf16 v[200:203], v[120:123], v[80:83], v[200:203]\n\t"
      "buffer_load_dwordx4 %[vL16], %[srdWS], s40 offen offset:2048 sc1 lds\n\t"
      "buffer_load_dwordx4 %[vL16], %[srdWS], s40 offen offset:3072 sc1 lds\n\t"
      "v_mfma_f32_16x16x32_bf16 v[232:235], v[120:123], v[88:91], v[232:235]\n\t"
      "ds_read_b128 v[80:83], %[vXr] offset:8192\n\t"
      "v_mfma_f32_16x16x32_bf16 v[236:239], v[124:127], v[88:91], v[236:239]\n\t"
      "ds_read_b64_tr_b16 v[108:109], %[vWr1] offset:256\n\t"
      "v_mfma_f32_16x16x32_bf16 v[252:255], v[124:127], v[92:95], v[252:255]\n\t"
      "ds_read_b64_tr_b16 v[110:111], %[vWr1] offset:8448\n\t"
      "v_mfma_f32_16x16x32_bf16 v[248:251], v[120:123], v[92:95], v[248:251]\n\t"
      "v_mfma_f32_16x16x32_bf16 v[240:243], v[112:115], v[92:95], v[240:243]\n\t"
      "ds_read_b128 v[84:87], %[vXr] offset:8448\n\t"
      "v_mfma_f32_16x16x32_bf16 v[244:247], v[116:119], v[92:95], v[244:247]\n\t"
      "v_mfma_f32_16x16x32_bf16 v[228:231], v[116:119], v[88:91], v[228:231]\n\t"
      "v_mfma_f32_16x16x32_bf16 v[224:227], v[112:115], v[88:91], v[224:227]\n\t"
      "v_xor_b32 v50, 8192, v50\n\t"
      "s_sub_u32 s43, s43, 1\n\t"
      "s_cmp_lg_u32 s43, 0\n\t"
      "s_cbranch_scc1 P1_LOOPY_%=\n\t"
      "s_waitcnt lgkmcnt(10)\n\t"
      "v_mfma_f32_16x16x32_bf16 v[128:131], v[96:99], v[64:67], v[128:131]\n\t"
      "ds_read_b128 v[88:91], %[vXr] offset:12288\n\t"
      "s_waitcnt lgkmcnt(8)\n\t"
      "v_mfma_f32_16x16x32_bf16 v[132:135], v[100:103], v[64:67], v[132:135]\n\t"
      "ds_read_b64_tr_b16 v[112:113], %[vWr0] offset:16384\n\t"
      "v_mfma_f32_16x16x32_bf16 v[148:151], v[100:103], v[68:71], v[148:151]\n\t"
      "ds_read_b64_tr_b16 v[114:115], %[vWr0] offset:24576\n\t"
      "v_mfma_f32_16x16x32_bf16 v[144:147], v[96:99], v[68:71], v[144:147]\n\t"
      "s_mov_b32 m0, s46\n\t"
      "s_xor_b32 s46, s46, 8192\n\t"
      "buffer_load_dwordx4 %[vKVsrc], %[srdKW], s45 offen lds\n\t"
      "s_add_u32 s45, s45, 8192\n\t"
      "s_waitcnt lgkmcnt(7)\n\t"
      "v_mfma_f32_16x16x32_bf16 v[152:155], v[104:107], v[68:71], v[152:155]\n\t"
      "ds_read_b128 v[92:95], %[vXr] offset:12544\n\t"
      "s_waitcnt lgkmcnt(5)\n\t"
      "v_mfma_f32_16x16x32_bf16 v[156:159], v[108:111], v[68:71], v[156:159]\n\t"
      "ds_read_b64_tr_b16 v[116:117], %[vWr1] offset:16384\n\t"
      "v_mfma_f32_16x16x32_bf16 v[140:143], v[108:111], v[64:67], v[140:143]\n\t"
      "ds_read_b64_tr_b16 v[118:119], %[vWr1] offset:24576\n\t"
      "v_mfma_f32_16x16x32_bf16 v[136:139], v[104:107], v[64:67], v[136:139]\n\t"
      "s_add_u32 s48, %[sXd], 0\n\t"
      "buffer_load_dwordx4 v[32:35], %[vOut], %[srdWS], s48 offen sc1\n\t"
      "s_add_u32 s48, %[sXd], 4096\n\t"
      "buffer_load_dwordx4 v[36:39], %[vOut], %[srdWS], s48 offen sc1\n\t"
      "v_mfma_f32_16x16x32_bf16 v[168:171], v[104:107], v[72:75], v[168:171]\n\t"
      "ds_read_b128 v[64:67], %[vXr] offset:2048\n\t"
      "v_mfma_f32_16x16x32_bf16 v[172:175], v[108:111], v[72:75], v[172:175]\n\t"
      "ds_read_b64_tr_b16 v[120:121], %[vWr0] offset:16640\n\t"
      "v_mfma_f32_16x16x32_bf16 v[188:191], v[108:111], v[76:79], v[188:191]\n\t"
      "ds_read_b64_tr_b16 v[122:123], %[vWr0] offset:24832\n\t"
      "v_mfma_f32_16x16x32_bf16 v[184:187], v[104:107], v[76:79], v[184:187]\n\t"
      "buffer_load_dword v51, %[vPF], %[srdP], s47 offen\n\t"
      "s_add_u32 s47, s47, %[sPFs]\n\t"
      "v_mfma_f32_16x16x32_bf16 v[176:179], v[96:99], v[76:79], v[176:179]\n\t"
      "ds_read_b128 v[68:71], %[vXr] offset:2304\n\t"
      "v_mfma_f32_16x16x32_bf16 v[180:183], v[100:103], v[76:79], v[180:183]\n\t"
      "ds_read_b64_tr_b16 v[124:125], %[vWr1] offset:16640\n\t"
      "v_mfma_f32_16x16x32_bf16 v[164:167], v[100:103], v[72:75], v[164:167]\n\t"
      "ds_read_b64_tr_b16 v[126:127], %[vWr1] offset:24832\n\t"
      "v_mfma_f32_16x16x32_bf16 v[160:163], v[96:99], v[72:75], v[160:163]\n\t"
      "v_mfma_f32_16x16x32_bf16 v[192:195], v[96:99], v[80:83], v[192:195]\n\t"
      "ds_read_b128 v[72:75], %[vXr] offset:6144\n\t"
      "v_mfma_f32_16x16x32_bf16 v[196:199], v[100:103], v[80:83], v[196:199]\n\t"
      "ds_read_b64_tr_b16 v[52:53], v50 offset:0\n\t"
      "s_waitcnt lgkmcnt(14)\n\t"
      "v_mfma_f32_16x16x32_bf16 v[212:215], v[100:103], v[84:87], v[212:215]\n\t"
      "ds_read_b64_tr_b16 v[54:55], v50 offset:1024\n\t"
      "v_mfma_f32_16x16x32_bf16 v[208:211], v[96:99], v[84:87], v[208:211]\n\t"
      "v_mfma_f32_16x16x32_bf16 v[216:219], v[104:107], v[84:87], v[216:219]\n\t"
      "ds_read_b128 v[76:79], %[vXr] offset:6400\n\t"
      "v_mfma_f32_16x16x32_bf16 v[220:223], v[108:111], v[84:87], v[220:223]\n\t"
      "ds_read_b128 v[56:59], %[vXkv] offset:0\n\t"
      "v_mfma_f32_16x16x32_bf16 v[204:207], v[108:111], v[80:83], v[204:207]\n\t"
      "v_mfma_f32_16x16x32_bf16 v[200:203], v[104:107], v[80:83], v[200:203]\n\t"
      "s_waitcnt lgkmcnt(14)\n\t"
      "v_mfma_f32_16x16x32_bf16 v[232:235], v[104:107], v[88:91], v[232:235]\n\t"
      "ds_read_b128 v[80:83], %[vXr] offset:10240\n\t"
      "v_mfma_f32_16x16x32_bf16 v[236:239], v[108:111], v[88:91], v[236:239]\n\t"
      "s_waitcnt lgkmcnt(14)\n\t"
      "v_mfma_f32_16x16x32_bf16 v[252:255], v[108:111], v[92:95], v[252:255]\n\t"
      "v_mfma_f32_16x16x32_bf16 v[248:251], v[104:107], v[92:95], v[248:251]\n\t"
      "v_mfma_f32_16x16x32_bf16 v[240:243], v[96:99], v[92:95], v[240:243]\n\t"
      "ds_read_b128 v[84:87], %[vXr] offset:10496\n\t"
      "v_mfma_f32_16x16x32_bf16 v[244:247], v[100:103], v[92:95], v[244:247]\n\t"
      "v_mfma_f32_16x16x32_bf16 v[228:231], v[100:103], v[88:91], v[228:231]\n\t"
      "v_mfma_f32_16x16x32_bf16 v[224:227], v[96:99], v[88:91], v[224:227]\n\t"
      "s_waitcnt lgkmcnt(12)\n\t"
      "v_mfma_f32_16x16x32_bf16 v[128:131], v[112:115], v[64:67], v[128:131]\n\t"
      "ds_read_b128 v[88:91], %[vXr] offset:14336\n\t"
      "v_mfma_f32_16x16x32_bf16 v[132:135], v[116:119], v[64:67], v[132:135]\n\t"
      "ds_read_b128 v[92:95], %[vXr] offset:14592\n\t"
      "s_waitcnt lgkmcnt(11)\n\t"
      "v_mfma_f32_16x16x32_bf16 v[148:151], v[116:119], v[68:71], v[148:151]\n\t"
      "v_mfma_f32_16x16x32_bf16 v[144:147], v[112:115], v[68:71], v[144:147]\n\t"
      "v_mfma_f32_16x16x32_bf16 v[152:155], v[120:123], v[68:71], v[152:155]\n\t"
      "s_waitcnt lgkmcnt(9)\n\t"
      "v_mfma_f32_16x16x32_bf16 v[156:159], v[124:127], v[68:71], v[156:159]\n\t"
      "v_mfma_f32_16x16x32_bf16 v[140:143], v[124:127], v[64:67], v[140:143]\n\t"
      "v_mfma_f32_16x16x32_bf16 v[136:139], v[120:123], v[64:67], v[136:139]\n\t"
      "s_waitcnt vmcnt(1) lgkmcnt(0)\n\t"
      "s_barrier\n\t"
      "s_add_u32 s48, %[sXd], 8192\n\t"
      "buffer_load_dwordx4 v[40:43], %[vOut], %[srdWS], s48 offen sc1\n\t"
      "s_add_u32 s48, %[sXd], 12288\n\t"
      "buffer_load_dwordx4 v[44:47], %[vOut], %[srdWS], s48 offen sc1\n\t"
      "v_mfma_f32_16x16x32_bf16 v[60:63], v[52:55], v[56:59], v[60:63]\n\t"
      "v_mfma_f32_16x16x32_bf16 v[168:171], v[120:123], v[72:75], v[168:171]\n\t"
      "ds_read_b64_tr_b16 v[52:53], %[vKVr] offset:0\n\t"
      "v_mfma_f32_16x16x32_bf16 v[172:175], v[124:127], v[72:75], v[172:175]\n\t"
      "ds_read_b64_tr_b16 v[54:55], %[vKVr] offset:1024\n\t"
      "v_mfma_f32_16x16x32_bf16 v[188:191], v[124:127], v[76:79], v[188:191]\n\t"
      "ds_read_b128 v[64:67], %[vXr] offset:32768\n\t"
      "v_mfma_f32_16x16x32_bf16 v[184:187], v[120:123], v[76:79], v[184:187]\n\t"
      "s_add_u32 m0, s42, 0\n\t"
      "v_lshl_add_u64 v[48:49], %[vWst], 0, v[48:49]\n\t"
      "global_load_lds_dwordx4 v[48:49], off\n\t"
      "s_add_u32 m0, s42, 8192\n\t"
      "v_lshl_add_u64 v[48:49], %[vWst], 0, v[48:49]\n\t"
      "global_load_lds_dwordx4 v[48:49], off\n\t"
      "v_mfma_f32_16x16x32_bf16 v[176:179], v[112:115], v[76:79], v[176:179]\n\t"
      "ds_read_b128 v[68:71], %[vXr] offset:33024\n\t"
      "v_mfma_f32_16x16x32_bf16 v[180:183], v[116:119], v[76:79], v[180:183]\n\t"
      "ds_read_b64_tr_b16 v[96:97], %[vWr0] offset:32768\n\t"
      "v_mfma_f32_16x16x32_bf16 v[164:167], v[116:119], v[72:75], v[164:167]\n\t"
      "ds_read_b64_tr_b16 v[98:99], %[vWr0] offset:40960\n\t"
      "v_mfma_f32_16x16x32_bf16 v[160:163], v[112:115], v[72:75], v[160:163]\n\t"
      "s_add_u32 m0, s42, 16384\n\t"
      "v_lshl_add_u64 v[48:49], %[vWst], 0, v[48:49]\n\t"
      "global_load_lds_dwordx4 v[48:49], off\n\t"
      "s_add_u32 m0, s42, 24576\n\t"
      "v_lshl_add_u64 v[48:49], %[vWst], 0, v[48:49]\n\t"
      "global_load_lds_dwordx4 v[48:49], off\n\t"
      "s_waitcnt lgkmcnt(4)\n\t"
      "v_mfma_f32_16x16x32_bf16 v[60:63], v[52:55], v[32:35], v[60:63]\n\t"
      "v_mfma_f32_16x16x32_bf16 v[192:195], v[112:115], v[80:83], v[192:195]\n\t"
      "ds_read_b64_tr_b16 v[52:53], %[vKVr] offset:4096\n\t"
      "v_mfma_f32_16x16x32_bf16 v[196:199], v[116:119], v[80:83], v[196:199]\n\t"
      "ds_read_b64_tr_b16 v[54:55], %[vKVr] offset:5120\n\t"
      "v_mfma_f32_16x16x32_bf16 v[212:215], v[116:119], v[84:87], v[212:215]\n\t"
      "ds_read_b128 v[72:75], %[vXr] offset:36864\n\t"
      "v_mfma_f32_16x16x32_bf16 v[208:211], v[112:115], v[84:87], v[208:211]\n\t"
      "ds_read_b64_tr_b16 v[100:101], %[vWr1] offset:32768\n\t"
      "ds_read_b64_tr_b16 v[102:103], %[vWr1] offset:40960\n\t"
      "s_add_u32 m0, s41, 0\n\t"
      "s_add_u32 s40, s40, 4096\n\t"
      "buffer_load_dwordx4 %[vL16], %[srdWS], s40 offen offset:0 sc1 lds\n\t"
      "buffer_load_dwordx4 %[vL16], %[srdWS], s40 offen offset:1024 sc1 lds\n\t"
      "v_mfma_f32_16x16x32_bf16 v[216:219], v[120:123], v[84:87], v[216:219]\n\t"
      "ds_read_b128 v[76:79], %[vXr] offset:37120\n\t"
      "v_mfma_f32_16x16x32_bf16 v[220:223], v[124:127], v[84:87], v[220:223]\n\t"
      "ds_read_b64_tr_b16 v[104:105], %[vWr0] offset:33024\n\t"
      "v_mfma_f32_16x16x32_bf16 v[204:207], v[124:127], v[80:83], v[204:207]\n\t"
      "ds_read_b64_tr_b16 v[106:107], %[vWr0] offset:41216\n\t"
      "v_mfma_f32_16x16x32_bf16 v[200:203], v[120:123], v[80:83], v[200:203]\n\t"
      "buffer_load_dwordx4 %[vL16], %[srdWS], s40 offen offset:2048 sc1 lds\n\t"
      "buffer_load_dwordx4 %[vL16], %[srdWS], s40 offen offset:3072 sc1 lds\n\t"
      "s_waitcnt lgkmcnt(6)\n\t"
      "v_mfma_f32_16x16x32_bf16 v[60:63], v[52:55], v[36:39], v[60:63]\n\t"
      "v_mfma_f32_16x16x32_bf16 v[232:235], v[120:123], v[88:91], v[232:235]\n\t"
      "ds_read_b128 v[80:83], %[vXr] offset:40960\n\t"
      "v_mfma_f32_16x16x32_bf16 v[236:239], v[124:127], v[88:91], v[236:239]\n\t"
      "ds_read_b64_tr_b16 v[108:109], %[vWr1] offset:33024\n\t"
      "v_mfma_f32_16x16x32_bf16 v[252:255], v[124:127], v[92:95], v[252:255]\n\t"
      "ds_read_b64_tr_b16 v[110:111], %[vWr1] offset:41216\n\t"
      "v_mfma_f32_16x16x32_bf16 v[248:251], v[120:123], v[92:95], v[248:251]\n\t"
      "v_mfma_f32_16x16x32_bf16 v[240:243], v[112:115], v[92:95], v[240:243]\n\t"
      "ds_read_b128 v[84:87], %[vXr] offset:41216\n\t"
      "v_mfma_f32_16x16x32_bf16 v[244:247], v[116:119], v[92:95], v[244:247]\n\t"
      "v_mfma_f32_16x16x32_bf16 v[228:231], v[116:119], v[88:91], v[228:231]\n\t"
      "v_mfma_f32_16x16x32_bf16 v[224:227], v[112:115], v[88:91], v[224:227]\n\t"
      "v_mfma_f32_16x16x32_bf16 v[128:131], v[96:99], v[64:67], v[128:131]\n\t"
      "ds_read_b64_tr_b16 v[52:53], %[vKVr] offset:12288\n\t"
      "s_waitcnt lgkmcnt(8)\n\t"
      "v_mfma_f32_16x16x32_bf16 v[132:135], v[100:103], v[64:67], v[132:135]\n\t"
      "ds_read_b64_tr_b16 v[54:55], %[vKVr] offset:13312\n\t"
      "v_mfma_f32_16x16x32_bf16 v[148:151], v[100:103], v[68:71], v[148:151]\n\t"
      "ds_read_b128 v[56:59], %[vXkv] offset:32768\n\t"
      "v_mfma_f32_16x16x32_bf16 v[144:147], v[96:99], v[68:71], v[144:147]\n\t"
      "ds_read_b128 v[88:91], %[vXr] offset:45056\n\t"
      "ds_read_b64_tr_b16 v[112:113], %[vWr0] offset:49152\n\t"
      "ds_read_b64_tr_b16 v[114:115], %[vWr0] offset:57344\n\t"
      "s_waitcnt lgkmcnt(10)\n\t"
      "v_mfma_f32_16x16x32_bf16 v[152:155], v[104:107], v[68:71], v[152:155]\n\t"
      "ds_read_b128 v[92:95], %[vXr] offset:45312\n\t"
      "s_waitcnt lgkmcnt(8)\n\t"
      "v_mfma_f32_16x16x32_bf16 v[156:159], v[108:111], v[68:71], v[156:159]\n\t"
      "ds_read_b64_tr_b16 v[116:117], %[vWr1] offset:49152\n\t"
      "v_mfma_f32_16x16x32_bf16 v[140:143], v[108:111], v[64:67], v[140:143]\n\t"
      "ds_read_b64_tr_b16 v[118:119], %[vWr1] offset:57344\n\t"
      "v_mfma_f32_16x16x32_bf16 v[136:139], v[104:107], v[64:67], v[136:139]\n\t"
      "s_waitcnt lgkmcnt(6)\n\t"
      "v_mfma_f32_16x16x32_bf16 v[60:63], v[52:55], v[56:59], v[60:63]\n\t"
      "v_mfma_f32_16x16x32_bf16 v[168:171], v[104:107], v[72:75], v[168:171]\n\t"
      "ds_read_b64_tr_b16 v[52:53], %[vKVr] offset:16384\n\t"
      "v_mfma_f32_16x16x32_bf16 v[172:175], v[108:111], v[72:75], v[172:175]\n\t"
      "ds_read_b64_tr_b16 v[54:55], %[vKVr] offset:17408\n\t"
      "v_mfma_f32_16x16x32_bf16 v[188:191], v[108:111], v[76:79], v[188:191]\n\t"
      "ds_read_b128 v[64:67], %[vXr] offset:34816\n\t"
      "v_mfma_f32_16x16x32_bf16 v[184:187], v[104:107], v[76:79], v[184:187]\n\t"
      "ds_read_b64_tr_b16 v[120:121], %[vWr0] offset:49408\n\t"
      "ds_read_b64_tr_b16 v[122:123], %[vWr0] offset:57600\n\t"
      "buffer_load_dword v51, %[vPF], %[srdP], s47 offen\n\t"
      "s_add_u32 s47, s47, %[sPFs]\n\t"
      "v_mfma_f32_16x16x32_bf16 v[176:179], v[96:99], v[76:79], v[176:179]\n\t"
      "ds_read_b128 v[68:71], %[vXr] offset:35072\n\t"
      "v_mfma_f32_16x16x32_bf16 v[180:183], v[100:103], v[76:79], v[180:183]\n\t"
      "ds_read_b64_tr_b16 v[124:125], %[vWr1] offset:49408\n\t"
      "v_mfma_f32_16x16x32_bf16 v[164:167], v[100:103], v[72:75], v[164:167]\n\t"
      "ds_read_b64_tr_b16 v[126:127], %[vWr1] offset:57600\n\t"
      "v_mfma_f32_16x16x32_bf16 v[160:163], v[96:99], v[72:75], v[160:163]\n\t"
      "s_waitcnt vmcnt(9)\n\t"
      "s_waitcnt lgkmcnt(6)\n\t"
      "v_mfma_f32_16x16x32_bf16 v[60:63], v[52:55], v[40:43], v[60:63]\n\t"
      "v_mfma_f32_16x16x32_bf16 v[192:195], v[96:99], v[80:83], v[192:195]\n\t"
      "ds_read_b64_tr_b16 v[52:53], %[vKVr] offset:20480\n\t"
      "v_mfma_f32_16x16x32_bf16 v[196:199], v[100:103], v[80:83], v[196:199]\n\t"
      "ds_read_b64_tr_b16 v[54:55], %[vKVr] offset:21504\n\t"
      "v_mfma_f32_16x16x32_bf16 v[212:215], v[100:103], v[84:87], v[212:215]\n\t"
      "ds_read_b128 v[72:75], %[vXr] offset:38912\n\t"
      "v_mfma_f32_16x16x32_bf16 v[208:211], v[96:99], v[84:87], v[208:211]\n\t"
      "v_mfma_f32_16x16x32_bf16 v[216:219], v[104:107], v[84:87], v[216:219]\n\t"
      "ds_read_b128 v[76:79], %[vXr] offset:39168\n\t"
      "v_mfma_f32_16x16x32_bf16 v[220:223], v[108:111], v[84:87], v[220:223]\n\t"
      "v_mfma_f32_16x16x32_bf16 v[204:207], v[108:111], v[80:83], v[204:207]\n\t"
      "v_mfma_f32_16x16x32_bf16 v[200:203], v[104:107], v[80:83], v[200:203]\n\t"
      "s_waitcnt lgkmcnt(2)\n\t"
      "v_mfma_f32_16x16x32_bf16 v[60:63], v[52:55], v[44:47], v[60:63]\n\t"
      "v_mfma_f32_16x16x32_bf16 v[232:235], v[104:107], v[88:91], v[232:235]\n\t"
      "ds_read_b128 v[80:83], %[vXr] offset:43008\n\t"
      "v_mfma_f32_16x16x32_bf16 v[236:239], v[108:111], v[88:91], v[236:239]\n\t"
      "v_mfma_f32_16x16x32_bf16 v[252:255], v[108:111], v[92:95], v[252:255]\n\t"
      "v_mfma_f32_16x16x32_bf16 v[248:251], v[104:107], v[92:95], v[248:251]\n\t"
      "v_mfma_f32_16x16x32_bf16 v[240:243], v[96:99], v[92:95], v[240:243]\n\t"
      "ds_read_b128 v[84:87], %[vXr] offset:43264\n\t"
      "v_mfma_f32_16x16x32_bf16 v[244:247], v[100:103], v[92:95], v[244:247]\n\t"
      "v_mfma_f32_16x16x32_bf16 v[228:231], v[100:103], v[88:91], v[228:231]\n\t"
      "v_mfma_f32_16x16x32_bf16 v[224:227], v[96:99], v[88:91], v[224:227]\n\t"
      "v_mfma_f32_16x16x32_bf16 v[128:131], v[112:115], v[64:67], v[128:131]\n\t"
      "ds_read_b128 v[88:91], %[vXr] offset:47104\n\t"
      "v_mfma_f32_16x16x32_bf16 v[132:135], v[116:119], v[64:67], v[132:135]\n\t"
      "ds_read_b128 v[92:95], %[vXr] offset:47360\n\t"
      "v_mfma_f32_16x16x32_bf16 v[148:151], v[116:119], v[68:71], v[148:151]\n\t"
      "v_mov_b32 v50, %[vKVw]\n\t"
      "ds_write_b32 v50, v60\n\t"
      "v_add_u32 v50, %[sKVst], v50\n\t"
      "ds_write_b32 v50, v61\n\t"
      "v_add_u32 v50, %[sKVst], v50\n\t"
      "ds_write_b32 v50, v62\n\t"
      "v_add_u32 v50, %[sKVst], v50\n\t"
      "ds_write_b32 v50, v63\n\t"
      "v_mfma_f32_16x16x32_bf16 v[144:147], v[112:115], v[68:71], v[144:147]\n\t"
      "v_mfma_f32_16x16x32_bf16 v[152:155], v[120:123], v[68:71], v[152:155]\n\t"
      "v_mfma_f32_16x16x32_bf16 v[156:159], v[124:127], v[68:71], v[156:159]\n\t"
      "v_mfma_f32_16x16x32_bf16 v[140:143], v[124:127], v[64:67], v[140:143]\n\t"
      "v_mfma_f32_16x16x32_bf16 v[136:139], v[120:123], v[64:67], v[136:139]\n\t"
      "s_waitcnt vmcnt(1) lgkmcnt(0)\n\t"
      "s_barrier\n\t"
      "v_mfma_f32_16x16x32_bf16 v[168:171], v[120:123], v[72:75], v[168:171]\n\t"
      "ds_read_b128 v[64:67], %[vXr] offset:0\n\t"
      "v_mfma_f32_16x16x32_bf16 v[172:175], v[124:127], v[72:75], v[172:175]\n\t"
      "s_add_u32 m0, s42, 32768\n\t"
      "v_lshl_add_u64 v[48:49], %[vWst], 0, v[48:49]\n\t"
      "global_load_lds_dwordx4 v[48:49], off\n\t"
      "v_mfma_f32_16x16x32_bf16 v[188:191], v[124:127], v[76:79], v[188:191]\n\t"
      "s_add_u32 m0, s42, 40960\n\t"
      "v_lshl_add_u64 v[48:49], %[vWst], 0, v[48:49]\n\t"
      "global_load_lds_dwordx4 v[48:49], off\n\t"
      "v_mfma_f32_16x16x32_bf16 v[184:187], v[120:123], v[76:79], v[184:187]\n\t"
      "v_mfma_f32_16x16x32_bf16 v[176:179], v[112:115], v[76:79], v[176:179]\n\t"
      "ds_read_b128 v[68:71], %[vXr] offset:256\n\t"
      "v_mfma_f32_16x16x32_bf16 v[180:183], v[116:119], v[76:79], v[180:183]\n\t"
      "ds_read_b64_tr_b16 v[96:97], %[vWr0] offset:0\n\t"
      "v_mfma_f32_16x16x32_bf16 v[164:167], v[116:119], v[72:75], v[164:167]\n\t"
      "ds_read_b64_tr_b16 v[98:99], %[vWr0] offset:8192\n\t"
      "v_mfma_f32_16x16x32_bf16 v[160:163], v[112:115], v[72:75], v[160:163]\n\t"
      "s_add_u32 m0, s42, 49152\n\t"
      "v_lshl_add_u64 v[48:49], %[vWst], 0, v[48:49]\n\t"
      "global_load_lds_dwordx4 v[48:49], off\n\t"
      "s_add_u32 m0, s42, 57344\n\t"
      "v_lshl_add_u64 v[48:49], %[vWst], 0, v[48:49]\n\t"
      "global_load_lds_dwordx4 v[48:49], off\n\t"
      "v_mfma_f32_16x16x32_bf16 v[192:195], v[112:115], v[80:83], v[192:195]\n\t"
      "ds_read_b128 v[72:75], %[vXr] offset:4096\n\t"
      "v_mfma_f32_16x16x32_bf16 v[196:199], v[116:119], v[80:83], v[196:199]\n\t"
      "ds_read_b64_tr_b16 v[100:101], %[vWr1] offset:0\n\t"
      "v_mfma_f32_16x16x32_bf16 v[212:215], v[116:119], v[84:87], v[212:215]\n\t"
      "ds_read_b64_tr_b16 v[102:103], %[vWr1] offset:8192\n\t"
      "v_mfma_f32_16x16x32_bf16 v[208:211], v[112:115], v[84:87], v[208:211]\n\t"
      "s_add_u32 m0, s41, 32768\n\t"
      "s_add_u32 s40, s40, 4096\n\t"
      "buffer_load_dwordx4 %[vL16], %[srdWS], s40 offen offset:0 sc1 lds\n\t"
      "buffer_load_dwordx4 %[vL16], %[srdWS], s40 offen offset:1024 sc1 lds\n\t"
      "v_mfma_f32_16x16x32_bf16 v[216:219], v[120:123], v[84:87], v[216:219]\n\t"
      "ds_read_b128 v[76:79], %[vXr] offset:4352\n\t"
      "v_mfma_f32_16x16x32_bf16 v[220:223], v[124:127], v[84:87], v[220:223]\n\t"
      "ds_read_b64_tr_b16 v[104:105], %[vWr0] offset:256\n\t"
      "v_mfma_f32_16x16x32_bf16 v[204:207], v[124:127], v[80:83], v[204:207]\n\t"
      "ds_read_b64_tr_b16 v[106:107], %[vWr0] offset:8448\n\t"
      "v_mfma_f32_16x16x32_bf16 v[200:203], v[120:123], v[80:83], v[200:203]\n\t"
      "buffer_load_dwordx4 %[vL16], %[srdWS], s40 offen offset:2048 sc1 lds\n\t"
      "buffer_load_dwordx4 %[vL16], %[srdWS], s40 offen offset:3072 sc1 lds\n\t"
      "v_mfma_f32_16x16x32_bf16 v[232:235], v[120:123], v[88:91], v[232:235]\n\t"
      "ds_read_b128 v[80:83], %[vXr] offset:8192\n\t"
      "v_mfma_f32_16x16x32_bf16 v[236:239], v[124:127], v[88:91], v[236:239]\n\t"
      "ds_read_b64_tr_b16 v[108:109], %[vWr1] offset:256\n\t"
      "v_mfma_f32_16x16x32_bf16 v[252:255], v[124:127], v[92:95], v[252:255]\n\t"
      "ds_read_b64_tr_b16 v[110:111], %[vWr1] offset:8448\n\t"
      "v_mfma_f32_16x16x32_bf16 v[248:251], v[120:123], v[92:95], v[248:251]\n\t"
      "v_mfma_f32_16x16x32_bf16 v[240:243], v[112:115], v[92:95], v[240:243]\n\t"
      "ds_read_b128 v[84:87], %[vXr] offset:8448\n\t"
      "v_mfma_f32_16x16x32_bf16 v[244:247], v[116:119], v[92:95], v[244:247]\n\t"
      "v_mfma_f32_16x16x32_bf16 v[228:231], v[116:119], v[88:91], v[228:231]\n\t"
      "v_mfma_f32_16x16x32_bf16 v[224:227], v[112:115], v[88:91], v[224:227]\n\t"
      "s_waitcnt lgkmcnt(10)\n\t"
      "v_mfma_f32_16x16x32_bf16 v[128:131], v[96:99], v[64:67], v[128:131]\n\t"
      "ds_read_b128 v[88:91], %[vXr] offset:12288\n\t"
      "s_waitcnt lgkmcnt(8)\n\t"
      "v_mfma_f32_16x16x32_bf16 v[132:135], v[100:103], v[64:67], v[132:135]\n\t"
      "ds_read_b64_tr_b16 v[112:113], %[vWr0] offset:16384\n\t"
      "v_mfma_f32_16x16x32_bf16 v[148:151], v[100:103], v[68:71], v[148:151]\n\t"
      "ds_read_b64_tr_b16 v[114:115], %[vWr0] offset:24576\n\t"
      "v_mfma_f32_16x16x32_bf16 v[144:147], v[96:99], v[68:71], v[144:147]\n\t"
      "s_waitcnt lgkmcnt(7)\n\t"
      "v_mfma_f32_16x16x32_bf16 v[152:155], v[104:107], v[68:71], v[152:155]\n\t"
      "ds_read_b128 v[92:95], %[vXr] offset:12544\n\t"
      "s_waitcnt lgkmcnt(5)\n\t"
      "v_mfma_f32_16x16x32_bf16 v[156:159], v[108:111], v[68:71], v[156:159]\n\t"
      "ds_read_b64_tr_b16 v[116:117], %[vWr1] offset:16384\n\t"
      "v_mfma_f32_16x16x32_bf16 v[140:143], v[108:111], v[64:67], v[140:143]\n\t"
      "ds_read_b64_tr_b16 v[118:119], %[vWr1] offset:24576\n\t"
      "v_mfma_f32_16x16x32_bf16 v[136:139], v[104:107], v[64:67], v[136:139]\n\t"
      "v_mfma_f32_16x16x32_bf16 v[168:171], v[104:107], v[72:75], v[168:171]\n\t"
      "ds_read_b128 v[64:67], %[vXr] offset:2048\n\t"
      "v_mfma_f32_16x16x32_bf16 v[172:175], v[108:111], v[72:75], v[172:175]\n\t"
      "ds_read_b64_tr_b16 v[120:121], %[vWr0] offset:16640\n\t"
      "v_mfma_f32_16x16x32_bf16 v[188:191], v[108:111], v[76:79], v[188:191]\n\t"
      "ds_read_b64_tr_b16 v[122:123], %[vWr0] offset:24832\n\t"
      "v_mfma_f32_16x16x32_bf16 v[184:187], v[104:107], v[76:79], v[184:187]\n\t"
      "buffer_load_dword v51, %[vPF], %[srdP], s47 offen\n\t"
      "s_add_u32 s47, s47, %[sPFs]\n\t"
      "v_mfma_f32_16x16x32_bf16 v[176:179], v[96:99], v[76:79], v[176:179]\n\t"
      "ds_read_b128 v[68:71], %[vXr] offset:2304\n\t"
      "v_mfma_f32_16x16x32_bf16 v[180:183], v[100:103], v[76:79], v[180:183]\n\t"
      "ds_read_b64_tr_b16 v[124:125], %[vWr1] offset:16640\n\t"
      "v_mfma_f32_16x16x32_bf16 v[164:167], v[100:103], v[72:75], v[164:167]\n\t"
      "ds_read_b64_tr_b16 v[126:127], %[vWr1] offset:24832\n\t"
      "v_mfma_f32_16x16x32_bf16 v[160:163], v[96:99], v[72:75], v[160:163]\n\t"
      "v_mfma_f32_16x16x32_bf16 v[192:195], v[96:99], v[80:83], v[192:195]\n\t"
      "ds_read_b128 v[72:75], %[vXr] offset:6144\n\t"
      "v_mfma_f32_16x16x32_bf16 v[196:199], v[100:103], v[80:83], v[196:199]\n\t"
      "s_waitcnt lgkmcnt(13)\n\t"
      "v_mfma_f32_16x16x32_bf16 v[212:215], v[100:103], v[84:87], v[212:215]\n\t"
      "v_mfma_f32_16x16x32_bf16 v[208:211], v[96:99], v[84:87], v[208:211]\n\t"
      "v_mfma_f32_16x16x32_bf16 v[216:219], v[104:107], v[84:87], v[216:219]\n\t"
      "ds_read_b128 v[76:79], %[vXr] offset:6400\n\t"
      "v_mfma_f32_16x16x32_bf16 v[220:223], v[108:111], v[84:87], v[220:223]\n\t"
      "v_mfma_f32_16x16x32_bf16 v[204:207], v[108:111], v[80:83], v[204:207]\n\t"
      "v_mfma_f32_16x16x32_bf16 v[200:203], v[104:107], v[80:83], v[200:203]\n\t"
      "s_waitcnt lgkmcnt(13)\n\t"
      "v_mfma_f32_16x16x32_bf16 v[232:235], v[104:107], v[88:91], v[232:235]\n\t"
      "ds_read_b128 v[80:83], %[vXr] offset:10240\n\t"
      "v_mfma_f32_16x16x32_bf16 v[236:239], v[108:111], v[88:91], v[236:239]\n\t"
      "s_waitcnt lgkmcnt(11)\n\t"
      "v_mfma_f32_16x16x32_bf16 v[252:255], v[108:111], v[92:95], v[252:255]\n\t"
      "v_mfma_f32_16x16x32_bf16 v[248:251], v[104:107], v[92:95], v[248:251]\n\t"
      "v_mfma_f32_16x16x32_bf16 v[240:243], v[96:99], v[92:95], v[240:243]\n\t"
      "ds_read_b128 v[84:87], %[vXr] offset:10496\n\t"
      "v_mfma_f32_16x16x32_bf16 v[244:247], v[100:103], v[92:95], v[244:247]\n\t"
      "v_mfma_f32_16x16x32_bf16 v[228:231], v[100:103], v[88:91], v[228:231]\n\t"
      "v_mfma_f32_16x16x32_bf16 v[224:227], v[96:99], v[88:91], v[224:227]\n\t"
      "s_waitcnt lgkmcnt(9)\n\t"
      "v_mfma_f32_16x16x32_bf16 v[128:131], v[112:115], v[64:67], v[128:131]\n\t"
      "ds_read_b128 v[88:91], %[vXr] offset:14336\n\t"
      "v_mfma_f32_16x16x32_bf16 v[132:135], v[116:119], v[64:67], v[132:135]\n\t"
      "ds_read_b128 v[92:95], %[vXr] offset:14592\n\t"
      "s_waitcnt lgkmcnt(8)\n\t"
      "v_mfma_f32_16x16x32_bf16 v[148:151], v[116:119], v[68:71], v[148:151]\n\t"
      "v_mfma_f32_16x16x32_bf16 v[144:147], v[112:115], v[68:71], v[144:147]\n\t"
      "v_mfma_f32_16x16x32_bf16 v[152:155], v[120:123], v[68:71], v[152:155]\n\t"
      "s_waitcnt lgkmcnt(6)\n\t"
      "v_mfma_f32_16x16x32_bf16 v[156:159], v[124:127], v[68:71], v[156:159]\n\t"
      "v_mfma_f32_16x16x32_bf16 v[140:143], v[124:127], v[64:67], v[140:143]\n\t"
      "v_mfma_f32_16x16x32_bf16 v[136:139], v[120:123], v[64:67], v[136:139]\n\t"
      "s_waitcnt vmcnt(1) lgkmcnt(0)\n\t"
      "s_barrier\n\t"
      "v_mfma_f32_16x16x32_bf16 v[168:171], v[120:123], v[72:75], v[168:171]\n\t"
      "ds_read_b128 v[64:67], %[vXr] offset:32768\n\t"
      "v_mfma_f32_16x16x32_bf16 v[172:175], v[124:127], v[72:75], v[172:175]\n\t"
      "s_add_u32 m0, s42, 0\n\t"
      "v_lshl_add_u64 v[48:49], %[vWst], 0, v[48:49]\n\t"
      "global_load_lds_dwordx4 v[48:49], off\n\t"
      "v_mfma_f32_16x16x32_bf16 v[188:191], v[124:127], v[76:79], v[188:191]\n\t"
      "s_add_u32 m0, s42, 8192\n\t"
      "v_lshl_add_u64 v[48:49], %[vWst], 0, v[48:49]\n\t"
      "global_load_lds_dwordx4 v[48:49], off\n\t"
      "v_mfma_f32_16x16x32_bf16 v[184:187], v[120:123], v[76:79], v[184:187]\n\t"
      "v_mfma_f32_16x16x32_bf16 v[176:179], v[112:115], v[76:79], v[176:179]\n\t"
      "ds_read_b128 v[68:71], %[vXr] offset:33024\n\t"
      "v_mfma_f32_16x16x32_bf16 v[180:183], v[116:119], v[76:79], v[180:183]\n\t"
      "ds_read_b64_tr_b16 v[96:97], %[vWr0] offset:32768\n\t"
      "v_mfma_f32_16x16x32_bf16 v[164:167], v[116:119], v[72:75], v[164:167]\n\t"
      "ds_read_b64_tr_b16 v[98:99], %[vWr0] offset:40960\n\t"
      "v_mfma_f32_16x16x32_bf16 v[160:163], v[112:115], v[72:75], v[160:163]\n\t"
      "s_add_u32 m0, s42, 16384\n\t"
      "v_lshl_add_u64 v[48:49], %[vWst], 0, v[48:49]\n\t"
      "global_load_lds_dwordx4 v[48:49], off\n\t"
      "s_add_u32 m0, s42, 24576\n\t"
      "v_lshl_add_u64 v[48:49], %[vWst], 0, v[48:49]\n\t"
      "global_load_lds_dwordx4 v[48:49], off\n\t"
      "v_mfma_f32_16x16x32_bf16 v[192:195], v[112:115], v[80:83], v[192:195]\n\t"
      "ds_read_b128 v[72:75], %[vXr] offset:36864\n\t"
      "v_mfma_f32_16x16x32_bf16 v[196:199], v[116:119], v[80:83], v[196:199]\n\t"
      "ds_read_b64_tr_b16 v[100:101], %[vWr1] offset:32768\n\t"
      "v_mfma_f32_16x16x32_bf16 v[212:215], v[116:119], v[84:87], v[212:215]\n\t"
      "ds_read_b64_tr_b16 v[102:103], %[vWr1] offset:40960\n\t"
      "v_mfma_f32_16x16x32_bf16 v[208:211], v[112:115], v[84:87], v[208:211]\n\t"
      "s_add_u32 m0, s41, 0\n\t"
      "s_add_u32 s40, s40, 4096\n\t"
      "buffer_load_dwordx4 %[vL16], %[srdWS], s40 offen offset:0 sc1 lds\n\t"
      "buffer_load_dwordx4 %[vL16], %[srdWS], s40 offen offset:1024 sc1 lds\n\t"
      "v_mfma_f32_16x16x32_bf16 v[216:219], v[120:123], v[84:87], v[216:219]\n\t"
      "ds_read_b128 v[76:79], %[vXr] offset:37120\n\t"
      "v_mfma_f32_16x16x32_bf16 v[220:223], v[124:127], v[84:87], v[220:223]\n\t"
      "ds_read_b64_tr_b16 v[104:105], %[vWr0] offset:33024\n\t"
      "v_mfma_f32_16x16x32_bf16 v[204:207], v[124:127], v[80:83], v[204:207]\n\t"
      "ds_read_b64_tr_b16 v[106:107], %[vWr0] offset:41216\n\t"
      "v_mfma_f32_16x16x32_bf16 v[200:203], v[120:123], v[80:83], v[200:203]\n\t"
      "buffer_load_dwordx4 %[vL16], %[srdWS], s40 offen offset:2048 sc1 lds\n\t"
      "buffer_load_dwordx4 %[vL16], %[srdWS], s40 offen offset:3072 sc1 lds\n\t"
      "v_mfma_f32_16x16x32_bf16 v[232:235], v[120:123], v[88:91], v[232:235]\n\t"
      "ds_read_b128 v[80:83], %[vXr] offset:40960\n\t"
      "v_mfma_f32_16x16x32_bf16 v[236:239], v[124:127], v[88:91], v[236:239]\n\t"
      "ds_read_b64_tr_b16 v[108:109], %[vWr1] offset:33024\n\t"
      "v_mfma_f32_16x16x32_bf16 v[252:255], v[124:127], v[92:95], v[252:255]\n\t"
      "ds_read_b64_tr_b16 v[110:111], %[vWr1] offset:41216\n\t"
      "v_mfma_f32_16x16x32_bf16 v[248:251], v[120:123], v[92:95], v[248:251]\n\t"
      "v_mfma_f32_16x16x32_bf16 v[240:243], v[112:115], v[92:95], v[240:243]\n\t"
      "ds_read_b128 v[84:87], %[vXr] offset:41216\n\t"
      "v_mfma_f32_16x16x32_bf16 v[244:247], v[116:119], v[92:95], v[244:247]\n\t"
      "v_mfma_f32_16x16x32_bf16 v[228:231], v[116:119], v[88:91], v[228:231]\n\t"
      "v_mfma_f32_16x16x32_bf16 v[224:227], v[112:115], v[88:91], v[224:227]\n\t"
      "s_waitcnt lgkmcnt(10)\n\t"
      "v_mfma_f32_16x16x32_bf16 v[128:131], v[96:99], v[64:67], v[128:131]\n\t"
      "ds_read_b128 v[88:91], %[vXr] offset:45056\n\t"
      "s_waitcnt lgkmcnt(8)\n\t"
      "v_mfma_f32_16x16x32_bf16 v[132:135], v[100:103], v[64:67], v[132:135]\n\t"
      "ds_read_b64_tr_b16 v[112:113], %[vWr0] offset:49152\n\t"
      "v_mfma_f32_16x16x32_bf16 v[148:151], v[100:103], v[68:71], v[148:151]\n\t"
      "ds_read_b64_tr_b16 v[114:115], %[vWr0] offset:57344\n\t"
      "v_mfma_f32_16x16x32_bf16 v[144:147], v[96:99], v[68:71], v[144:147]\n\t"
      "s_waitcnt lgkmcnt(7)\n\t"
      "v_mfma_f32_16x16x32_bf16 v[152:155], v[104:107], v[68:71], v[152:155]\n\t"
      "ds_read_b128 v[92:95], %[vXr] offset:45312\n\t"
      "s_waitcnt lgkmcnt(5)\n\t"
      "v_mfma_f32_16x16x32_bf16 v[156:159], v[108:111], v[68:71], v[156:159]\n\t"
      "ds_read_b64_tr_b16 v[116:117], %[vWr1] offset:49152\n\t"
      "v_mfma_f32_16x16x32_bf16 v[140:143], v[108:111], v[64:67], v[140:143]\n\t"
      "ds_read_b64_tr_b16 v[118:119], %[vWr1] offset:57344\n\t"
      "v_mfma_f32_16x16x32_bf16 v[136:139], v[104:107], v[64:67], v[136:139]\n\t"
      "v_mfma_f32_16x16x32_bf16 v[168:171], v[104:107], v[72:75], v[168:171]\n\t"
      "ds_read_b128 v[64:67], %[vXr] offset:34816\n\t"
      "v_mfma_f32_16x16x32_bf16 v[172:175], v[108:111], v[72:75], v[172:175]\n\t"
      "ds_read_b64_tr_b16 v[120:121], %[vWr0] offset:49408\n\t"
      "v_mfma_f32_16x16x32_bf16 v[188:191], v[108:111], v[76:79], v[188:191]\n\t"
      "ds_read_b64_tr_b16 v[122:123], %[vWr0] offset:57600\n\t"
      "v_mfma_f32_16x16x32_bf16 v[184:187], v[104:107], v[76:79], v[184:187]\n\t"
      "buffer_load_dword v51, %[vPF], %[srdP], s47 offen\n\t"
      "s_add_u32 s47, s47, %[sPFs]\n\t"
      "v_lshlrev_b32 v30, 3, %[vL16]\n\t"
      "buffer_load_dword v51, v30, %[srdWO], %[sPFo] offen\n\t"
      "v_mfma_f32_16x16x32_bf16 v[176:179], v[96:99], v[76:79], v[176:179]\n\t"
      "ds_read_b128 v[68:71], %[vXr] offset:35072\n\t"
      "v_mfma_f32_16x16x32_bf16 v[180:183], v[100:103], v[76:79], v[180:183]\n\t"
      "ds_read_b64_tr_b16 v[124:125], %[vWr1] offset:49408\n\t"
      "v_mfma_f32_16x16x32_bf16 v[164:167], v[100:103], v[72:75], v[164:167]\n\t"
      "ds_read_b64_tr_b16 v[126:127], %[vWr1] offset:57600\n\t"
      "v_mfma_f32_16x16x32_bf16 v[160:163], v[96:99], v[72:75], v[160:163]\n\t"
      "v_mfma_f32_16x16x32_bf16 v[192:195], v[96:99], v[80:83], v[192:195]\n\t"
      "ds_read_b128 v[72:75], %[vXr] offset:38912\n\t"
      "v_mfma_f32_16x16x32_bf16 v[196:199], v[100:103], v[80:83], v[196:199]\n\t"
      "s_waitcnt lgkmcnt(13)\n\t"
      "v_mfma_f32_16x16x32_bf16 v[212:215], v[100:103], v[84:87], v[212:215]\n\t"
      "v_mfma_f32_16x16x32_bf16 v[208:211], v[96:99], v[84:87], v[208:211]\n\t"
      "v_mfma_f32_16x16x32_bf16 v[216:219], v[104:107], v[84:87], v[216:219]\n\t"
      "ds_read_b128 v[76:79], %[vXr] offset:39168\n\t"
      "v_mfma_f32_16x16x32_bf16 v[220:223], v[108:111], v[84:87], v[220:223]\n\t"
      "v_mfma_f32_16x16x32_bf16 v[204:207], v[108:111], v[80:83], v[204:207]\n\t"
      "v_mfma_f32_16x16x32_bf16 v[200:203], v[104:107], v[80:83], v[200:203]\n\t"
      "s_waitcnt lgkmcnt(13)\n\t"
      "v_mfma_f32_16x16x32_bf16 v[232:235], v[104:107], v[88:91], v[232:235]\n\t"
      "ds_read_b128 v[80:83], %[vXr] offset:43008\n\t"
      "v_mfma_f32_16x16x32_bf16 v[236:239], v[108:111], v[88:91], v[236:239]\n\t"
      "s_waitcnt lgkmcnt(11)\n\t"
      "v_mfma_f32_16x16x32_bf16 v[252:255], v[108:111], v[92:95], v[252:255]\n\t"
      "v_mfma_f32_16x16x32_bf16 v[248:251], v[104:107], v[92:95], v[248:251]\n\t"
      "v_mfma_f32_16x16x32_bf16 v[240:243], v[96:99], v[92:95], v[240:243]\n\t"
      "ds_read_b128 v[84:87], %[vXr] offset:43264\n\t"
      "v_mfma_f32_16x16x32_bf16 v[244:247], v[100:103], v[92:95], v[244:247]\n\t"
      "v_mfma_f32_16x16x32_bf16 v[228:231], v[100:103], v[88:91], v[228:231]\n\t"
      "v_mfma_f32_16x16x32_bf16 v[224:227], v[96:99], v[88:91], v[224:227]\n\t"
      "s_waitcnt lgkmcnt(9)\n\t"
      "v_mfma_f32_16x16x32_bf16 v[128:131], v[112:115], v[64:67], v[128:131]\n\t"
      "ds_read_b128 v[88:91], %[vXr] offset:47104\n\t"
      "v_mfma_f32_16x16x32_bf16 v[132:135], v[116:119], v[64:67], v[132:135]\n\t"
      "ds_read_b128 v[92:95], %[vXr] offset:47360\n\t"
      "s_waitcnt lgkmcnt(8)\n\t"
      "v_mfma_f32_16x16x32_bf16 v[148:151], v[116:119], v[68:71], v[148:151]\n\t"
      "v_mfma_f32_16x16x32_bf16 v[144:147], v[112:115], v[68:71], v[144:147]\n\t"
      "v_mfma_f32_16x16x32_bf16 v[152:155], v[120:123], v[68:71], v[152:155]\n\t"
      "s_waitcnt lgkmcnt(6)\n\t"
      "v_mfma_f32_16x16x32_bf16 v[156:159], v[124:127], v[68:71], v[156:159]\n\t"
      "v_mfma_f32_16x16x32_bf16 v[140:143], v[124:127], v[64:67], v[140:143]\n\t"
      "v_mfma_f32_16x16x32_bf16 v[136:139], v[120:123], v[64:67], v[136:139]\n\t"
      "s_waitcnt vmcnt(2) lgkmcnt(0)\n\t"
      "s_barrier\n\t"
      "v_mfma_f32_16x16x32_bf16 v[168:171], v[120:123], v[72:75], v[168:171]\n\t"
      "ds_read_b128 v[64:67], %[vXr] offset:0\n\t"
      "v_mfma_f32_16x16x32_bf16 v[172:175], v[124:127], v[72:75], v[172:175]\n\t"
      "s_add_u32 m0, s42, 32768\n\t"
      "v_lshl_add_u64 v[48:49], %[vWst], 0, v[48:49]\n\t"
      "global_load_lds_dwordx4 v[48:49], off\n\t"
      "v_mfma_f32_16x16x32_bf16 v[188:191], v[124:127], v[76:79], v[188:191]\n\t"
      "s_add_u32 m0, s42, 40960\n\t"
      "v_lshl_add_u64 v[48:49], %[vWst], 0, v[48:49]\n\t"
      "global_load_lds_dwordx4 v[48:49], off\n\t"
      "v_mfma_f32_16x16x32_bf16 v[184:187], v[120:123], v[76:79], v[184:187]\n\t"
      "v_mfma_f32_16x16x32_bf16 v[176:179], v[112:115], v[76:79], v[176:179]\n\t"
      "ds_read_b128 v[68:71], %[vXr] offset:256\n\t"
      "v_mfma_f32_16x16x32_bf16 v[180:183], v[116:119], v[76:79], v[180:183]\n\t"
      "ds_read_b64_tr_b16 v[96:97], %[vWr0] offset:0\n\t"
      "v_mfma_f32_16x16x32_bf16 v[164:167], v[116:119], v[72:75], v[164:167]\n\t"
      "ds_read_b64_tr_b16 v[98:99], %[vWr0] offset:8192\n\t"
      "v_mfma_f32_16x16x32_bf16 v[160:163], v[112:115], v[72:75], v[160:163]\n\t"
      "s_add_u32 m0, s42, 49152\n\t"
      "v_lshl_add_u64 v[48:49], %[vWst], 0, v[48:49]\n\t"
      "global_load_lds_dwordx4 v[48:49], off\n\t"
      "s_add_u32 m0, s42, 57344\n\t"
      "v_lshl_add_u64 v[48:49], %[vWst], 0, v[48:49]\n\t"
      "global_load_lds_dwordx4 v[48:49], off\n\t"
      "v_mfma_f32_16x16x32_bf16 v[192:195], v[112:115], v[80:83], v[192:195]\n\t"
      "ds_read_b128 v[72:75], %[vXr] offset:4096\n\t"
      "v_mfma_f32_16x16x32_bf16 v[196:199], v[116:119], v[80:83], v[196:199]\n\t"
      "ds_read_b64_tr_b16 v[100:101], %[vWr1] offset:0\n\t"
      "v_mfma_f32_16x16x32_bf16 v[212:215], v[116:119], v[84:87], v[212:215]\n\t"
      "ds_read_b64_tr_b16 v[102:103], %[vWr1] offset:8192\n\t"
      "v_mfma_f32_16x16x32_bf16 v[208:211], v[112:115], v[84:87], v[208:211]\n\t"
      "s_add_u32 m0, s41, 32768\n\t"
      "s_add_u32 s40, s40, 4096\n\t"
      "buffer_load_dwordx4 %[vL16], %[srdWS], s40 offen offset:0 sc1 lds\n\t"
      "buffer_load_dwordx4 %[vL16], %[srdWS], s40 offen offset:1024 sc1 lds\n\t"
      "v_mfma_f32_16x16x32_bf16 v[216:219], v[120:123], v[84:87], v[216:219]\n\t"
      "ds_read_b128 v[76:79], %[vXr] offset:4352\n\t"
      "v_mfma_f32_16x16x32_bf16 v[220:223], v[124:127], v[84:87], v[220:223]\n\t"
      "ds_read_b64_tr_b16 v[104:105], %[vWr0] offset:256\n\t"
      "v_mfma_f32_16x16x32_bf16 v[204:207], v[124:127], v[80:83], v[204:207]\n\t"
      "ds_read_b64_tr_b16 v[106:107], %[vWr0] offset:8448\n\t"
      "v_mfma_f32_16x16x32_bf16 v[200:203], v[120:123], v[80:83], v[200:203]\n\t"
      "buffer_load_dwordx4 %[vL16], %[srdWS], s40 offen offset:2048 sc1 lds\n\t"
      "buffer_load_dwordx4 %[vL16], %[srdWS], s40 offen offset:3072 sc1 lds\n\t"
      "v_mfma_f32_16x16x32_bf16 v[232:235], v[120:123], v[88:91], v[232:235]\n\t"
      "ds_read_b128 v[80:83], %[vXr] offset:8192\n\t"
      "v_mfma_f32_16x16x32_bf16 v[236:239], v[124:127], v[88:91], v[236:239]\n\t"
      "ds_read_b64_tr_b16 v[108:109], %[vWr1] offset:256\n\t"
      "v_mfma_f32_16x16x32_bf16 v[252:255], v[124:127], v[92:95], v[252:255]\n\t"
      "ds_read_b64_tr_b16 v[110:111], %[vWr1] offset:8448\n\t"
      "v_mfma_f32_16x16x32_bf16 v[248:251], v[120:123], v[92:95], v[248:251]\n\t"
      "v_mfma_f32_16x16x32_bf16 v[240:243], v[112:115], v[92:95], v[240:243]\n\t"
      "ds_read_b128 v[84:87], %[vXr] offset:8448\n\t"
      "v_mfma_f32_16x16x32_bf16 v[244:247], v[116:119], v[92:95], v[244:247]\n\t"
      "v_mfma_f32_16x16x32_bf16 v[228:231], v[116:119], v[88:91], v[228:231]\n\t"
      "v_mfma_f32_16x16x32_bf16 v[224:227], v[112:115], v[88:91], v[224:227]\n\t"
      "P1_T14Y_%=:\n\t"
      "s_waitcnt lgkmcnt(10)\n\t"
      "v_mfma_f32_16x16x32_bf16 v[128:131], v[96:99], v[64:67], v[128:131]\n\t"
      "ds_read_b128 v[88:91], %[vXr] offset:12288\n\t"
      "s_waitcnt lgkmcnt(8)\n\t"
      "v_mfma_f32_16x16x32_bf16 v[132:135], v[100:103], v[64:67], v[132:135]\n\t"
      "ds_read_b64_tr_b16 v[112:113], %[vWr0] offset:16384\n\t"
      "v_mfma_f32_16x16x32_bf16 v[148:151], v[100:103], v[68:71], v[148:151]\n\t"
      "ds_read_b64_tr_b16 v[114:115], %[vWr0] offset:24576\n\t"
      "v_mfma_f32_16x16x32_bf16 v[144:147], v[96:99], v[68:71], v[144:147]\n\t"
      "s_waitcnt lgkmcnt(7)\n\t"
      "v_mfma_f32_16x16x32_bf16 v[152:155], v[104:107], v[68:71], v[152:155]\n\t"
      "ds_read_b128 v[92:95], %[vXr] offset:12544\n\t"
      "s_waitcnt lgkmcnt(5)\n\t"
      "v_mfma_f32_16x16x32_bf16 v[156:159], v[108:111], v[68:71], v[156:159]\n\t"
      "ds_read_b64_tr_b16 v[116:117], %[vWr1] offset:16384\n\t"
      "v_mfma_f32_16x16x32_bf16 v[140:143], v[108:111], v[64:67], v[140:143]\n\t"
      "ds_read_b64_tr_b16 v[118:119], %[vWr1] offset:24576\n\t"
      "v_mfma_f32_16x16x32_bf16 v[136:139], v[104:107], v[64:67], v[136:139]\n\t"
      "v_mfma_f32_16x16x32_bf16 v[168:171], v[104:107], v[72:75], v[168:171]\n\t"
      "ds_read_b128 v[64:67], %[vXr] offset:2048\n\t"
      "v_mfma_f32_16x16x32_bf16 v[172:175], v[108:111], v[72:75], v[172:175]\n\t"
      "ds_read_b64_tr_b16 v[120:121], %[vWr0] offset:16640\n\t"
      "v_mfma_f32_16x16x32_bf16 v[188:191], v[108:111], v[76:79], v[188:191]\n\t"
      "ds_read_b64_tr_b16 v[122:123], %[vWr0] offset:24832\n\t"
      "v_mfma_f32_16x16x32_bf16 v[184:187], v[104:107], v[76:79], v[184:187]\n\t"
      "v_mfma_f32_16x16x32_bf16 v[176:179], v[96:99], v[76:79], v[176:179]\n\t"
      "ds_read_b128 v[68:71], %[vXr] offset:2304\n\t"
      "v_mfma_f32_16x16x32_bf16 v[180:183], v[100:103], v[76:79], v[180:183]\n\t"
      "ds_read_b64_tr_b16 v[124:125], %[vWr1] offset:16640\n\t"
      "v_mfma_f32_16x16x32_bf16 v[164:167], v[100:103], v[72:75], v[164:167]\n\t"
      "ds_read_b64_tr_b16 v[126:127], %[vWr1] offset:24832\n\t"
      "v_mfma_f32_16x16x32_bf16 v[160:163], v[96:99], v[72:75], v[160:163]\n\t"
      "v_mfma_f32_16x16x32_bf16 v[192:195], v[96:99], v[80:83], v[192:195]\n\t"
      "ds_read_b128 v[72:75], %[vXr] offset:6144\n\t"
      "v_mfma_f32_16x16x32_bf16 v[196:199], v[100:103], v[80:83], v[196:199]\n\t"
      "s_waitcnt lgkmcnt(13)\n\t"
      "v_mfma_f32_16x16x32_bf16 v[212:215], v[100:103], v[84:87], v[212:215]\n\t"
      "v_mfma_f32_16x16x32_bf16 v[208:211], v[96:99], v[84:87], v[208:211]\n\t"
      "v_mfma_f32_16x16x32_bf16 v[216:219], v[104:107], v[84:87], v[216:219]\n\t"
      "ds_read_b128 v[76:79], %[vXr] offset:6400\n\t"
      "v_mfma_f32_16x16x32_bf16 v[220:223], v[108:111], v[84:87], v[220:223]\n\t"
      "v_mfma_f32_16x16x32_bf16 v[204:207], v[108:111], v[80:83], v[204:207]\n\t"
      "v_mfma_f32_16x16x32_bf16 v[200:203], v[104:107], v[80:83], v[200:203]\n\t"
      "s_waitcnt lgkmcnt(13)\n\t"
      "v_mfma_f32_16x16x32_bf16 v[232:235], v[104:107], v[88:91], v[232:235]\n\t"
      "ds_read_b128 v[80:83], %[vXr] offset:10240\n\t"
      "v_mfma_f32_16x16x32_bf16 v[236:239], v[108:111], v[88:91], v[236:239]\n\t"
      "s_waitcnt lgkmcnt(11)\n\t"
      "v_mfma_f32_16x16x32_bf16 v[252:255], v[108:111], v[92:95], v[252:255]\n\t"
      "v_mfma_f32_16x16x32_bf16 v[248:251], v[104:107], v[92:95], v[248:251]\n\t"
      "v_mfma_f32_16x16x32_bf16 v[240:243], v[96:99], v[92:95], v[240:243]\n\t"
      "ds_read_b128 v[84:87], %[vXr] offset:10496\n\t"
      "v_mfma_f32_16x16x32_bf16 v[244:247], v[100:103], v[92:95], v[244:247]\n\t"
      "v_mfma_f32_16x16x32_bf16 v[228:231], v[100:103], v[88:91], v[228:231]\n\t"
      "v_mfma_f32_16x16x32_bf16 v[224:227], v[96:99], v[88:91], v[224:227]\n\t"
      "s_waitcnt lgkmcnt(9)\n\t"
      "v_mfma_f32_16x16x32_bf16 v[128:131], v[112:115], v[64:67], v[128:131]\n\t"
      "ds_read_b128 v[88:91], %[vXr] offset:14336\n\t"
      "v_mfma_f32_16x16x32_bf16 v[132:135], v[116:119], v[64:67], v[132:135]\n\t"
      "ds_read_b128 v[92:95], %[vXr] offset:14592\n\t"
      "s_waitcnt lgkmcnt(8)\n\t"
      "v_mfma_f32_16x16x32_bf16 v[148:151], v[116:119], v[68:71], v[148:151]\n\t"
      "v_mfma_f32_16x16x32_bf16 v[144:147], v[112:115], v[68:71], v[144:147]\n\t"
      "v_mfma_f32_16x16x32_bf16 v[152:155], v[120:123], v[68:71], v[152:155]\n\t"
      "s_waitcnt lgkmcnt(6)\n\t"
      "v_mfma_f32_16x16x32_bf16 v[156:159], v[124:127], v[68:71], v[156:159]\n\t"
      "v_mfma_f32_16x16x32_bf16 v[140:143], v[124:127], v[64:67], v[140:143]\n\t"
      "v_mfma_f32_16x16x32_bf16 v[136:139], v[120:123], v[64:67], v[136:139]\n\t"
      "s_waitcnt vmcnt(0) lgkmcnt(0)\n\t"
      "s_barrier\n\t"
      "v_mfma_f32_16x16x32_bf16 v[168:171], v[120:123], v[72:75], v[168:171]\n\t"
      "ds_read_b128 v[64:67], %[vXr] offset:32768\n\t"
      "v_mfma_f32_16x16x32_bf16 v[172:175], v[124:127], v[72:75], v[172:175]\n\t"
      "v_mfma_f32_16x16x32_bf16 v[188:191], v[124:127], v[76:79], v[188:191]\n\t"
      "v_mfma_f32_16x16x32_bf16 v[184:187], v[120:123], v[76:79], v[184:187]\n\t"
      "v_mfma_f32_16x16x32_bf16 v[176:179], v[112:115], v[76:79], v[176:179]\n\t"
      "ds_read_b128 v[68:71], %[vXr] offset:33024\n\t"
      "v_mfma_f32_16x16x32_bf16 v[180:183], v[116:119], v[76:79], v[180:183]\n\t"
      "ds_read_b64_tr_b16 v[96:97], %[vWr0] offset:32768\n\t"
      "v_mfma_f32_16x16x32_bf16 v[164:167], v[116:119], v[72:75], v[164:167]\n\t"
      "ds_read_b64_tr_b16 v[98:99], %[vWr0] offset:40960\n\t"
      "v_mfma_f32_16x16x32_bf16 v[160:163], v[112:115], v[72:75], v[160:163]\n\t"
      "v_mfma_f32_16x16x32_bf16 v[192:195], v[112:115], v[80:83], v[192:195]\n\t"
      "ds_read_b128 v[72:75], %[vXr] offset:36864\n\t"
      "v_mfma_f32_16x16x32_bf16 v[196:199], v[116:119], v[80:83], v[196:199]\n\t"
      "ds_read_b64_tr_b16 v[100:101], %[vWr1] offset:32768\n\t"
      "v_mfma_f32_16x16x32_bf16 v[212:215], v[116:119], v[84:87], v[212:215]\n\t"
      "ds_read_b64_tr_b16 v[102:103], %[vWr1] offset:40960\n\t"
      "v_mfma_f32_16x16x32_bf16 v[208:211], v[112:115], v[84:87], v[208:211]\n\t"
      "v_mfma_f32_16x16x32_bf16 v[216:219], v[120:123], v[84:87], v[216:219]\n\t"
      "ds_read_b128 v[76:79], %[vXr] offset:37120\n\t"
      "v_mfma_f32_16x16x32_bf16 v[220:223], v[124:127], v[84:87], v[220:223]\n\t"
      "ds_read_b64_tr_b16 v[104:105], %[vWr0] offset:33024\n\t"
      "v_mfma_f32_16x16x32_bf16 v[204:207], v[124:127], v[80:83], v[204:207]\n\t"
      "ds_read_b64_tr_b16 v[106:107], %[vWr0] offset:41216\n\t"
      "v_mfma_f32_16x16x32_bf16 v[200:203], v[120:123], v[80:83], v[200:203]\n\t"
      "v_mfma_f32_16x16x32_bf16 v[232:235], v[120:123], v[88:91], v[232:235]\n\t"
      "ds_read_b128 v[80:83], %[vXr] offset:40960\n\t"
      "v_mfma_f32_16x16x32_bf16 v[236:239], v[124:127], v[88:91], v[236:239]\n\t"
      "ds_read_b64_tr_b16 v[108:109], %[vWr1] offset:33024\n\t"
      "v_mfma_f32_16x16x32_bf16 v[252:255], v[124:127], v[92:95], v[252:255]\n\t"
      "ds_read_b64_tr_b16 v[110:111], %[vWr1] offset:41216\n\t"
      "v_mfma_f32_16x16x32_bf16 v[248:251], v[120:123], v[92:95], v[248:251]\n\t"
      "v_mfma_f32_16x16x32_bf16 v[240:243], v[112:115], v[92:95], v[240:243]\n\t"
      "ds_read_b128 v[84:87], %[vXr] offset:41216\n\t"
      "v_mfma_f32_16x16x32_bf16 v[244:247], v[116:119], v[92:95], v[244:247]\n\t"
      "v_mfma_f32_16x16x32_bf16 v[228:231], v[116:119], v[88:91], v[228:231]\n\t"
      "v_mfma_f32_16x16x32_bf16 v[224:227], v[112:115], v[88:91], v[224:227]\n\t"
      "s_waitcnt lgkmcnt(10)\n\t"
      "v_mfma_f32_16x16x32_bf16 v[128:131], v[96:99], v[64:67], v[128:131]\n\t"
      "ds_read_b128 v[88:91], %[vXr] offset:45056\n\t"
      "s_waitcnt lgkmcnt(8)\n\t"
      "v_mfma_f32_16x16x32_bf16 v[132:135], v[100:103], v[64:67], v[132:135]\n\t"
      "ds_read_b64_tr_b16 v[112:113], %[vWr0] offset:49152\n\t"
      "v_mfma_f32_16x16x32_bf16 v[148:151], v[100:103], v[68:71], v[148:151]\n\t"
      "ds_read_b64_tr_b16 v[114:115], %[vWr0] offset:57344\n\t"
      "v_mfma_f32_16x16x32_bf16 v[144:147], v[96:99], v[68:71], v[144:147]\n\t"
      "s_waitcnt lgkmcnt(7)\n\t"
      "v_mfma_f32_16x16x32_bf16 v[152:155], v[104:107], v[68:71], v[152:155]\n\t"
      "ds_read_b128 v[92:95], %[vXr] offset:45312\n\t"
      "s_waitcnt lgkmcnt(5)\n\t"
      "v_mfma_f32_16x16x32_bf16 v[156:159], v[108:111], v[68:71], v[156:159]\n\t"
      "ds_read_b64_tr_b16 v[116:117], %[vWr1] offset:49152\n\t"
      "v_mfma_f32_16x16x32_bf16 v[140:143], v[108:111], v[64:67], v[140:143]\n\t"
      "ds_read_b64_tr_b16 v[118:119], %[vWr1] offset:57344\n\t"
      "v_mfma_f32_16x16x32_bf16 v[136:139], v[104:107], v[64:67], v[136:139]\n\t"
      "v_mfma_f32_16x16x32_bf16 v[168:171], v[104:107], v[72:75], v[168:171]\n\t"
      "ds_read_b128 v[64:67], %[vXr] offset:34816\n\t"
      "v_mfma_f32_16x16x32_bf16 v[172:175], v[108:111], v[72:75], v[172:175]\n\t"
      "ds_read_b64_tr_b16 v[120:121], %[vWr0] offset:49408\n\t"
      "v_mfma_f32_16x16x32_bf16 v[188:191], v[108:111], v[76:79], v[188:191]\n\t"
      "ds_read_b64_tr_b16 v[122:123], %[vWr0] offset:57600\n\t"
      "v_mfma_f32_16x16x32_bf16 v[184:187], v[104:107], v[76:79], v[184:187]\n\t"
      "v_mfma_f32_16x16x32_bf16 v[176:179], v[96:99], v[76:79], v[176:179]\n\t"
      "ds_read_b128 v[68:71], %[vXr] offset:35072\n\t"
      "v_mfma_f32_16x16x32_bf16 v[180:183], v[100:103], v[76:79], v[180:183]\n\t"
      "ds_read_b64_tr_b16 v[124:125], %[vWr1] offset:49408\n\t"
      "v_mfma_f32_16x16x32_bf16 v[164:167], v[100:103], v[72:75], v[164:167]\n\t"
      "ds_read_b64_tr_b16 v[126:127], %[vWr1] offset:57600\n\t"
      "v_mfma_f32_16x16x32_bf16 v[160:163], v[96:99], v[72:75], v[160:163]\n\t"
      "v_mfma_f32_16x16x32_bf16 v[192:195], v[96:99], v[80:83], v[192:195]\n\t"
      "ds_read_b128 v[72:75], %[vXr] offset:38912\n\t"
      "v_mfma_f32_16x16x32_bf16 v[196:199], v[100:103], v[80:83], v[196:199]\n\t"
      "s_waitcnt lgkmcnt(13)\n\t"
      "v_mfma_f32_16x16x32_bf16 v[212:215], v[100:103], v[84:87], v[212:215]\n\t"
      "v_mfma_f32_16x16x32_bf16 v[208:211], v[96:99], v[84:87], v[208:211]\n\t"
      "v_mfma_f32_16x16x32_bf16 v[216:219], v[104:107], v[84:87], v[216:219]\n\t"
      "ds_read_b128 v[76:79], %[vXr] offset:39168\n\t"
      "v_mfma_f32_16x16x32_bf16 v[220:223], v[108:111], v[84:87], v[220:223]\n\t"
      "v_mfma_f32_16x16x32_bf16 v[204:207], v[108:111], v[80:83], v[204:207]\n\t"
      "v_mfma_f32_16x16x32_bf16 v[200:203], v[104:107], v[80:83], v[200:203]\n\t"
      "s_waitcnt lgkmcnt(13)\n\t"
      "v_mfma_f32_16x16x32_bf16 v[232:235], v[104:107], v[88:91], v[232:235]\n\t"
      "ds_read_b128 v[80:83], %[vXr] offset:43008\n\t"
      "v_mfma_f32_16x16x32_bf16 v[236:239], v[108:111], v[88:91], v[236:239]\n\t"
      "s_waitcnt lgkmcnt(11)\n\t"
      "v_mfma_f32_16x16x32_bf16 v[252:255], v[108:111], v[92:95], v[252:255]\n\t"
      "v_mfma_f32_16x16x32_bf16 v[248:251], v[104:107], v[92:95], v[248:251]\n\t"
      "v_mfma_f32_16x16x32_bf16 v[240:243], v[96:99], v[92:95], v[240:243]\n\t"
      "ds_read_b128 v[84:87], %[vXr] offset:43264\n\t"
      "v_mfma_f32_16x16x32_bf16 v[244:247], v[100:103], v[92:95], v[244:247]\n\t"
      "v_mfma_f32_16x16x32_bf16 v[228:231], v[100:103], v[88:91], v[228:231]\n\t"
      "v_mfma_f32_16x16x32_bf16 v[224:227], v[96:99], v[88:91], v[224:227]\n\t"
      "s_waitcnt lgkmcnt(9)\n\t"
      "v_mfma_f32_16x16x32_bf16 v[128:131], v[112:115], v[64:67], v[128:131]\n\t"
      "ds_read_b128 v[88:91], %[vXr] offset:47104\n\t"
      "v_mfma_f32_16x16x32_bf16 v[132:135], v[116:119], v[64:67], v[132:135]\n\t"
      "ds_read_b128 v[92:95], %[vXr] offset:47360\n\t"
      "s_waitcnt lgkmcnt(8)\n\t"
      "v_mfma_f32_16x16x32_bf16 v[148:151], v[116:119], v[68:71], v[148:151]\n\t"
      "v_mfma_f32_16x16x32_bf16 v[144:147], v[112:115], v[68:71], v[144:147]\n\t"
      "v_mfma_f32_16x16x32_bf16 v[152:155], v[120:123], v[68:71], v[152:155]\n\t"
      "s_waitcnt lgkmcnt(6)\n\t"
      "v_mfma_f32_16x16x32_bf16 v[156:159], v[124:127], v[68:71], v[156:159]\n\t"
      "v_mfma_f32_16x16x32_bf16 v[140:143], v[124:127], v[64:67], v[140:143]\n\t"
      "v_mfma_f32_16x16x32_bf16 v[136:139], v[120:123], v[64:67], v[136:139]\n\t"
      "s_waitcnt lgkmcnt(5)\n\t"
      "v_mfma_f32_16x16x32_bf16 v[168:171], v[120:123], v[72:75], v[168:171]\n\t"
      "v_mfma_f32_16x16x32_bf16 v[172:175], v[124:127], v[72:75], v[172:175]\n\t"
      "s_waitcnt lgkmcnt(4)\n\t"
      "v_mfma_f32_16x16x32_bf16 v[188:191], v[124:127], v[76:79], v[188:191]\n\t"
      "v_mfma_f32_16x16x32_bf16 v[184:187], v[120:123], v[76:79], v[184:187]\n\t"
      "v_mfma_f32_16x16x32_bf16 v[176:179], v[112:115], v[76:79], v[176:179]\n\t"
      "v_mfma_f32_16x16x32_bf16 v[180:183], v[116:119], v[76:79], v[180:183]\n\t"
      "v_mfma_f32_16x16x32_bf16 v[164:167], v[116:119], v[72:75], v[164:167]\n\t"
      "v_mfma_f32_16x16x32_bf16 v[160:163], v[112:115], v[72:75], v[160:163]\n\t"
      "s_waitcnt lgkmcnt(3)\n\t"
      "v_mfma_f32_16x16x32_bf16 v[192:195], v[112:115], v[80:83], v[192:195]\n\t"
      "v_mfma_f32_16x16x32_bf16 v[196:199], v[116:119], v[80:83], v[196:199]\n\t"
      "s_waitcnt lgkmcnt(2)\n\t"
      "v_mfma_f32_16x16x32_bf16 v[212:215], v[116:119], v[84:87], v[212:215]\n\t"
      "v_mfma_f32_16x16x32_bf16 v[208:211], v[112:115], v[84:87], v[208:211]\n\t"
      "v_mfma_f32_16x16x32_bf16 v[216:219], v[120:123], v[84:87], v[216:219]\n\t"
      "v_mfma_f32_16x16x32_bf16 v[220:223], v[124:127], v[84:87], v[220:223]\n\t"
      "v_mfma_f32_16x16x32_bf16 v[204:207], v[124:127], v[80:83], v[204:207]\n\t"
      "v_mfma_f32_16x16x32_bf16 v[200:203], v[120:123], v[80:83], v[200:203]\n\t"
      "s_waitcnt lgkmcnt(1)\n\t"
      "v_mfma_f32_16x16x32_bf16 v[232:235], v[120:123], v[88:91], v[232:235]\n\t"
      "v_mfma_f32_16x16x32_bf16 v[236:239], v[124:127], v[88:91], v[236:239]\n\t"
      "s_waitcnt lgkmcnt(0)\n\t"
      "v_mfma_f32_16x16x32_bf16 v[252:255], v[124:127], v[92:95], v[252:255]\n\t"
      "v_mfma_f32_16x16x32_bf16 v[248:251], v[120:123], v[92:95], v[248:251]\n\t"
      "v_mfma_f32_16x16x32_bf16 v[240:243], v[112:115], v[92:95], v[240:243]\n\t"
      "v_mfma_f32_16x16x32_bf16 v[244:247], v[116:119], v[92:95], v[244:247]\n\t"
      "v_mfma_f32_16x16x32_bf16 v[228:231], v[116:119], v[88:91], v[228:231]\n\t"
      "v_mfma_f32_16x16x32_bf16 v[224:227], v[112:115], v[88:91], v[224:227]\n\t"
      "s_nop 7\n\t"
      "v_max_f32 v128, 0, v128\n\t"
      "v_max_f32 v129, 0, v129\n\t"
      "v_max_f32 v130, 0, v130\n\t"
      "v_max_f32 v131, 0, v131\n\t"
      "v_max_f32 v132, 0, v132\n\t"
      "v_max_f32 v133, 0, v133\n\t"
      "v_max_f32 v134, 0, v134\n\t"
      "v_max_f32 v135, 0, v135\n\t"
      "v_mul_f32 v128, v128, v136\n\t"
      "v_mul_f32 v129, v129, v137\n\t"
      "v_mul_f32 v130, v130, v138\n\t"
      "v_mul_f32 v131, v131, v139\n\t"
      "v_mul_f32 v132, v132, v140\n\t"
      "v_mul_f32 v133, v133, v141\n\t"
      "v_mul_f32 v134, v134, v142\n\t"
      "v_mul_f32 v135, v135, v143\n\t"
      "v_cvt_pk_bf16_f32 v64, v128, v129\n\t"
      "v_cvt_pk_bf16_f32 v65, v130, v131\n\t"
      "v_cvt_pk_bf16_f32 v66, v132, v133\n\t"
      "v_cvt_pk_bf16_f32 v67, v134, v135\n\t"
      "buffer_store_dwordx4 v[64:67], %[vOut], %[srdWS], s44 offen offset:0 sc1\n\t"
      "v_max_f32 v144, 0, v144\n\t"
      "v_max_f32 v145, 0, v145\n\t"
      "v_max_f32 v146, 0, v146\n\t"
      "v_max_f32 v147, 0, v147\n\t"
      "v_max_f32 v148, 0, v148\n\t"
      "v_max_f32 v149, 0, v149\n\t"
      "v_max_f32 v150, 0, v150\n\t"
      "v_max_f32 v151, 0, v151\n\t"
      "v_mul_f32 v144, v144, v152\n\t"
      "v_mul_f32 v145, v145, v153\n\t"
      "v_mul_f32 v146, v146, v154\n\t"
      "v_mul_f32 v147, v147, v155\n\t"
      "v_mul_f32 v148, v148, v156\n\t"
      "v_mul_f32 v149, v149, v157\n\t"
      "v_mul_f32 v150, v150, v158\n\t"
      "v_mul_f32 v151, v151, v159\n\t"
      "v_cvt_pk_bf16_f32 v68, v144, v145\n\t"
      "v_cvt_pk_bf16_f32 v69, v146, v147\n\t"
      "v_cvt_pk_bf16_f32 v70, v148, v149\n\t"
      "v_cvt_pk_bf16_f32 v71, v150, v151\n\t"
      "buffer_store_dwordx4 v[68:71], %[vOut], %[srdWS], s44 offen offset:256 sc1\n\t"
      "s_add_u32 s44, s44, 294912\n\t"
      "v_max_f32 v160, 0, v160\n\t"
      "v_max_f32 v161, 0, v161\n\t"
      "v_max_f32 v162, 0, v162\n\t"
      "v_max_f32 v163, 0, v163\n\t"
      "v_max_f32 v164, 0, v164\n\t"
      "v_max_f32 v165, 0, v165\n\t"
      "v_max_f32 v166, 0, v166\n\t"
      "v_max_f32 v167, 0, v167\n\t"
      "v_mul_f32 v160, v160, v168\n\t"
      "v_mul_f32 v161, v161, v169\n\t"
      "v_mul_f32 v162, v162, v170\n\t"
      "v_mul_f32 v163, v163, v171\n\t"
      "v_mul_f32 v164, v164, v172\n\t"
      "v_mul_f32 v165, v165, v173\n\t"
      "v_mul_f32 v166, v166, v174\n\t"
      "v_mul_f32 v167, v167, v175\n\t"
      "v_cvt_pk_bf16_f32 v72, v160, v161\n\t"
      "v_cvt_pk_bf16_f32 v73, v162, v163\n\t"
      "v_cvt_pk_bf16_f32 v74, v164, v165\n\t"
      "v_cvt_pk_bf16_f32 v75, v166, v167\n\t"
      "buffer_store_dwordx4 v[72:75], %[vOut], %[srdWS], s44 offen offset:0 sc1\n\t"
      "v_max_f32 v176, 0, v176\n\t"
      "v_max_f32 v177, 0, v177\n\t"
      "v_max_f32 v178, 0, v178\n\t"
      "v_max_f32 v179, 0, v179\n\t"
      "v_max_f32 v180, 0, v180\n\t"
      "v_max_f32 v181, 0, v181\n\t"
      "v_max_f32 v182, 0, v182\n\t"
      "v_max_f32 v183, 0, v183\n\t"
      "v_mul_f32 v176, v176, v184\n\t"
      "v_mul_f32 v177, v177, v185\n\t"
      "v_mul_f32 v178, v178, v186\n\t"
      "v_mul_f32 v179, v179, v187\n\t"
      "v_mul_f32 v180, v180, v188\n\t"
      "v_mul_f32 v181, v181, v189\n\t"
      "v_mul_f32 v182, v182, v190\n\t"
      "v_mul_f32 v183, v183, v191\n\t"
      "v_cvt_pk_bf16_f32 v76, v176, v177\n\t"
      "v_cvt_pk_bf16_f32 v77, v178, v179\n\t"
      "v_cvt_pk_bf16_f32 v78, v180, v181\n\t"
      "v_cvt_pk_bf16_f32 v79, v182, v183\n\t"
      "buffer_store_dwordx4 v[76:79], %[vOut], %[srdWS], s44 offen offset:256 sc1\n\t"
      "s_add_u32 s44, s44, 294912\n\t"
      "v_max_f32 v192, 0, v192\n\t"
      "v_max_f32 v193, 0, v193\n\t"
      "v_max_f32 v194, 0, v194\n\t"
      "v_max_f32 v195, 0, v195\n\t"
      "v_max_f32 v196, 0, v196\n\t"
      "v_max_f32 v197, 0, v197\n\t"
      "v_max_f32 v198, 0, v198\n\t"
      "v_max_f32 v199, 0, v199\n\t"
      "v_mul_f32 v192, v192, v200\n\t"
      "v_mul_f32 v193, v193, v201\n\t"
      "v_mul_f32 v194, v194, v202\n\t"
      "v_mul_f32 v195, v195, v203\n\t"
      "v_mul_f32 v196, v196, v204\n\t"
      "v_mul_f32 v197, v197, v205\n\t"
      "v_mul_f32 v198, v198, v206\n\t"
      "v_mul_f32 v199, v199, v207\n\t"
      "v_cvt_pk_bf16_f32 v80, v192, v193\n\t"
      "v_cvt_pk_bf16_f32 v81, v194, v195\n\t"
      "v_cvt_pk_bf16_f32 v82, v196, v197\n\t"
      "v_cvt_pk_bf16_f32 v83, v198, v199\n\t"
      "buffer_store_dwordx4 v[80:83], %[vOut], %[srdWS], s44 offen offset:0 sc1\n\t"
      "v_max_f32 v208, 0, v208\n\t"
      "v_max_f32 v209, 0, v209\n\t"
      "v_max_f32 v210, 0, v210\n\t"
      "v_max_f32 v211, 0, v211\n\t"
      "v_max_f32 v212, 0, v212\n\t"
      "v_max_f32 v213, 0, v213\n\t"
      "v_max_f32 v214, 0, v214\n\t"
      "v_max_f32 v215, 0, v215\n\t"
      "v_mul_f32 v208, v208, v216\n\t"
      "v_mul_f32 v209, v209, v217\n\t"
      "v_mul_f32 v210, v210, v218\n\t"
      "v_mul_f32 v211, v211, v219\n\t"
      "v_mul_f32 v212, v212, v220\n\t"
      "v_mul_f32 v213, v213, v221\n\t"
      "v_mul_f32 v214, v214, v222\n\t"
      "v_mul_f32 v215, v215, v223\n\t"
      "v_cvt_pk_bf16_f32 v84, v208, v209\n\t"
      "v_cvt_pk_bf16_f32 v85, v210, v211\n\t"
      "v_cvt_pk_bf16_f32 v86, v212, v213\n\t"
      "v_cvt_pk_bf16_f32 v87, v214, v215\n\t"
      "buffer_store_dwordx4 v[84:87], %[vOut], %[srdWS], s44 offen offset:256 sc1\n\t"
      "s_add_u32 s44, s44, 294912\n\t"
      "v_max_f32 v224, 0, v224\n\t"
      "v_max_f32 v225, 0, v225\n\t"
      "v_max_f32 v226, 0, v226\n\t"
      "v_max_f32 v227, 0, v227\n\t"
      "v_max_f32 v228, 0, v228\n\t"
      "v_max_f32 v229, 0, v229\n\t"
      "v_max_f32 v230, 0, v230\n\t"
      "v_max_f32 v231, 0, v231\n\t"
      "v_mul_f32 v224, v224, v232\n\t"
      "v_mul_f32 v225, v225, v233\n\t"
      "v_mul_f32 v226, v226, v234\n\t"
      "v_mul_f32 v227, v227, v235\n\t"
      "v_mul_f32 v228, v228, v236\n\t"
      "v_mul_f32 v229, v229, v237\n\t"
      "v_mul_f32 v230, v230, v238\n\t"
      "v_mul_f32 v231, v231, v239\n\t"
      "v_cvt_pk_bf16_f32 v88, v224, v225\n\t"
      "v_cvt_pk_bf16_f32 v89, v226, v227\n\t"
      "v_cvt_pk_bf16_f32 v90, v228, v229\n\t"
      "v_cvt_pk_bf16_f32 v91, v230, v231\n\t"
      "buffer_store_dwordx4 v[88:91], %[vOut], %[srdWS], s44 offen offset:0 sc1\n\t"
      "v_max_f32 v240, 0, v240\n\t"
      "v_max_f32 v241, 0, v241\n\t"
      "v_max_f32 v242, 0, v242\n\t"
      "v_max_f32 v243, 0, v243\n\t"
      "v_max_f32 v244, 0, v244\n\t"
      "v_max_f32 v245, 0, v245\n\t"
      "v_max_f32 v246, 0, v246\n\t"
      "v_max_f32 v247, 0, v247\n\t"
      "v_mul_f32 v240, v240, v248\n\t"
      "v_mul_f32 v241, v241, v249\n\t"
      "v_mul_f32 v242, v242, v250\n\t"
      "v_mul_f32 v243, v243, v251\n\t"
      "v_mul_f32 v244, v244, v252\n\t"
      "v_mul_f32 v245, v245, v253\n\t"
      "v_mul_f32 v246, v246, v254\n\t"
      "v_mul_f32 v247, v247, v255\n\t"
      "v_cvt_pk_bf16_f32 v92, v240, v241\n\t"
      "v_cvt_pk_bf16_f32 v93, v242, v243\n\t"
      "v_cvt_pk_bf16_f32 v94, v244, v245\n\t"
      "v_cvt_pk_bf16_f32 v95, v246, v247\n\t"
      "buffer_store_dwordx4 v[92:95], %[vOut], %[srdWS], s44 offen offset:256 sc1\n\t"
      "s_nop 1\n\t"
      "P1_END_%=:\n\t"
      :
      : [taglo] "s"(taglo), [taghi] "s"(taghi), [srdWS] "s"(srdWS), [srdKW] "s"(srdKW), [srdP] "s"(srdP), [srdWO] "s"(srdWO), [wslo] "s"(srdWS[0]), [wshi] "s"(srdWS[1]),
        [sX0] "s"(sX0), [sMX] "s"(sMX), [sMW] "s"(sMW), [sO] "s"(sO), [sPF0] "s"(sPF0), [sPFs] "s"(sPFs), [sPFo] "s"(sPFo), [sXd] "s"(sXd),
        [w] "s"(w), [sKVst] "s"(sKVst), [kvno] "s"(kvno), [light] "s"(xcd & 1), [sKVi] "s"(sKVi), [sP0] "s"(sP0), [sPkv] "s"(sPkv), [sOa] "s"(sOa), [sFO] "s"(sFO), [sE] "s"(b | w), [kr] "s"(krot),
        [vL16] "v"(vL16), [vWlo] "v"(vWlo), [vWhi] "v"(vWhi), [vWst] "v"(vWst), [vWr0] "v"(vWr0), [vWr1] "v"(vWr1), [vXr] "v"(vXr), [vOut] "v"(vOut),
        [vKVsrc] "v"(vKVsrc), [vKVr] "v"(vKVr), [vXkv] "v"(vXkv), [vKVw] "v"(vKVw), [vKVo] "v"(vKVo), [vPF] "v"(vPF), [vKi] "v"(vKi),
        [pf] "v"(pfc)
      : "v30","v31","v32","v33","v34","v35","v36","v37","v38","v39","v40","v41","v42","v43","v44","v45","v46","v47","v48","v49","v50","v51","v52","v53","v54","v55","v56","v57","v58","v59","v60","v61","v62","v63","v64","v65","v66","v67","v68","v69","v70","v71","v72","v73","v74","v75","v76","v77","v78","v79","v80","v81","v82","v83","v84","v85","v86","v87","v88","v89","v90","v91","v92","v93","v94","v95","v96","v97","v98","v99","v100","v101","v102","v103","v104","v105","v106","v107","v108","v109","v110","v111","v112","v113","v114","v115","v116","v117","v118","v119","v120","v121","v122","v123","v124","v125","v126","v127","v128","v129","v130","v131","v132","v133","v134","v135","v136","v137","v138","v139","v140","v141","v142","v143","v144","v145","v146","v147","v148","v149","v150","v151","v152","v153","v154","v155","v156","v157","v158","v159","v160","v161","v162","v163","v164","v165","v166","v167","v168","v169","v170","v171","v172","v173","v174","v175","v176","v177","v178","v179","v180","v181","v182","v183","v184","v185","v186","v187","v188","v189","v190","v191","v192","v193","v194","v195","v196","v197","v198","v199","v200","v201","v202","v203","v204","v205","v206","v207","v208","v209","v210","v211","v212","v213","v214","v215","v216","v217","v218","v219","v220","v221","v222","v223","v224","v225","v226","v227","v228","v229","v230","v231","v232","v233","v234","v235","v236","v237","v238","v239","v240","v241","v242","v243","v244","v245","v246","v247","v248","v249","v250","v251","v252","v253","v254","v255","s40","s41","s42","s43","s44","s45","s46","s47","s48","s49","s50","s51","s52","s53","s54","s55","s56","s57","s58","s59","vcc","scc","memory");

  if (w >= 4) {

    const int lane = fresh_lane();
    const unsigned ln = lane >> 3, ch = lane & 7, g = lane >> 4, q = (lane >> 2) & 3, p = lane & 3, rho = (g & 1) * 4 + q, r16 = lane & 15;
    const unsigned wg_ = w - 4, kh = wg_ & 1, tm = (((xcd & 2) ^ 2) + (xcd >> 2)) * 4 + (jj & 3), tb = tm * 4 + wg_;
    const unsigned t0 = (xcd & 1) * 8 + c8, tn = (sel(t0 == 4, 11u, sel(t0 == 11, 4u, t0)) - ((unsigned)ad >> 7)) & 15;
    const u32x4 srdR = mk_srd((u64)x, 2048u * 2048u), srdO = mk_srd((u64)out, 2048u * 2048u);
    const unsigned gX0 = 10485760u + tb * (288u * 1024u) + kh * 2048u, gXo = (1u - 2u * kh) * (288u * 1024u);
    const unsigned gW0 = wg_ * 16u * 2048u + tn * 128u, gM = wg_ * 2048u;
    const unsigned vL16 = lane * 16u, vXsrc = g * 512u + r16 * 16u, vWsrc = ln * 2048u + (((ch >> 1) ^ (ln >> 1)) * 32) + (ch & 1) * 16;
    const unsigned vWr = kh * 4096u + (g >> 1) * 1024u + rho * 128u + ((rho >> 1) * 32) + p * 8;
    const unsigned vO = (tb * 32 + r16) * 2048u + tn * 128u + (g & 1) * 32 + (g >> 1) * 16;
    const unsigned vPl = (lane & 31) * 128u, sPH = 9699328u + (tm >> 1) * 4096u, sPO = 9830400u + (tb >> 1) * 4096u, sFH = 9699328u + (pm * 32 + pn) * 128u;
    const unsigned wdlo = (unsigned)ad, wdhi = (unsigned)(ad >> 32) & 0xffffu;
    const u64 tag = ((u64)taghi << 32) | taglo;
    asm volatile(
      "s_mov_b32 s40, %[sX0]\n\t"
      "s_mov_b32 s41, %[sW0]\n\t"
      "s_mov_b32 s42, %[sM]\n\t"
      "s_lshl_b32 s49, %[sM], 1\n\t"
      "s_add_u32 s49, s49, 8421376\n\t"
      "s_add_u32 s49, s49, %[kr]\n\t"
      "s_bitset0_b32 s49, 18\n\t"
      "s_mov_b32 s44, %[wdlo]\n\t"
      "s_mov_b32 s45, %[wdhi]\n\t"
      "s_mov_b32 s46, 7340032\n\t"
      "s_mov_b32 s47, 0x20000\n\t"
      "v_mov_b32 v208, 0\n\t"
      "v_mov_b32 v209, 0\n\t"
      "v_mov_b32 v210, 0\n\t"
      "v_mov_b32 v211, 0\n\t"
      "v_xor_b32 v8, 0, %[vWr0]\n\t"
      "v_xor_b32 v9, 32, %[vWr0]\n\t"
      "v_xor_b32 v10, 64, %[vWr0]\n\t"
      "v_xor_b32 v11, 96, %[vWr0]\n\t"
      "v_mfma_f32_32x32x16_bf16 v[16:31], v[208:211], v[208:211], 0\n\t"
      "v_mfma_f32_32x32x16_bf16 v[32:47], v[208:211], v[208:211], 0\n\t"
      "v_mfma_f32_32x32x16_bf16 v[48:63], v[208:211], v[208:211], 0\n\t"
      "v_mfma_f32_32x32x16_bf16 v[64:79], v[208:211], v[208:211], 0\n\t"
      "s_waitcnt vmcnt(0)\n\t"
      "s_barrier\n\t"
      "s_cmp_lg_u32 %[sM], 0\n\t"
      "s_cbranch_scc1 PG_NOF_%=\n\t"
      "v_lshrrev_b32 v14, 1, %[vL16]\n\t"
      "v_mov_b32 v12, %[taglo]\n\t"
      "v_mov_b32 v13, %[taghi]\n\t"
      "s_mov_b64 exec, 0xffff\n\t"
      "buffer_store_dwordx2 v[12:13], v14, %[srdWS], %[sFH] offen sc1\n\t"
      "s_mov_b64 exec, -1\n\t"
      "PG_NOF_%=:\n\t"
      "s_add_u32 m0, s42, 32768\n\t"
      "s_add_u32 s43, s41, 15360\n\t"
      "buffer_load_dwordx4 %[vWsrc], s[44:47], s41 offen lds\n\t"
      "buffer_load_dwordx4 %[vWsrc], s[44:47], s43 offen offset:1024 lds\n\t"
      "s_add_u32 s41, s41, 131072\n\t"
      "s_add_u32 m0, s42, 40960\n\t"
      "s_add_u32 s43, s41, 15360\n\t"
      "buffer_load_dwordx4 %[vWsrc], s[44:47], s41 offen lds\n\t"
      "buffer_load_dwordx4 %[vWsrc], s[44:47], s43 offen offset:1024 lds\n\t"
      "s_add_u32 s41, s41, 131072\n\t"
      "s_add_u32 m0, s42, 49152\n\t"
      "s_add_u32 s43, s41, 15360\n\t"
      "buffer_load_dwordx4 %[vWsrc], s[44:47], s41 offen lds\n\t"
      "buffer_load_dwordx4 %[vWsrc], s[44:47], s43 offen offset:1024 lds\n\t"
      "s_add_u32 s41, s41, 131072\n\t"
      "s_add_u32 m0, s42, 57344\n\t"
      "s_add_u32 s43, s41, 15360\n\t"
      "buffer_load_dwordx4 %[vWsrc], s[44:47], s41 offen lds\n\t"
      "buffer_load_dwordx4 %[vWsrc], s[44:47], s43 offen offset:1024 lds\n\t"
      "s_add_u32 s41, s41, 131072\n\t"
      "s_add_u32 m0, s42, 98304\n\t"
      "s_add_u32 s43, s41, 15360\n\t"
      "buffer_load_dwordx4 %[vWsrc], s[44:47], s41 offen lds\n\t"
      "buffer_load_dwordx4 %[vWsrc], s[44:47], s43 offen offset:1024 lds\n\t"
      "s_add_u32 s41, s41, 131072\n\t"
      "s_add_u32 m0, s42, 106496\n\t"
      "s_add_u32 s43, s41, 15360\n\t"
      "buffer_load_dwordx4 %[vWsrc], s[44:47], s41 offen lds\n\t"
      "buffer_load_dwordx4 %[vWsrc], s[44:47], s43 offen offset:1024 lds\n\t"
      "s_add_u32 s41, s41, 131072\n\t"
      "s_add_u32 m0, s42, 114688\n\t"
      "s_add_u32 s43, s41, 15360\n\t"
      "buffer_load_dwordx4 %[vWsrc], s[44:47], s41 offen lds\n\t"
      "buffer_load_dwordx4 %[vWsrc], s[44:47], s43 offen offset:1024 lds\n\t"
      "s_add_u32 s41, s41, 131072\n\t"
      "s_add_u32 m0, s42, 122880\n\t"
      "s_add_u32 s43, s41, 15360\n\t"
      "buffer_load_dwordx4 %[vWsrc], s[44:47], s41 offen lds\n\t"
      "buffer_load_dwordx4 %[vWsrc], s[44:47], s43 offen offset:1024 lds\n\t"
      "s_add_u32 s41, s41, 131072\n\t"
      "s_lshl_b32 s43, s42, 1\n\t"
      "s_add_u32 m0, s43, 81920\n\t"
      "s_add_u32 s49, s49, 16384\n\t"
      "s_bitset0_b32 s49, 18\n\t"
      "buffer_load_dwordx4 %[vL16], %[srdWS], s49 offen offset:0 sc1 lds\n\t"
      "buffer_load_dwordx4 %[vL16], %[srdWS], s49 offen offset:1024 sc1 lds\n\t"
      "buffer_load_dwordx4 %[vL16], %[srdWS], s49 offen offset:2048 sc1 lds\n\t"
      "buffer_load_dwordx4 %[vL16], %[srdWS], s49 offen offset:3072 sc1 lds\n\t"
      "s_waitcnt vmcnt(14) lgkmcnt(0)\n\t"
      "s_barrier\n\t"
      "s_mov_b32 s48, 0\n\t"
      "PG_PH_%=:\n\t"
      "buffer_load_dwordx2 v[12:13], %[vPl], %[srdWS], %[sPH] offen sc1\n\t"
      "s_waitcnt vmcnt(0)\n\t"
      "v_cmp_eq_u64 vcc, %[tag], v[12:13]\n\t"
      "s_add_u32 s48, s48, 1\n\t"
      "s_cmp_eq_u64 vcc, -1\n\t"
      "s_cbranch_scc1 PG_PH_OK_%=\n\t"
      "s_cmp_lt_u32 s48, 0x40000\n\t"
      "s_cbranch_scc1 PG_PH_%=\n\t"
      "PG_PH_OK_%=:\n\t"
      "buffer_load_dwordx4 v[112:115], %[vXsrc], %[srdWS], s40 offen offset:0 sc1\n\t"
      "s_add_u32 s50, s40, %[sXo]\n\t"
      "buffer_load_dwordx4 v[116:119], %[vXsrc], %[srdWS], s40 offen offset:256 sc1\n\t"
      "buffer_load_dwordx4 v[120:123], %[vXsrc], %[srdWS], s50 offen offset:0 sc1\n\t"
      "buffer_load_dwordx4 v[124:127], %[vXsrc], %[srdWS], s50 offen offset:256 sc1\n\t"
      "s_add_u32 s40, s40, 4096\n\t"
      "buffer_load_dwordx4 v[128:131], %[vXsrc], %[srdWS], s40 offen offset:0 sc1\n\t"
      "s_add_u32 s50, s40, %[sXo]\n\t"
      "buffer_load_dwordx4 v[132:135], %[vXsrc], %[srdWS], s40 offen offset:256 sc1\n\t"
      "buffer_load_dwordx4 v[136:139], %[vXsrc], %[srdWS], s50 offen offset:0 sc1\n\t"
      "buffer_load_dwordx4 v[140:143], %[vXsrc], %[srdWS], s50 offen offset:256 sc1\n\t"
      "s_add_u32 s40, s40, 4096\n\t"
      "ds_read_b64_tr_b16 v[208:209], v8 offset:32768\n\t"
      "ds_read_b64_tr_b16 v[210:211], v8 offset:34816\n\t"
      "ds_read_b64_tr_b16 v[212:213], v9 offset:32768\n\t"
      "ds_read_b64_tr_b16 v[214:215], v9 offset:34816\n\t"
      "ds_read_b64_tr_b16 v[216:217], v10 offset:32768\n\t"
      "ds_read_b64_tr_b16 v[218:219], v10 offset:34816\n\t"
      "ds_read_b64_tr_b16 v[220:221], v11 offset:32768\n\t"
      "ds_read_b64_tr_b16 v[222:223], v11 offset:34816\n\t"
      "s_waitcnt vmcnt(4) lgkmcnt(0)\n\t"
      "v_mfma_f32_16x16x32_bf16 v[16:19], v[208:211], v[112:115], v[16:19]\n\t"
      "ds_read_b64_tr_b16 v[224:225], v8 offset:40960\n\t"
      "buffer_load_dwordx4 v[144:147], %[vXsrc], %[srdWS], s40 offen offset:0 sc1\n\t"
      "v_mfma_f32_16x16x32_bf16 v[20:23], v[212:215], v[112:115], v[20:23]\n\t"
      "ds_read_b64_tr_b16 v[226:227], v8 offset:43008\n\t"
      "s_add_u32 s50, s40, %[sXo]\n\t"
      "buffer_load_dwordx4 v[148:151], %[vXsrc], %[srdWS], s40 offen offset:256 sc1\n\t"
      "v_mfma_f32_16x16x32_bf16 v[24:27], v[216:219], v[112:115], v[24:27]\n\t"
      "ds_read_b64_tr_b16 v[228:229], v9 offset:40960\n\t"
      "buffer_load_dwordx4 v[152:155], %[vXsrc], %[srdWS], s50 offen offset:0 sc1\n\t"
      "v_mfma_f32_16x16x32_bf16 v[28:31], v[220:223], v[112:115], v[28:31]\n\t"
      "ds_read_b64_tr_b16 v[230:231], v9 offset:43008\n\t"
      "v_mfma_f32_16x16x32_bf16 v[44:47], v[220:223], v[116:119], v[44:47]\n\t"
      "ds_read_b64_tr_b16 v[232:233], v10 offset:40960\n\t"
      "buffer_load_dwordx4 v[156:159], %[vXsrc], %[srdWS], s50 offen offset:256 sc1\n\t"
      "s_add_u32 s40, s40, 4096\n\t"
      "v_mfma_f32_16x16x32_bf16 v[40:43], v[216:219], v[116:119], v[40:43]\n\t"
      "ds_read_b64_tr_b16 v[234:235], v10 offset:43008\n\t"
      "buffer_load_dwordx4 v[160:163], %[vXsrc], %[srdWS], s40 offen offset:0 sc1\n\t"
      "v_mfma_f32_16x16x32_bf16 v[36:39], v[212:215], v[116:119], v[36:39]\n\t"
      "ds_read_b64_tr_b16 v[236:237], v11 offset:40960\n\t"
      "s_add_u32 s50, s40, %[sXo]\n\t"
      "buffer_load_dwordx4 v[164:167], %[vXsrc], %[srdWS], s40 offen offset:256 sc1\n\t"
      "v_mfma_f32_16x16x32_bf16 v[32:35], v[208:211], v[116:119], v[32:35]\n\t"
      "ds_read_b64_tr_b16 v[238:239], v11 offset:43008\n\t"
      "v_mfma_f32_16x16x32_bf16 v[48:51], v[208:211], v[120:123], v[48:51]\n\t"
      "buffer_load_dwordx4 v[168:171], %[vXsrc], %[srdWS], s50 offen offset:0 sc1\n\t"
      "v_mfma_f32_16x16x32_bf16 v[52:55], v[212:215], v[120:123], v[52:55]\n\t"
      "buffer_load_dwordx4 v[172:175], %[vXsrc], %[srdWS], s50 offen offset:256 sc1\n\t"
      "s_add_u32 s40, s40, 4096\n\t"
      "v_mfma_f32_16x16x32_bf16 v[56:59], v[216:219], v[120:123], v[56:59]\n\t"
      "s_add_u32 m0, s42, 131072\n\t"
      "s_add_u32 s43, s41, 15360\n\t"
      "buffer_load_dwordx4 %[vWsrc], s[44:47], s41 offen lds\n\t"
      "v_mfma_f32_16x16x32_bf16 v[60:63], v[220:223], v[120:123], v[60:63]\n\t"
      "v_mfma_f32_16x16x32_bf16 v[76:79], v[220:223], v[124:127], v[76:79]\n\t"
      "buffer_load_dwordx4 %[vWsrc], s[44:47], s43 offen offset:1024 lds\n\t"
      "s_add_u32 s41, s41, 131072\n\t"
      "v_mfma_f32_16x16x32_bf16 v[72:75], v[216:219], v[124:127], v[72:75]\n\t"
      "s_add_u32 m0, s42, 139264\n\t"
      "s_add_u32 s43, s41, 15360\n\t"
      "buffer_load_dwordx4 %[vWsrc], s[44:47], s41 offen lds\n\t"
      "v_mfma_f32_16x16x32_bf16 v[68:71], v[212:215], v[124:127], v[68:71]\n\t"
      "buffer_load_dwordx4 %[vWsrc], s[44:47], s43 offen offset:1024 lds\n\t"
      "s_add_u32 s41, s41, 131072\n\t"
      "v_mfma_f32_16x16x32_bf16 v[64:67], v[208:211], v[124:127], v[64:67]\n\t"
      "s_waitcnt vmcnt(12) lgkmcnt(0)\n\t"
      "v_mfma_f32_16x16x32_bf16 v[16:19], v[224:227], v[128:131], v[16:19]\n\t"
      "ds_read_b64_tr_b16 v[208:209], v8 offset:49152\n\t"
      "buffer_load_dwordx4 v[176:179], %[vXsrc], %[srdWS], s40 offen offset:0 sc1\n\t"
      "v_mfma_f32_16x16x32_bf16 v[20:23], v[228:231], v[128:131], v[20:23]\n\t"
      "ds_read_b64_tr_b16 v[210:211], v8 offset:51200\n\t"
      "s_add_u32 s50, s40, %[sXo]\n\t"
      "buffer_load_dwordx4 v[180:183], %[vXsrc], %[srdWS], s40 offen offset:256 sc1\n\t"
      "v_mfma_f32_16x16x32_bf16 v[24:27], v[232:235], v[128:131], v[24:27]\n\t"
      "ds_read_b64_tr_b16 v[212:213], v9 offset:49152\n\t"
      "buffer_load_dwordx4 v[184:187], %[vXsrc], %[srdWS], s50 offen offset:0 sc1\n\t"
      "v_mfma_f32_16x16x32_bf16 v[28:31], v[236:239], v[128:131], v[28:31]\n\t"
      "ds_read_b64_tr_b16 v[214:215], v9 offset:51200\n\t"
      "v_mfma_f32_16x16x32_bf16 v[44:47], v[236:239], v[132:135], v[44:47]\n\t"
      "ds_read_b64_tr_b16 v[216:217], v10 offset:49152\n\t"
      "buffer_load_dwordx4 v[188:191], %[vXsrc], %[srdWS], s50 offen offset:256 sc1\n\t"
      "s_add_u32 s40, s40, 4096\n\t"
      "v_mfma_f32_16x16x32_bf16 v[40:43], v[232:235], v[132:135], v[40:43]\n\t"
      "ds_read_b64_tr_b16 v[218:219], v10 offset:51200\n\t"
      "s_add_u32 m0, s42, 147456\n\t"
      "s_add_u32 s43, s41, 15360\n\t"
      "buffer_load_dwordx4 %[vWsrc], s[44:47], s41 offen lds\n\t"
      "v_mfma_f32_16x16x32_bf16 v[36:39], v[228:231], v[132:135], v[36:39]\n\t"
      "ds_read_b64_tr_b16 v[220:221], v11 offset:49152\n\t"
      "buffer_load_dwordx4 %[vWsrc], s[44:47], s43 offen offset:1024 lds\n\t"
      "s_add_u32 s41, s41, 131072\n\t"
      "v_mfma_f32_16x16x32_bf16 v[32:35], v[224:227], v[132:135], v[32:35]\n\t"
      "ds_read_b64_tr_b16 v[222:223], v11 offset:51200\n\t"
      "v_mfma_f32_16x16x32_bf16 v[48:51], v[224:227], v[136:139], v[48:51]\n\t"
      "s_add_u32 m0, s42, 155648\n\t"
      "s_add_u32 s43, s41, 15360\n\t"
      "buffer_load_dwordx4 %[vWsrc], s[44:47], s41 offen lds\n\t"
      "v_mfma_f32_16x16x32_bf16 v[52:55], v[228:231], v[136:139], v[52:55]\n\t"
      "buffer_load_dwordx4 %[vWsrc], s[44:47], s43 offen offset:1024 lds\n\t"
      "s_add_u32 s41, s41, 131072\n\t"
      "v_mfma_f32_16x16x32_bf16 v[56:59], v[232:235], v[136:139], v[56:59]\n\t"
      "s_lshl_b32 s43, s42, 1\n\t"
      "s_add_u32 m0, s43, 0\n\t"
      "s_add_u32 s49, s49, 16384\n\t"
      "s_bitset0_b32 s49, 18\n\t"
      "buffer_load_dwordx4 %[vL16], %[srdWS], s49 offen offset:0 sc1 lds\n\t"
      "v_mfma_f32_16x16x32_bf16 v[60:63], v[236:239], v[136:139], v[60:63]\n\t"
      "v_mfma_f32_16x16x32_bf16 v[76:79], v[236:239], v[140:143], v[76:79]\n\t"
      "buffer_load_dwordx4 %[vL16], %[srdWS], s49 offen offset:1024 sc1 lds\n\t"
      "v_mfma_f32_16x16x32_bf16 v[72:75], v[232:235], v[140:143], v[72:75]\n\t"
      "buffer_load_dwordx4 %[vL16], %[srdWS], s49 offen offset:2048 sc1 lds\n\t"
      "v_mfma_f32_16x16x32_bf16 v[68:71], v[228:231], v[140:143], v[68:71]\n\t"
      "buffer_load_dwordx4 %[vL16], %[srdWS], s49 offen offset:3072 sc1 lds\n\t"
      "v_mfma_f32_16x16x32_bf16 v[64:67], v[224:227], v[140:143], v[64:67]\n\t"
      "s_waitcnt vmcnt(24) lgkmcnt(0)\n\t"
      "s_barrier\n\t"
      "s_waitcnt vmcnt(20) lgkmcnt(0)\n\t"
      "v_mfma_f32_16x16x32_bf16 v[16:19], v[208:211], v[144:147], v[16:19]\n\t"
      "ds_read_b64_tr_b16 v[224:225], v8 offset:57344\n\t"
      "buffer_load_dwordx4 v[192:195], %[vXsrc], %[srdWS], s40 offen offset:0 sc1\n\t"
      "v_mfma_f32_16x16x32_bf16 v[20:23], v[212:215], v[144:147], v[20:23]\n\t"
      "ds_read_b64_tr_b16 v[226:227], v8 offset:59392\n\t"
      "v_mfma_f32_16x16x32_bf16 v[24:27], v[216:219], v[144:147], v[24:27]\n\t"
      "ds_read_b64_tr_b16 v[228:229], v9 offset:57344\n\t"
      "s_add_u32 s50, s40, %[sXo]\n\t"
      "buffer_load_dwordx4 v[196:199], %[vXsrc], %[srdWS], s40 offen offset:256 sc1\n\t"
      "v_mfma_f32_16x16x32_bf16 v[28:31], v[220:223], v[144:147], v[28:31]\n\t"
      "ds_read_b64_tr_b16 v[230:231], v9 offset:59392\n\t"
      "v_mfma_f32_16x16x32_bf16 v[44:47], v[220:223], v[148:151], v[44:47]\n\t"
      "ds_read_b64_tr_b16 v[232:233], v10 offset:57344\n\t"
      "buffer_load_dwordx4 v[200:203], %[vXsrc], %[srdWS], s50 offen offset:0 sc1\n\t"
      "v_mfma_f32_16x16x32_bf16 v[40:43], v[216:219], v[148:151], v[40:43]\n\t"
      "ds_read_b64_tr_b16 v[234:235], v10 offset:59392\n\t"
      "v_mfma_f32_16x16x32_bf16 v[36:39], v[212:215], v[148:151], v[36:39]\n\t"
      "ds_read_b64_tr_b16 v[236:237], v11 offset:57344\n\t"
      "buffer_load_dwordx4 v[204:207], %[vXsrc], %[srdWS], s50 offen offset:256 sc1\n\t"
      "s_add_u32 s40, s40, 4096\n\t"
      "v_mfma_f32_16x16x32_bf16 v[32:35], v[208:211], v[148:151], v[32:35]\n\t"
      "ds_read_b64_tr_b16 v[238:239], v11 offset:59392\n\t"
      "v_mfma_f32_16x16x32_bf16 v[48:51], v[208:211], v[152:155], v[48:51]\n\t"
      "s_add_u32 m0, s42, 32768\n\t"
      "s_add_u32 s43, s41, 15360\n\t"
      "buffer_load_dwordx4 %[vWsrc], s[44:47], s41 offen lds\n\t"
      "v_mfma_f32_16x16x32_bf16 v[52:55], v[212:215], v[152:155], v[52:55]\n\t"
      "v_mfma_f32_16x16x32_bf16 v[56:59], v[216:219], v[152:155], v[56:59]\n\t"
      "buffer_load_dwordx4 %[vWsrc], s[44:47], s43 offen offset:1024 lds\n\t"
      "s_add_u32 s41, s41, 131072\n\t"
      "v_mfma_f32_16x16x32_bf16 v[60:63], v[220:223], v[152:155], v[60:63]\n\t"
      "v_mfma_f32_16x16x32_bf16 v[76:79], v[220:223], v[156:159], v[76:79]\n\t"
      "v_mfma_f32_16x16x32_bf16 v[72:75], v[216:219], v[156:159], v[72:75]\n\t"
      "v_mfma_f32_16x16x32_bf16 v[68:71], v[212:215], v[156:159], v[68:71]\n\t"
      "v_mfma_f32_16x16x32_bf16 v[64:67], v[208:211], v[156:159], v[64:67]\n\t"
      "s_waitcnt vmcnt(22) lgkmcnt(0)\n\t"
      "v_mfma_f32_16x16x32_bf16 v[16:19], v[224:227], v[160:163], v[16:19]\n\t"
      "v_add_u32 v8, 98304, v8\n\t"
      "v_add_u32 v9, 98304, v9\n\t"
      "v_add_u32 v10, 98304, v10\n\t"
      "v_add_u32 v11, 98304, v11\n\t"
      "ds_read_b64_tr_b16 v[208:209], v8 offset:0\n\t"
      "buffer_load_dwordx4 v[80:83], %[vXsrc], %[srdWS], s40 offen offset:0 sc1\n\t"
      "v_mfma_f32_16x16x32_bf16 v[20:23], v[228:231], v[160:163], v[20:23]\n\t"
      "ds_read_b64_tr_b16 v[210:211], v8 offset:2048\n\t"
      "s_add_u32 s50, s40, %[sXo]\n\t"
      "buffer_load_dwordx4 v[84:87], %[vXsrc], %[srdWS], s40 offen offset:256 sc1\n\t"
      "v_mfma_f32_16x16x32_bf16 v[24:27], v[232:235], v[160:163], v[24:27]\n\t"
      "ds_read_b64_tr_b16 v[212:213], v9 offset:0\n\t"
      "v_mfma_f32_16x16x32_bf16 v[28:31], v[236:239], v[160:163], v[28:31]\n\t"
      "ds_read_b64_tr_b16 v[214:215], v9 offset:2048\n\t"
      "buffer_load_dwordx4 v[88:91], %[vXsrc], %[srdWS], s50 offen offset:0 sc1\n\t"
      "v_mfma_f32_16x16x32_bf16 v[44:47], v[236:239], v[164:167], v[44:47]\n\t"
      "ds_read_b64_tr_b16 v[216:217], v10 offset:0\n\t"
      "buffer_load_dwordx4 v[92:95], %[vXsrc], %[srdWS], s50 offen offset:256 sc1\n\t"
      "s_add_u32 s40, s40, 4096\n\t"
      "v_mfma_f32_16x16x32_bf16 v[40:43], v[232:235], v[164:167], v[40:43]\n\t"
      "ds_read_b64_tr_b16 v[218:219], v10 offset:2048\n\t"
      "v_mfma_f32_16x16x32_bf16 v[36:39], v[228:231], v[164:167], v[36:39]\n\t"
      "ds_read_b64_tr_b16 v[220:221], v11 offset:0\n\t"
      "s_add_u32 m0, s42, 40960\n\t"
      "s_add_u32 s43, s41, 15360\n\t"
      "buffer_load_dwordx4 %[vWsrc], s[44:47], s41 offen lds\n\t"
      "v_mfma_f32_16x16x32_bf16 v[32:35], v[224:227], v[164:167], v[32:35]\n\t"
      "ds_read_b64_tr_b16 v[222:223], v11 offset:2048\n\t"
      "v_mfma_f32_16x16x32_bf16 v[48:51], v[224:227], v[168:171], v[48:51]\n\t"
      "buffer_load_dwordx4 %[vWsrc], s[44:47], s43 offen offset:1024 lds\n\t"
      "s_add_u32 s41, s41, 131072\n\t"
      "v_mfma_f32_16x16x32_bf16 v[52:55], v[228:231], v[168:171], v[52:55]\n\t"
      "s_lshl_b32 s43, s42, 1\n\t"
      "s_add_u32 m0, s43, 16384\n\t"
      "s_add_u32 s49, s49, 16384\n\t"
      "s_bitset0_b32 s49, 18\n\t"
      "buffer_load_dwordx4 %[vL16], %[srdWS], s49 offen offset:0 sc1 lds\n\t"
      "v_mfma_f32_16x16x32_bf16 v[56:59], v[232:235], v[168:171], v[56:59]\n\t"
      "v_mfma_f32_16x16x32_bf16 v[60:63], v[236:239], v[168:171], v[60:63]\n\t"
      "buffer_load_dwordx4 %[vL16], %[srdWS], s49 offen offset:1024 sc1 lds\n\t"
      "v_mfma_f32_16x16x32_bf16 v[76:79], v[236:239], v[172:175], v[76:79]\n\t"
      "buffer_load_dwordx4 %[vL16], %[srdWS], s49 offen offset:2048 sc1 lds\n\t"
      "v_mfma_f32_16x16x32_bf16 v[72:75], v[232:235], v[172:175], v[72:75]\n\t"
      "v_mfma_f32_16x16x32_bf16 v[68:71], v[228:231], v[172:175], v[68:71]\n\t"
      "buffer_load_dwordx4 %[vL16], %[srdWS], s49 offen offset:3072 sc1 lds\n\t"
      "v_mfma_f32_16x16x32_bf16 v[64:67], v[224:227], v[172:175], v[64:67]\n\t"
      "s_waitcnt vmcnt(24) lgkmcnt(0)\n\t"
      "v_mfma_f32_16x16x32_bf16 v[16:19], v[208:211], v[176:179], v[16:19]\n\t"
      "ds_read_b64_tr_b16 v[224:225], v8 offset:8192\n\t"
      "buffer_load_dwordx4 v[96:99], %[vXsrc], %[srdWS], s40 offen offset:0 sc1\n\t"
      "v_mfma_f32_16x16x32_bf16 v[20:23], v[212:215], v[176:179], v[20:23]\n\t"
      "ds_read_b64_tr_b16 v[226:227], v8 offset:10240\n\t"
      "v_mfma_f32_16x16x32_bf16 v[24:27], v[216:219], v[176:179], v[24:27]\n\t"
      "ds_read_b64_tr_b16 v[228:229], v9 offset:8192\n\t"
      "s_add_u32 s50, s40, %[sXo]\n\t"
      "buffer_load_dwordx4 v[100:103], %[vXsrc], %[srdWS], s40 offen offset:256 sc1\n\t"
      "v_mfma_f32_16x16x32_bf16 v[28:31], v[220:223], v[176:179], v[28:31]\n\t"
      "ds_read_b64_tr_b16 v[230:231], v9 offset:10240\n\t"
      "v_mfma_f32_16x16x32_bf16 v[44:47], v[220:223], v[180:183], v[44:47]\n\t"
      "ds_read_b64_tr_b16 v[232:233], v10 offset:8192\n\t"
      "buffer_load_dwordx4 v[104:107], %[vXsrc], %[srdWS], s50 offen offset:0 sc1\n\t"
      "v_mfma_f32_16x16x32_bf16 v[40:43], v[216:219], v[180:183], v[40:43]\n\t"
      "ds_read_b64_tr_b16 v[234:235], v10 offset:10240\n\t"
      "v_mfma_f32_16x16x32_bf16 v[36:39], v[212:215], v[180:183], v[36:39]\n\t"
      "ds_read_b64_tr_b16 v[236:237], v11 offset:8192\n\t"
      "buffer_load_dwordx4 v[108:111], %[vXsrc], %[srdWS], s50 offen offset:256 sc1\n\t"
      "s_add_u32 s40, s40, 4096\n\t"
      "v_mfma_f32_16x16x32_bf16 v[32:35], v[208:211], v[180:183], v[32:35]\n\t"
      "ds_read_b64_tr_b16 v[238:239], v11 offset:10240\n\t"
      "v_mfma_f32_16x16x32_bf16 v[48:51], v[208:211], v[184:187], v[48:51]\n\t"
      "s_add_u32 m0, s42, 49152\n\t"
      "s_add_u32 s43, s41, 15360\n\t"
      "buffer_load_dwordx4 %[vWsrc], s[44:47], s41 offen lds\n\t"
      "v_mfma_f32_16x16x32_bf16 v[52:55], v[212:215], v[184:187], v[52:55]\n\t"
      "v_mfma_f32_16x16x32_bf16 v[56:59], v[216:219], v[184:187], v[56:59]\n\t"
      "buffer_load_dwordx4 %[vWsrc], s[44:47], s43 offen offset:1024 lds\n\t"
      "s_add_u32 s41, s41, 131072\n\t"
      "v_mfma_f32_16x16x32_bf16 v[60:63], v[220:223], v[184:187], v[60:63]\n\t"
      "v_mfma_f32_16x16x32_bf16 v[76:79], v[220:223], v[188:191], v[76:79]\n\t"
      "v_mfma_f32_16x16x32_bf16 v[72:75], v[216:219], v[188:191], v[72:75]\n\t"
      "v_mfma_f32_16x16x32_bf16 v[68:71], v[212:215], v[188:191], v[68:71]\n\t"
      "v_mfma_f32_16x16x32_bf16 v[64:67], v[208:211], v[188:191], v[64:67]\n\t"
      "s_waitcnt vmcnt(22) lgkmcnt(0)\n\t"
      "s_barrier\n\t"
      "s_waitcnt vmcnt(18) lgkmcnt(0)\n\t"
      "v_mfma_f32_16x16x32_bf16 v[16:19], v[224:227], v[192:195], v[16:19]\n\t"
      "ds_read_b64_tr_b16 v[208:209], v8 offset:16384\n\t"
      "buffer_load_dwordx4 v[112:115], %[vXsrc], %[srdWS], s40 offen offset:0 sc1\n\t"
      "v_mfma_f32_16x16x32_bf16 v[20:23], v[228:231], v[192:195], v[20:23]\n\t"
      "ds_read_b64_tr_b16 v[210:211], v8 offset:18432\n\t"
      "v_mfma_f32_16x16x32_bf16 v[24:27], v[232:235], v[192:195], v[24:27]\n\t"
      "ds_read_b64_tr_b16 v[212:213], v9 offset:16384\n\t"
      "s_add_u32 s50, s40, %[sXo]\n\t"
      "buffer_load_dwordx4 v[116:119], %[vXsrc], %[srdWS], s40 offen offset:256 sc1\n\t"
      "v_mfma_f32_16x16x32_bf16 v[28:31], v[236:239], v[192:195], v[28:31]\n\t"
      "ds_read_b64_tr_b16 v[214:215], v9 offset:18432\n\t"
      "v_mfma_f32_16x16x32_bf16 v[44:47], v[236:239], v[196:199], v[44:47]\n\t"
      "ds_read_b64_tr_b16 v[216:217], v10 offset:16384\n\t"
      "buffer_load_dwordx4 v[120:123], %[vXsrc], %[srdWS], s50 offen offset:0 sc1\n\t"
      "v_mfma_f32_16x16x32_bf16 v[40:43], v[232:235], v[196:199], v[40:43]\n\t"
      "ds_read_b64_tr_b16 v[218:219], v10 offset:18432\n\t"
      "v_mfma_f32_16x16x32_bf16 v[36:39], v[228:231], v[196:199], v[36:39]\n\t"
      "ds_read_b64_tr_b16 v[220:221], v11 offset:16384\n\t"
      "buffer_load_dwordx4 v[124:127], %[vXsrc], %[srdWS], s50 offen offset:256 sc1\n\t"
      "s_add_u32 s40, s40, 4096\n\t"
      "v_mfma_f32_16x16x32_bf16 v[32:35], v[224:227], v[196:199], v[32:35]\n\t"
      "ds_read_b64_tr_b16 v[222:223], v11 offset:18432\n\t"
      "v_mfma_f32_16x16x32_bf16 v[48:51], v[224:227], v[200:203], v[48:51]\n\t"
      "s_add_u32 m0, s42, 57344\n\t"
      "s_add_u32 s43, s41, 15360\n\t"
      "buffer_load_dwordx4 %[vWsrc], s[44:47], s41 offen lds\n\t"
      "v_mfma_f32_16x16x32_bf16 v[52:55], v[228:231], v[200:203], v[52:55]\n\t"
      "v_mfma_f32_16x16x32_bf16 v[56:59], v[232:235], v[200:203], v[56:59]\n\t"
      "buffer_load_dwordx4 %[vWsrc], s[44:47], s43 offen offset:1024 lds\n\t"
      "s_add_u32 s41, s41, 131072\n\t"
      "v_mfma_f32_16x16x32_bf16 v[60:63], v[236:239], v[200:203], v[60:63]\n\t"
      "v_mfma_f32_16x16x32_bf16 v[76:79], v[236:239], v[204:207], v[76:79]\n\t"
      "v_mfma_f32_16x16x32_bf16 v[72:75], v[232:235], v[204:207], v[72:75]\n\t"
      "v_mfma_f32_16x16x32_bf16 v[68:71], v[228:231], v[204:207], v[68:71]\n\t"
      "v_mfma_f32_16x16x32_bf16 v[64:67], v[224:227], v[204:207], v[64:67]\n\t"
      "s_waitcnt vmcnt(18) lgkmcnt(0)\n\t"
      "v_mfma_f32_16x16x32_bf16 v[16:19], v[208:211], v[80:83], v[16:19]\n\t"
      "ds_read_b64_tr_b16 v[224:225], v8 offset:24576\n\t"
      "buffer_load_dwordx4 v[128:131], %[vXsrc], %[srdWS], s40 offen offset:0 sc1\n\t"
      "v_mfma_f32_16x16x32_bf16 v[20:23], v[212:215], v[80:83], v[20:23]\n\t"
      "ds_read_b64_tr_b16 v[226:227], v8 offset:26624\n\t"
      "s_add_u32 s50, s40, %[sXo]\n\t"
      "buffer_load_dwordx4 v[132:135], %[vXsrc], %[srdWS], s40 offen offset:256 sc1\n\t"
      "v_mfma_f32_16x16x32_bf16 v[24:27], v[216:219], v[80:83], v[24:27]\n\t"
      "ds_read_b64_tr_b16 v[228:229], v9 offset:24576\n\t"
      "v_mfma_f32_16x16x32_bf16 v[28:31], v[220:223], v[80:83], v[28:31]\n\t"
      "ds_read_b64_tr_b16 v[230:231], v9 offset:26624\n\t"
      "buffer_load_dwordx4 v[136:139], %[vXsrc], %[srdWS], s50 offen offset:0 sc1\n\t"
      "v_mfma_f32_16x16x32_bf16 v[44:47], v[220:223], v[84:87], v[44:47]\n\t"
      "ds_read_b64_tr_b16 v[232:233], v10 offset:24576\n\t"
      "buffer_load_dwordx4 v[140:143], %[vXsrc], %[srdWS], s50 offen offset:256 sc1\n\t"
      "s_add_u32 s40, s40, 4096\n\t"
      "v_mfma_f32_16x16x32_bf16 v[40:43], v[216:219], v[84:87], v[40:43]\n\t"
      "ds_read_b64_tr_b16 v[234:235], v10 offset:26624\n\t"
      "v_mfma_f32_16x16x32_bf16 v[36:39], v[212:215], v[84:87], v[36:39]\n\t"
      "ds_read_b64_tr_b16 v[236:237], v11 offset:24576\n\t"
      "s_add_u32 m0, s42, 98304\n\t"
      "s_add_u32 s43, s41, 15360\n\t"
      "buffer_load_dwordx4 %[vWsrc], s[44:47], s41 offen lds\n\t"
      "v_mfma_f32_16x16x32_bf16 v[32:35], v[208:211], v[84:87], v[32:35]\n\t"
      "ds_read_b64_tr_b16 v[238:239], v11 offset:26624\n\t"
      "v_mfma_f32_16x16x32_bf16 v[48:51], v[208:211], v[88:91], v[48:51]\n\t"
      "buffer_load_dwordx4 %[vWsrc], s[44:47], s43 offen offset:1024 lds\n\t"
      "s_add_u32 s41, s41, 131072\n\t"
      "v_mfma_f32_16x16x32_bf16 v[52:55], v[212:215], v[88:91], v[52:55]\n\t"
      "s_lshl_b32 s43, s42, 1\n\t"
      "s_add_u32 m0, s43, 65536\n\t"
      "s_add_u32 s49, s49, 16384\n\t"
      "s_bitset0_b32 s49, 18\n\t"
      "buffer_load_dwordx4 %[vL16], %[srdWS], s49 offen offset:0 sc1 lds\n\t"
      "v_mfma_f32_16x16x32_bf16 v[56:59], v[216:219], v[88:91], v[56:59]\n\t"
      "v_mfma_f32_16x16x32_bf16 v[60:63], v[220:223], v[88:91], v[60:63]\n\t"
      "buffer_load_dwordx4 %[vL16], %[srdWS], s49 offen offset:1024 sc1 lds\n\t"
      "v_mfma_f32_16x16x32_bf16 v[76:79], v[220:223], v[92:95], v[76:79]\n\t"
      "buffer_load_dwordx4 %[vL16], %[srdWS], s49 offen offset:2048 sc1 lds\n\t"
      "v_mfma_f32_16x16x32_bf16 v[72:75], v[216:219], v[92:95], v[72:75]\n\t"
      "v_mfma_f32_16x16x32_bf16 v[68:71], v[212:215], v[92:95], v[68:71]\n\t"
      "buffer_load_dwordx4 %[vL16], %[srdWS], s49 offen offset:3072 sc1 lds\n\t"
      "v_mfma_f32_16x16x32_bf16 v[64:67], v[208:211], v[92:95], v[64:67]\n\t"
      "s_waitcnt vmcnt(18) lgkmcnt(0)\n\t"
      "v_mfma_f32_16x16x32_bf16 v[16:19], v[224:227], v[96:99], v[16:19]\n\t"
      "ds_read_b64_tr_b16 v[208:209], v8 offset:32768\n\t"
      "buffer_load_dwordx4 v[144:147], %[vXsrc], %[srdWS], s40 offen offset:0 sc1\n\t"
      "v_mfma_f32_16x16x32_bf16 v[20:23], v[228:231], v[96:99], v[20:23]\n\t"
      "ds_read_b64_tr_b16 v[210:211], v8 offset:34816\n\t"
      "v_mfma_f32_16x16x32_bf16 v[24:27], v[232:235], v[96:99], v[24:27]\n\t"
      "ds_read_b64_tr_b16 v[212:213], v9 offset:32768\n\t"
      "s_add_u32 s50, s40, %[sXo]\n\t"
      "buffer_load_dwordx4 v[148:151], %[vXsrc], %[srdWS], s40 offen offset:256 sc1\n\t"
      "v_mfma_f32_16x16x32_bf16 v[28:31], v[236:239], v[96:99], v[28:31]\n\t"
      "ds_read_b64_tr_b16 v[214:215], v9 offset:34816\n\t"
      "v_mfma_f32_16x16x32_bf16 v[44:47], v[236:239], v[100:103], v[44:47]\n\t"
      "ds_read_b64_tr_b16 v[216:217], v10 offset:32768\n\t"
      "buffer_load_dwordx4 v[152:155], %[vXsrc], %[srdWS], s50 offen offset:0 sc1\n\t"
      "v_mfma_f32_16x16x32_bf16 v[40:43], v[232:235], v[100:103], v[40:43]\n\t"
      "ds_read_b64_tr_b16 v[218:219], v10 offset:34816\n\t"
      "v_mfma_f32_16x16x32_bf16 v[36:39], v[228:231], v[100:103], v[36:39]\n\t"
      "ds_read_b64_tr_b16 v[220:221], v11 offset:32768\n\t"
      "buffer_load_dwordx4 v[156:159], %[vXsrc], %[srdWS], s50 offen offset:256 sc1\n\t"
      "s_add_u32 s40, s40, 4096\n\t"
      "v_mfma_f32_16x16x32_bf16 v[32:35], v[224:227], v[100:103], v[32:35]\n\t"
      "ds_read_b64_tr_b16 v[222:223], v11 offset:34816\n\t"
      "v_mfma_f32_16x16x32_bf16 v[48:51], v[224:227], v[104:107], v[48:51]\n\t"
      "s_add_u32 m0, s42, 106496\n\t"
      "s_add_u32 s43, s41, 15360\n\t"
      "buffer_load_dwordx4 %[vWsrc], s[44:47], s41 offen lds\n\t"
      "v_mfma_f32_16x16x32_bf16 v[52:55], v[228:231], v[104:107], v[52:55]\n\t"
      "v_mfma_f32_16x16x32_bf16 v[56:59], v[232:235], v[104:107], v[56:59]\n\t"
      "buffer_load_dwordx4 %[vWsrc], s[44:47], s43 offen offset:1024 lds\n\t"
      "s_add_u32 s41, s41, 131072\n\t"
      "v_mfma_f32_16x16x32_bf16 v[60:63], v[236:239], v[104:107], v[60:63]\n\t"
      "v_mfma_f32_16x16x32_bf16 v[76:79], v[236:239], v[108:111], v[76:79]\n\t"
      "v_mfma_f32_16x16x32_bf16 v[72:75], v[232:235], v[108:111], v[72:75]\n\t"
      "v_mfma_f32_16x16x32_bf16 v[68:71], v[228:231], v[108:111], v[68:71]\n\t"
      "v_mfma_f32_16x16x32_bf16 v[64:67], v[224:227], v[108:111], v[64:67]\n\t"
      "s_waitcnt vmcnt(18) lgkmcnt(0)\n\t"
      "v_mfma_f32_16x16x32_bf16 v[16:19], v[208:211], v[112:115], v[16:19]\n\t"
      "ds_read_b64_tr_b16 v[224:225], v8 offset:40960\n\t"
      "buffer_load_dwordx4 v[160:163], %[vXsrc], %[srdWS], s40 offen offset:0 sc1\n\t"
      "v_mfma_f32_16x16x32_bf16 v[20:23], v[212:215], v[112:115], v[20:23]\n\t"
      "ds_read_b64_tr_b16 v[226:227], v8 offset:43008\n\t"
      "v_mfma_f32_16x16x32_bf16 v[24:27], v[216:219], v[112:115], v[24:27]\n\t"
      "ds_read_b64_tr_b16 v[228:229], v9 offset:40960\n\t"
      "s_add_u32 s50, s40, %[sXo]\n\t"
      "buffer_load_dwordx4 v[164:167], %[vXsrc], %[srdWS], s40 offen offset:256 sc1\n\t"
      "v_mfma_f32_16x16x32_bf16 v[28:31], v[220:223], v[112:115], v[28:31]\n\t"
      "ds_read_b64_tr_b16 v[230:231], v9 offset:43008\n\t"
      "v_mfma_f32_16x16x32_bf16 v[44:47], v[220:223], v[116:119], v[44:47]\n\t"
      "ds_read_b64_tr_b16 v[232:233], v10 offset:40960\n\t"
      "buffer_load_dwordx4 v[168:171], %[vXsrc], %[srdWS], s50 offen offset:0 sc1\n\t"
      "v_mfma_f32_16x16x32_bf16 v[40:43], v[216:219], v[116:119], v[40:43]\n\t"
      "ds_read_b64_tr_b16 v[234:235], v10 offset:43008\n\t"
      "v_mfma_f32_16x16x32_bf16 v[36:39], v[212:215], v[116:119], v[36:39]\n\t"
      "ds_read_b64_tr_b16 v[236:237], v11 offset:40960\n\t"
      "buffer_load_dwordx4 v[172:175], %[vXsrc], %[srdWS], s50 offen offset:256 sc1\n\t"
      "s_add_u32 s40, s40, 4096\n\t"
      "v_mfma_f32_16x16x32_bf16 v[32:35], v[208:211], v[116:119], v[32:35]\n\t"
      "ds_read_b64_tr_b16 v[238:239], v11 offset:43008\n\t"
      "v_mfma_f32_16x16x32_bf16 v[48:51], v[208:211], v[120:123], v[48:51]\n\t"
      "buffer_load_dwordx4 v[240:243], %[vO], %[srdR], 0 offen offset:0\n\t"
      "v_mfma_f32_16x16x32_bf16 v[52:55], v[212:215], v[120:123], v[52:55]\n\t"
      "v_mfma_f32_16x16x32_bf16 v[56:59], v[216:219], v[120:123], v[56:59]\n\t"
      "buffer_load_dwordx4 v[244:247], %[vO], %[srdR], 0 offen offset:64\n\t"
      "v_mfma_f32_16x16x32_bf16 v[60:63], v[220:223], v[120:123], v[60:63]\n\t"
      "v_mfma_f32_16x16x32_bf16 v[76:79], v[220:223], v[124:127], v[76:79]\n\t"
      "buffer_load_dwordx4 v[248:251], %[vO], %[srdR], %[s32k] offen offset:0\n\t"
      "v_mfma_f32_16x16x32_bf16 v[72:75], v[216:219], v[124:127], v[72:75]\n\t"
      "v_mfma_f32_16x16x32_bf16 v[68:71], v[212:215], v[124:127], v[68:71]\n\t"
      "buffer_load_dwordx4 v[252:255], %[vO], %[srdR], %[s32k] offen offset:64\n\t"
      "v_mfma_f32_16x16x32_bf16 v[64:67], v[208:211], v[124:127], v[64:67]\n\t"
      "s_waitcnt vmcnt(26) lgkmcnt(0)\n\t"
      "s_barrier\n\t"
      "s_waitcnt vmcnt(20) lgkmcnt(0)\n\t"
      "v_mfma_f32_16x16x32_bf16 v[16:19], v[224:227], v[128:131], v[16:19]\n\t"
      "ds_read_b64_tr_b16 v[208:209], v8 offset:49152\n\t"
      "buffer_load_dwordx4 v[176:179], %[vXsrc], %[srdWS], s40 offen offset:0 sc1\n\t"
      "v_mfma_f32_16x16x32_bf16 v[20:23], v[228:231], v[128:131], v[20:23]\n\t"
      "ds_read_b64_tr_b16 v[210:211], v8 offset:51200\n\t"
      "v_mfma_f32_16x16x32_bf16 v[24:27], v[232:235], v[128:131], v[24:27]\n\t"
      "ds_read_b64_tr_b16 v[212:213], v9 offset:49152\n\t"
      "s_add_u32 s50, s40, %[sXo]\n\t"
      "buffer_load_dwordx4 v[180:183], %[vXsrc], %[srdWS], s40 offen offset:256 sc1\n\t"
      "v_mfma_f32_16x16x32_bf16 v[28:31], v[236:239], v[128:131], v[28:31]\n\t"
      "ds_read_b64_tr_b16 v[214:215], v9 offset:51200\n\t"
      "v_mfma_f32_16x16x32_bf16 v[44:47], v[236:239], v[132:135], v[44:47]\n\t"
      "ds_read_b64_tr_b16 v[216:217], v10 offset:49152\n\t"
      "buffer_load_dwordx4 v[184:187], %[vXsrc], %[srdWS], s50 offen offset:0 sc1\n\t"
      "v_mfma_f32_16x16x32_bf16 v[40:43], v[232:235], v[132:135], v[40:43]\n\t"
      "ds_read_b64_tr_b16 v[218:219], v10 offset:51200\n\t"
      "v_mfma_f32_16x16x32_bf16 v[36:39], v[228:231], v[132:135], v[36:39]\n\t"
      "ds_read_b64_tr_b16 v[220:221], v11 offset:49152\n\t"
      "buffer_load_dwordx4 v[188:191], %[vXsrc], %[srdWS], s50 offen offset:256 sc1\n\t"
      "s_add_u32 s40, s40, 4096\n\t"
      "v_mfma_f32_16x16x32_bf16 v[32:35], v[224:227], v[132:135], v[32:35]\n\t"
      "ds_read_b64_tr_b16 v[222:223], v11 offset:51200\n\t"
      "v_mfma_f32_16x16x32_bf16 v[48:51], v[224:227], v[136:139], v[48:51]\n\t"
      "s_add_u32 m0, s42, 114688\n\t"
      "s_add_u32 s43, s41, 15360\n\t"
      "buffer_load_dwordx4 %[vWsrc], s[44:47], s41 offen lds\n\t"
      "v_mfma_f32_16x16x32_bf16 v[52:55], v[228:231], v[136:139], v[52:55]\n\t"
      "v_mfma_f32_16x16x32_bf16 v[56:59], v[232:235], v[136:139], v[56:59]\n\t"
      "buffer_load_dwordx4 %[vWsrc], s[44:47], s43 offen offset:1024 lds\n\t"
      "s_add_u32 s41, s41, 131072\n\t"
      "v_mfma_f32_16x16x32_bf16 v[60:63], v[236:239], v[136:139], v[60:63]\n\t"
      "v_mfma_f32_16x16x32_bf16 v[76:79], v[236:239], v[140:143], v[76:79]\n\t"
      "s_add_u32 m0, s42, 122880\n\t"
      "s_add_u32 s43, s41, 15360\n\t"
      "buffer_load_dwordx4 %[vWsrc], s[44:47], s41 offen lds\n\t"
      "v_mfma_f32_16x16x32_bf16 v[72:75], v[232:235], v[140:143], v[72:75]\n\t"
      "v_mfma_f32_16x16x32_bf16 v[68:71], v[228:231], v[140:143], v[68:71]\n\t"
      "buffer_load_dwordx4 %[vWsrc], s[44:47], s43 offen offset:1024 lds\n\t"
      "s_add_u32 s41, s41, 131072\n\t"
      "v_mfma_f32_16x16x32_bf16 v[64:67], v[224:227], v[140:143], v[64:67]\n\t"
      "s_waitcnt vmcnt(18) lgkmcnt(0)\n\t"
      "v_mfma_f32_16x16x32_bf16 v[16:19], v[208:211], v[144:147], v[16:19]\n\t"
      "ds_read_b64_tr_b16 v[224:225], v8 offset:57344\n\t"
      "buffer_load_dwordx4 v[192:195], %[vXsrc], %[srdWS], s40 offen offset:0 sc1\n\t"
      "v_mfma_f32_16x16x32_bf16 v[20:23], v[212:215], v[144:147], v[20:23]\n\t"
      "ds_read_b64_tr_b16 v[226:227], v8 offset:59392\n\t"
      "s_add_u32 s50, s40, %[sXo]\n\t"
      "buffer_load_dwordx4 v[196:199], %[vXsrc], %[srdWS], s40 offen offset:256 sc1\n\t"
      "v_mfma_f32_16x16x32_bf16 v[24:27], v[216:219], v[144:147], v[24:27]\n\t"
      "ds_read_b64_tr_b16 v[228:229], v9 offset:57344\n\t"
      "v_mfma_f32_16x16x32_bf16 v[28:31], v[220:223], v[144:147], v[28:31]\n\t"
      "ds_read_b64_tr_b16 v[230:231], v9 offset:59392\n\t"
      "buffer_load_dwordx4 v[200:203], %[vXsrc], %[srdWS], s50 offen offset:0 sc1\n\t"
      "v_mfma_f32_16x16x32_bf16 v[44:47], v[220:223], v[148:151], v[44:47]\n\t"
      "ds_read_b64_tr_b16 v[232:233], v10 offset:57344\n\t"
      "buffer_load_dwordx4 v[204:207], %[vXsrc], %[srdWS], s50 offen offset:256 sc1\n\t"
      "s_add_u32 s40, s40, 4096\n\t"
      "v_mfma_f32_16x16x32_bf16 v[40:43], v[216:219], v[148:151], v[40:43]\n\t"
      "ds_read_b64_tr_b16 v[234:235], v10 offset:59392\n\t"
      "v_mfma_f32_16x16x32_bf16 v[36:39], v[212:215], v[148:151], v[36:39]\n\t"
      "ds_read_b64_tr_b16 v[236:237], v11 offset:57344\n\t"
      "s_add_u32 m0, s42, 131072\n\t"
      "s_add_u32 s43, s41, 15360\n\t"
      "buffer_load_dwordx4 %[vWsrc], s[44:47], s41 offen lds\n\t"
      "v_mfma_f32_16x16x32_bf16 v[32:35], v[208:211], v[148:151], v[32:35]\n\t"
      "ds_read_b64_tr_b16 v[238:239], v11 offset:59392\n\t"
      "v_mfma_f32_16x16x32_bf16 v[48:51], v[208:211], v[152:155], v[48:51]\n\t"
      "buffer_load_dwordx4 %[vWsrc], s[44:47], s43 offen offset:1024 lds\n\t"
      "s_add_u32 s41, s41, 131072\n\t"
      "v_mfma_f32_16x16x32_bf16 v[52:55], v[212:215], v[152:155], v[52:55]\n\t"
      "s_lshl_b32 s43, s42, 1\n\t"
      "s_add_u32 m0, s43, 81920\n\t"
      "s_add_u32 s49, s49, 16384\n\t"
      "s_bitset0_b32 s49, 18\n\t"
      "buffer_load_dwordx4 %[vL16], %[srdWS], s49 offen offset:0 sc1 lds\n\t"
      "v_mfma_f32_16x16x32_bf16 v[56:59], v[216:219], v[152:155], v[56:59]\n\t"
      "v_mfma_f32_16x16x32_bf16 v[60:63], v[220:223], v[152:155], v[60:63]\n\t"
      "buffer_load_dwordx4 %[vL16], %[srdWS], s49 offen offset:1024 sc1 lds\n\t"
      "v_mfma_f32_16x16x32_bf16 v[76:79], v[220:223], v[156:159], v[76:79]\n\t"
      "buffer_load_dwordx4 %[vL16], %[srdWS], s49 offen offset:2048 sc1 lds\n\t"
      "v_mfma_f32_16x16x32_bf16 v[72:75], v[216:219], v[156:159], v[72:75]\n\t"
      "v_mfma_f32_16x16x32_bf16 v[68:71], v[212:215], v[156:159], v[68:71]\n\t"
      "buffer_load_dwordx4 %[vL16], %[srdWS], s49 offen offset:3072 sc1 lds\n\t"
      "v_mfma_f32_16x16x32_bf16 v[64:67], v[208:211], v[156:159], v[64:67]\n\t"
      "s_waitcnt vmcnt(22) lgkmcnt(0)\n\t"
      "v_mfma_f32_16x16x32_bf16 v[16:19], v[224:227], v[160:163], v[16:19]\n\t"
      "v_add_u32 v8, 4294901760, v8\n\t"
      "v_add_u32 v9, 4294901760, v9\n\t"
      "v_add_u32 v10, 4294901760, v10\n\t"
      "v_add_u32 v11, 4294901760, v11\n\t"
      "ds_read_b64_tr_b16 v[208:209], v8 offset:0\n\t"
      "buffer_load_dwordx4 v[80:83], %[vXsrc], %[srdWS], s40 offen offset:0 sc1\n\t"
      "v_mfma_f32_16x16x32_bf16 v[20:23], v[228:231], v[160:163], v[20:23]\n\t"
      "ds_read_b64_tr_b16 v[210:211], v8 offset:2048\n\t"
      "v_mfma_f32_16x16x32_bf16 v[24:27], v[232:235], v[160:163], v[24:27]\n\t"
      "ds_read_b64_tr_b16 v[212:213], v9 offset:0\n\t"
      "s_add_u32 s50, s40, %[sXo]\n\t"
      "buffer_load_dwordx4 v[84:87], %[vXsrc], %[srdWS], s40 offen offset:256 sc1\n\t"
      "v_mfma_f32_16x16x32_bf16 v[28:31], v[236:239], v[160:163], v[28:31]\n\t"
      "ds_read_b64_tr_b16 v[214:215], v9 offset:2048\n\t"
      "v_mfma_f32_16x16x32_bf16 v[44:47], v[236:239], v[164:167], v[44:47]\n\t"
      "ds_read_b64_tr_b16 v[216:217], v10 offset:0\n\t"
      "buffer_load_dwordx4 v[88:91], %[vXsrc], %[srdWS], s50 offen offset:0 sc1\n\t"
      "v_mfma_f32_16x16x32_bf16 v[40:43], v[232:235], v[164:167], v[40:43]\n\t"
      "ds_read_b64_tr_b16 v[218:219], v10 offset:2048\n\t"
      "v_mfma_f32_16x16x32_bf16 v[36:39], v[228:231], v[164:167], v[36:39]\n\t"
      "ds_read_b64_tr_b16 v[220:221], v11 offset:0\n\t"
      "buffer_load_dwordx4 v[92:95], %[vXsrc], %[srdWS], s50 offen offset:256 sc1\n\t"
      "s_add_u32 s40, s40, 4096\n\t"
      "v_mfma_f32_16x16x32_bf16 v[32:35], v[224:227], v[164:167], v[32:35]\n\t"
      "ds_read_b64_tr_b16 v[222:223], v11 offset:2048\n\t"
      "v_mfma_f32_16x16x32_bf16 v[48:51], v[224:227], v[168:171], v[48:51]\n\t"
      "s_add_u32 m0, s42, 139264\n\t"
      "s_add_u32 s43, s41, 15360\n\t"
      "buffer_load_dwordx4 %[vWsrc], s[44:47], s41 offen lds\n\t"
      "v_mfma_f32_16x16x32_bf16 v[52:55], v[228:231], v[168:171], v[52:55]\n\t"
      "v_mfma_f32_16x16x32_bf16 v[56:59], v[232:235], v[168:171], v[56:59]\n\t"
      "buffer_load_dwordx4 %[vWsrc], s[44:47], s43 offen offset:1024 lds\n\t"
      "s_add_u32 s41, s41, 131072\n\t"
      "v_mfma_f32_16x16x32_bf16 v[60:63], v[236:239], v[168:171], v[60:63]\n\t"
      "v_mfma_f32_16x16x32_bf16 v[76:79], v[236:239], v[172:175], v[76:79]\n\t"
      "v_mfma_f32_16x16x32_bf16 v[72:75], v[232:235], v[172:175], v[72:75]\n\t"
      "v_mfma_f32_16x16x32_bf16 v[68:71], v[228:231], v[172:175], v[68:71]\n\t"
      "v_mfma_f32_16x16x32_bf16 v[64:67], v[224:227], v[172:175], v[64:67]\n\t"
      "s_waitcnt vmcnt(28) lgkmcnt(0)\n\t"
      "s_barrier\n\t"
      "s_waitcnt vmcnt(20) lgkmcnt(0)\n\t"
      "v_mfma_f32_16x16x32_bf16 v[16:19], v[208:211], v[176:179], v[16:19]\n\t"
      "ds_read_b64_tr_b16 v[224:225], v8 offset:8192\n\t"
      "buffer_load_dwordx4 v[96:99], %[vXsrc], %[srdWS], s40 offen offset:0 sc1\n\t"
      "v_mfma_f32_16x16x32_bf16 v[20:23], v[212:215], v[176:179], v[20:23]\n\t"
      "ds_read_b64_tr_b16 v[226:227], v8 offset:10240\n\t"
      "v_mfma_f32_16x16x32_bf16 v[24:27], v[216:219], v[176:179], v[24:27]\n\t"
      "ds_read_b64_tr_b16 v[228:229], v9 offset:8192\n\t"
      "s_add_u32 s50, s40, %[sXo]\n\t"
      "buffer_load_dwordx4 v[100:103], %[vXsrc], %[srdWS], s40 offen offset:256 sc1\n\t"
      "v_mfma_f32_16x16x32_bf16 v[28:31], v[220:223], v[176:179], v[28:31]\n\t"
      "ds_read_b64_tr_b16 v[230:231], v9 offset:10240\n\t"
      "v_mfma_f32_16x16x32_bf16 v[44:47], v[220:223], v[180:183], v[44:47]\n\t"
      "ds_read_b64_tr_b16 v[232:233], v10 offset:8192\n\t"
      "buffer_load_dwordx4 v[104:107], %[vXsrc], %[srdWS], s50 offen offset:0 sc1\n\t"
      "v_mfma_f32_16x16x32_bf16 v[40:43], v[216:219], v[180:183], v[40:43]\n\t"
      "ds_read_b64_tr_b16 v[234:235], v10 offset:10240\n\t"
      "v_mfma_f32_16x16x32_bf16 v[36:39], v[212:215], v[180:183], v[36:39]\n\t"
      "ds_read_b64_tr_b16 v[236:237], v11 offset:8192\n\t"
      "buffer_load_dwordx4 v[108:111], %[vXsrc], %[srdWS], s50 offen offset:256 sc1\n\t"
      "s_add_u32 s40, s40, 4096\n\t"
      "v_mfma_f32_16x16x32_bf16 v[32:35], v[208:211], v[180:183], v[32:35]\n\t"
      "ds_read_b64_tr_b16 v[238:239], v11 offset:10240\n\t"
      "v_mfma_f32_16x16x32_bf16 v[48:51], v[208:211], v[184:187], v[48:51]\n\t"
      "s_add_u32 m0, s42, 147456\n\t"
      "s_add_u32 s43, s41, 15360\n\t"
      "buffer_load_dwordx4 %[vWsrc], s[44:47], s41 offen lds\n\t"
      "v_mfma_f32_16x16x32_bf16 v[52:55], v[212:215], v[184:187], v[52:55]\n\t"
      "v_mfma_f32_16x16x32_bf16 v[56:59], v[216:219], v[184:187], v[56:59]\n\t"
      "buffer_load_dwordx4 %[vWsrc], s[44:47], s43 offen offset:1024 lds\n\t"
      "s_add_u32 s41, s41, 131072\n\t"
      "v_mfma_f32_16x16x32_bf16 v[60:63], v[220:223], v[184:187], v[60:63]\n\t"
      "v_mfma_f32_16x16x32_bf16 v[76:79], v[220:223], v[188:191], v[76:79]\n\t"
      "v_mfma_f32_16x16x32_bf16 v[72:75], v[216:219], v[188:191], v[72:75]\n\t"
      "v_mfma_f32_16x16x32_bf16 v[68:71], v[212:215], v[188:191], v[68:71]\n\t"
      "v_mfma_f32_16x16x32_bf16 v[64:67], v[208:211], v[188:191], v[64:67]\n\t"
      "s_waitcnt vmcnt(18) lgkmcnt(0)\n\t"
      "v_mfma_f32_16x16x32_bf16 v[16:19], v[224:227], v[192:195], v[16:19]\n\t"
      "ds_read_b64_tr_b16 v[208:209], v8 offset:16384\n\t"
      "buffer_load_dwordx4 v[112:115], %[vXsrc], %[srdWS], s40 offen offset:0 sc1\n\t"
      "v_mfma_f32_16x16x32_bf16 v[20:23], v[228:231], v[192:195], v[20:23]\n\t"
      "ds_read_b64_tr_b16 v[210:211], v8 offset:18432\n\t"
      "s_add_u32 s50, s40, %[sXo]\n\t"
      "buffer_load_dwordx4 v[116:119], %[vXsrc], %[srdWS], s40 offen offset:256 sc1\n\t"
      "v_mfma_f32_16x16x32_bf16 v[24:27], v[232:235], v[192:195], v[24:27]\n\t"
      "ds_read_b64_tr_b16 v[212:213], v9 offset:16384\n\t"
      "v_mfma_f32_16x16x32_bf16 v[28:31], v[236:239], v[192:195], v[28:31]\n\t"
      "ds_read_b64_tr_b16 v[214:215], v9 offset:18432\n\t"
      "buffer_load_dwordx4 v[120:123], %[vXsrc], %[srdWS], s50 offen offset:0 sc1\n\t"
      "v_mfma_f32_16x16x32_bf16 v[44:47], v[236:239], v[196:199], v[44:47]\n\t"
      "ds_read_b64_tr_b16 v[216:217], v10 offset:16384\n\t"
      "buffer_load_dwordx4 v[124:127], %[vXsrc], %[srdWS], s50 offen offset:256 sc1\n\t"
      "s_add_u32 s40, s40, 4096\n\t"
      "v_mfma_f32_16x16x32_bf16 v[40:43], v[232:235], v[196:199], v[40:43]\n\t"
      "ds_read_b64_tr_b16 v[218:219], v10 offset:18432\n\t"
      "v_mfma_f32_16x16x32_bf16 v[36:39], v[228:231], v[196:199], v[36:39]\n\t"
      "ds_read_b64_tr_b16 v[220:221], v11 offset:16384\n\t"
      "s_add_u32 m0, s42, 155648\n\t"
      "s_add_u32 s43, s41, 15360\n\t"
      "buffer_load_dwordx4 %[vWsrc], s[44:47], s41 offen lds\n\t"
      "v_mfma_f32_16x16x32_bf16 v[32:35], v[224:227], v[196:199], v[32:35]\n\t"
      "ds_read_b64_tr_b16 v[222:223], v11 offset:18432\n\t"
      "v_mfma_f32_16x16x32_bf16 v[48:51], v[224:227], v[200:203], v[48:51]\n\t"
      "buffer_load_dwordx4 %[vWsrc], s[44:47], s43 offen offset:1024 lds\n\t"
      "s_add_u32 s41, s41, 131072\n\t"
      "v_mfma_f32_16x16x32_bf16 v[52:55], v[228:231], v[200:203], v[52:55]\n\t"
      "s_lshl_b32 s43, s42, 1\n\t"
      "s_add_u32 m0, s43, 0\n\t"
      "s_add_u32 s49, s49, 16384\n\t"
      "s_bitset0_b32 s49, 18\n\t"
      "buffer_load_dwordx4 %[vL16], %[srdWS], s49 offen offset:0 sc1 lds\n\t"
      "v_mfma_f32_16x16x32_bf16 v[56:59], v[232:235], v[200:203], v[56:59]\n\t"
      "v_mfma_f32_16x16x32_bf16 v[60:63], v[236:239], v[200:203], v[60:63]\n\t"
      "buffer_load_dwordx4 %[vL16], %[srdWS], s49 offen offset:1024 sc1 lds\n\t"
      "v_mfma_f32_16x16x32_bf16 v[76:79], v[236:239], v[204:207], v[76:79]\n\t"
      "buffer_load_dwordx4 %[vL16], %[srdWS], s49 offen offset:2048 sc1 lds\n\t"
      "v_mfma_f32_16x16x32_bf16 v[72:75], v[232:235], v[204:207], v[72:75]\n\t"
      "v_mfma_f32_16x16x32_bf16 v[68:71], v[228:231], v[204:207], v[68:71]\n\t"
      "buffer_load_dwordx4 %[vL16], %[srdWS], s49 offen offset:3072 sc1 lds\n\t"
      "v_mfma_f32_16x16x32_bf16 v[64:67], v[224:227], v[204:207], v[64:67]\n\t"
      "s_waitcnt vmcnt(18) lgkmcnt(0)\n\t"
      "v_mfma_f32_16x16x32_bf16 v[16:19], v[208:211], v[80:83], v[16:19]\n\t"
      "ds_read_b64_tr_b16 v[224:225], v8 offset:24576\n\t"
      "buffer_load_dwordx4 v[128:131], %[vXsrc], %[srdWS], s40 offen offset:0 sc1\n\t"
      "v_mfma_f32_16x16x32_bf16 v[20:23], v[212:215], v[80:83], v[20:23]\n\t"
      "ds_read_b64_tr_b16 v[226:227], v8 offset:26624\n\t"
      "v_mfma_f32_16x16x32_bf16 v[24:27], v[216:219], v[80:83], v[24:27]\n\t"
      "ds_read_b64_tr_b16 v[228:229], v9 offset:24576\n\t"
      "s_add_u32 s50, s40, %[sXo]\n\t"
      "buffer_load_dwordx4 v[132:135], %[vXsrc], %[srdWS], s40 offen offset:256 sc1\n\t"
      "v_mfma_f32_16x16x32_bf16 v[28:31], v[220:223], v[80:83], v[28:31]\n\t"
      "ds_read_b64_tr_b16 v[230:231], v9 offset:26624\n\t"
      "v_mfma_f32_16x16x32_bf16 v[44:47], v[220:223], v[84:87], v[44:47]\n\t"
      "ds_read_b64_tr_b16 v[232:233], v10 offset:24576\n\t"
      "buffer_load_dwordx4 v[136:139], %[vXsrc], %[srdWS], s50 offen offset:0 sc1\n\t"
      "v_mfma_f32_16x16x32_bf16 v[40:43], v[216:219], v[84:87], v[40:43]\n\t"
      "ds_read_b64_tr_b16 v[234:235], v10 offset:26624\n\t"
      "v_mfma_f32_16x16x32_bf16 v[36:39], v[212:215], v[84:87], v[36:39]\n\t"
      "ds_read_b64_tr_b16 v[236:237], v11 offset:24576\n\t"
      "buffer_load_dwordx4 v[140:143], %[vXsrc], %[srdWS], s50 offen offset:256 sc1\n\t"
      "s_add_u32 s40, s40, 4096\n\t"
      "v_mfma_f32_16x16x32_bf16 v[32:35], v[208:211], v[84:87], v[32:35]\n\t"
      "ds_read_b64_tr_b16 v[238:239], v11 offset:26624\n\t"
      "v_mfma_f32_16x16x32_bf16 v[48:51], v[208:211], v[88:91], v[48:51]\n\t"
      "s_add_u32 m0, s42, 32768\n\t"
      "s_add_u32 s43, s41, 15360\n\t"
      "buffer_load_dwordx4 %[vWsrc], s[44:47], s41 offen lds\n\t"
      "v_mfma_f32_16x16x32_bf16 v[52:55], v[212:215], v[88:91], v[52:55]\n\t"
      "v_mfma_f32_16x16x32_bf16 v[56:59], v[216:219], v[88:91], v[56:59]\n\t"
      "buffer_load_dwordx4 %[vWsrc], s[44:47], s43 offen offset:1024 lds\n\t"
      "s_add_u32 s41, s41, 131072\n\t"
      "v_mfma_f32_16x16x32_bf16 v[60:63], v[220:223], v[88:91], v[60:63]\n\t"
      "v_mfma_f32_16x16x32_bf16 v[76:79], v[220:223], v[92:95], v[76:79]\n\t"
      "v_mfma_f32_16x16x32_bf16 v[72:75], v[216:219], v[92:95], v[72:75]\n\t"
      "v_mfma_f32_16x16x32_bf16 v[68:71], v[212:215], v[92:95], v[68:71]\n\t"
      "v_mfma_f32_16x16x32_bf16 v[64:67], v[208:211], v[92:95], v[64:67]\n\t"
      "s_waitcnt vmcnt(18) lgkmcnt(0)\n\t"
      "v_mfma_f32_16x16x32_bf16 v[16:19], v[224:227], v[96:99], v[16:19]\n\t"
      "v_add_u32 v8, 65536, v8\n\t"
      "v_add_u32 v9, 65536, v9\n\t"
      "v_add_u32 v10, 65536, v10\n\t"
      "v_add_u32 v11, 65536, v11\n\t"
      "ds_read_b64_tr_b16 v[208:209], v8 offset:0\n\t"
      "buffer_load_dwordx4 v[144:147], %[vXsrc], %[srdWS], s40 offen offset:0 sc1\n\t"
      "v_mfma_f32_16x16x32_bf16 v[20:23], v[228:231], v[96:99], v[20:23]\n\t"
      "ds_read_b64_tr_b16 v[210:211], v8 offset:2048\n\t"
      "v_mfma_f32_16x16x32_bf16 v[24:27], v[232:235], v[96:99], v[24:27]\n\t"
      "ds_read_b64_tr_b16 v[212:213], v9 offset:0\n\t"
      "s_add_u32 s50, s40, %[sXo]\n\t"
      "buffer_load_dwordx4 v[148:151], %[vXsrc], %[srdWS], s40 offen offset:256 sc1\n\t"
      "v_mfma_f32_16x16x32_bf16 v[28:31], v[236:239], v[96:99], v[28:31]\n\t"
      "ds_read_b64_tr_b16 v[214:215], v9 offset:2048\n\t"
      "v_mfma_f32_16x16x32_bf16 v[44:47], v[236:239], v[100:103], v[44:47]\n\t"
      "ds_read_b64_tr_b16 v[216:217], v10 offset:0\n\t"
      "buffer_load_dwordx4 v[152:155], %[vXsrc], %[srdWS], s50 offen offset:0 sc1\n\t"
      "v_mfma_f32_16x16x32_bf16 v[40:43], v[232:235], v[100:103], v[40:43]\n\t"
      "ds_read_b64_tr_b16 v[218:219], v10 offset:2048\n\t"
      "v_mfma_f32_16x16x32_bf16 v[36:39], v[228:231], v[100:103], v[36:39]\n\t"
      "ds_read_b64_tr_b16 v[220:221], v11 offset:0\n\t"
      "buffer_load_dwordx4 v[156:159], %[vXsrc], %[srdWS], s50 offen offset:256 sc1\n\t"
      "s_add_u32 s40, s40, 4096\n\t"
      "v_mfma_f32_16x16x32_bf16 v[32:35], v[224:227], v[100:103], v[32:35]\n\t"
      "ds_read_b64_tr_b16 v[222:223], v11 offset:2048\n\t"
      "v_mfma_f32_16x16x32_bf16 v[48:51], v[224:227], v[104:107], v[48:51]\n\t"
      "v_mfma_f32_16x16x32_bf16 v[52:55], v[228:231], v[104:107], v[52:55]\n\t"
      "v_mfma_f32_16x16x32_bf16 v[56:59], v[232:235], v[104:107], v[56:59]\n\t"
      "v_mfma_f32_16x16x32_bf16 v[60:63], v[236:239], v[104:107], v[60:63]\n\t"
      "v_mfma_f32_16x16x32_bf16 v[76:79], v[236:239], v[108:111], v[76:79]\n\t"
      "v_mfma_f32_16x16x32_bf16 v[72:75], v[232:235], v[108:111], v[72:75]\n\t"
      "v_mfma_f32_16x16x32_bf16 v[68:71], v[228:231], v[108:111], v[68:71]\n\t"
      "v_mfma_f32_16x16x32_bf16 v[64:67], v[224:227], v[108:111], v[64:67]\n\t"
      "s_waitcnt vmcnt(22) lgkmcnt(0)\n\t"
      "s_barrier\n\t"
      "s_waitcnt vmcnt(16) lgkmcnt(0)\n\t"
      "v_mfma_f32_16x16x32_bf16 v[16:19], v[208:211], v[112:115], v[16:19]\n\t"
      "ds_read_b64_tr_b16 v[224:225], v8 offset:8192\n\t"
      "buffer_load_dwordx4 v[160:163], %[vXsrc], %[srdWS], s40 offen offset:0 sc1\n\t"
      "v_mfma_f32_16x16x32_bf16 v[20:23], v[212:215], v[112:115], v[20:23]\n\t"
      "ds_read_b64_tr_b16 v[226:227], v8 offset:10240\n\t"
      "v_mfma_f32_16x16x32_bf16 v[24:27], v[216:219], v[112:115], v[24:27]\n\t"
      "ds_read_b64_tr_b16 v[228:229], v9 offset:8192\n\t"
      "s_add_u32 s50, s40, %[sXo]\n\t"
      "buffer_load_dwordx4 v[164:167], %[vXsrc], %[srdWS], s40 offen offset:256 sc1\n\t"
      "v_mfma_f32_16x16x32_bf16 v[28:31], v[220:223], v[112:115], v[28:31]\n\t"
      "ds_read_b64_tr_b16 v[230:231], v9 offset:10240\n\t"
      "v_mfma_f32_16x16x32_bf16 v[44:47], v[220:223], v[116:119], v[44:47]\n\t"
      "ds_read_b64_tr_b16 v[232:233], v10 offset:8192\n\t"
      "buffer_load_dwordx4 v[168:171], %[vXsrc], %[srdWS], s50 offen offset:0 sc1\n\t"
      "v_mfma_f32_16x16x32_bf16 v[40:43], v[216:219], v[116:119], v[40:43]\n\t"
      "ds_read_b64_tr_b16 v[234:235], v10 offset:10240\n\t"
      "v_mfma_f32_16x16x32_bf16 v[36:39], v[212:215], v[116:119], v[36:39]\n\t"
      "ds_read_b64_tr_b16 v[236:237], v11 offset:8192\n\t"
      "buffer_load_dwordx4 v[172:175], %[vXsrc], %[srdWS], s50 offen offset:256 sc1\n\t"
      "s_add_u32 s40, s40, 4096\n\t"
      "v_mfma_f32_16x16x32_bf16 v[32:35], v[208:211], v[116:119], v[32:35]\n\t"
      "ds_read_b64_tr_b16 v[238:239], v11 offset:10240\n\t"
      "v_mfma_f32_16x16x32_bf16 v[48:51], v[208:211], v[120:123], v[48:51]\n\t"
      "s_add_u32 m0, s42, 40960\n\t"
      "s_add_u32 s43, s41, 15360\n\t"
      "buffer_load_dwordx4 %[vWsrc], s[44:47], s41 offen lds\n\t"
      "v_mfma_f32_16x16x32_bf16 v[52:55], v[212:215], v[120:123], v[52:55]\n\t"
      "v_mfma_f32_16x16x32_bf16 v[56:59], v[216:219], v[120:123], v[56:59]\n\t"
      "buffer_load_dwordx4 %[vWsrc], s[44:47], s43 offen offset:1024 lds\n\t"
      "s_add_u32 s41, s41, 131072\n\t"
      "v_mfma_f32_16x16x32_bf16 v[60:63], v[220:223], v[120:123], v[60:63]\n\t"
      "v_mfma_f32_16x16x32_bf16 v[76:79], v[220:223], v[124:127], v[76:79]\n\t"
      "s_add_u32 m0, s42, 49152\n\t"
      "s_add_u32 s43, s41, 15360\n\t"
      "buffer_load_dwordx4 %[vWsrc], s[44:47], s41 offen lds\n\t"
      "v_mfma_f32_16x16x32_bf16 v[72:75], v[216:219], v[124:127], v[72:75]\n\t"
      "v_mfma_f32_16x16x32_bf16 v[68:71], v[212:215], v[124:127], v[68:71]\n\t"
      "buffer_load_dwordx4 %[vWsrc], s[44:47], s43 offen offset:1024 lds\n\t"
      "s_add_u32 s41, s41, 131072\n\t"
      "v_mfma_f32_16x16x32_bf16 v[64:67], v[208:211], v[124:127], v[64:67]\n\t"
      "s_waitcnt vmcnt(14) lgkmcnt(0)\n\t"
      "v_mfma_f32_16x16x32_bf16 v[16:19], v[224:227], v[128:131], v[16:19]\n\t"
      "ds_read_b64_tr_b16 v[208:209], v8 offset:16384\n\t"
      "buffer_load_dwordx4 v[176:179], %[vXsrc], %[srdWS], s40 offen offset:0 sc1\n\t"
      "v_mfma_f32_16x16x32_bf16 v[20:23], v[228:231], v[128:131], v[20:23]\n\t"
      "ds_read_b64_tr_b16 v[210:211], v8 offset:18432\n\t"
      "s_add_u32 s50, s40, %[sXo]\n\t"
      "buffer_load_dwordx4 v[180:183], %[vXsrc], %[srdWS], s40 offen offset:256 sc1\n\t"
      "v_mfma_f32_16x16x32_bf16 v[24:27], v[232:235], v[128:131], v[24:27]\n\t"
      "ds_read_b64_tr_b16 v[212:213], v9 offset:16384\n\t"
      "v_mfma_f32_16x16x32_bf16 v[28:31], v[236:239], v[128:131], v[28:31]\n\t"
      "ds_read_b64_tr_b16 v[214:215], v9 offset:18432\n\t"
      "buffer_load_dwordx4 v[184:187], %[vXsrc], %[srdWS], s50 offen offset:0 sc1\n\t"
      "v_mfma_f32_16x16x32_bf16 v[44:47], v[236:239], v[132:135], v[44:47]\n\t"
      "ds_read_b64_tr_b16 v[216:217], v10 offset:16384\n\t"
      "buffer_load_dwordx4 v[188:191], %[vXsrc], %[srdWS], s50 offen offset:256 sc1\n\t"
      "s_add_u32 s40, s40, 4096\n\t"
      "v_mfma_f32_16x16x32_bf16 v[40:43], v[232:235], v[132:135], v[40:43]\n\t"
      "ds_read_b64_tr_b16 v[218:219], v10 offset:18432\n\t"
      "v_mfma_f32_16x16x32_bf16 v[36:39], v[228:231], v[132:135], v[36:39]\n\t"
      "ds_read_b64_tr_b16 v[220:221], v11 offset:16384\n\t"
      "s_add_u32 m0, s42, 57344\n\t"
      "s_add_u32 s43, s41, 15360\n\t"
      "buffer_load_dwordx4 %[vWsrc], s[44:47], s41 offen lds\n\t"
      "v_mfma_f32_16x16x32_bf16 v[32:35], v[224:227], v[132:135], v[32:35]\n\t"
      "ds_read_b64_tr_b16 v[222:223], v11 offset:18432\n\t"
      "v_mfma_f32_16x16x32_bf16 v[48:51], v[224:227], v[136:139], v[48:51]\n\t"
      "buffer_load_dwordx4 %[vWsrc], s[44:47], s43 offen offset:1024 lds\n\t"
      "s_add_u32 s41, s41, 131072\n\t"
      "v_mfma_f32_16x16x32_bf16 v[52:55], v[228:231], v[136:139], v[52:55]\n\t"
      "s_lshl_b32 s43, s42, 1\n\t"
      "s_add_u32 m0, s43, 16384\n\t"
      "s_add_u32 s49, s49, 16384\n\t"
      "s_bitset0_b32 s49, 18\n\t"
      "buffer_load_dwordx4 %[vL16], %[srdWS], s49 offen offset:0 sc1 lds\n\t"
      "v_mfma_f32_16x16x32_bf16 v[56:59], v[232:235], v[136:139], v[56:59]\n\t"
      "v_mfma_f32_16x16x32_bf16 v[60:63], v[236:239], v[136:139], v[60:63]\n\t"
      "buffer_load_dwordx4 %[vL16], %[srdWS], s49 offen offset:1024 sc1 lds\n\t"
      "v_mfma_f32_16x16x32_bf16 v[76:79], v[236:239], v[140:143], v[76:79]\n\t"
      "buffer_load_dwordx4 %[vL16], %[srdWS], s49 offen offset:2048 sc1 lds\n\t"
      "v_mfma_f32_16x16x32_bf16 v[72:75], v[232:235], v[140:143], v[72:75]\n\t"
      "v_mfma_f32_16x16x32_bf16 v[68:71], v[228:231], v[140:143], v[68:71]\n\t"
      "buffer_load_dwordx4 %[vL16], %[srdWS], s49 offen offset:3072 sc1 lds\n\t"
      "v_mfma_f32_16x16x32_bf16 v[64:67], v[224:227], v[140:143], v[64:67]\n\t"
      "s_waitcnt vmcnt(18) lgkmcnt(0)\n\t"
      "v_mfma_f32_16x16x32_bf16 v[16:19], v[208:211], v[144:147], v[16:19]\n\t"
      "ds_read_b64_tr_b16 v[224:225], v8 offset:24576\n\t"
      "buffer_load_dwordx4 v[192:195], %[vXsrc], %[srdWS], s40 offen offset:0 sc1\n\t"
      "v_mfma_f32_16x16x32_bf16 v[20:23], v[212:215], v[144:147], v[20:23]\n\t"
      "ds_read_b64_tr_b16 v[226:227], v8 offset:26624\n\t"
      "v_mfma_f32_16x16x32_bf16 v[24:27], v[216:219], v[144:147], v[24:27]\n\t"
      "ds_read_b64_tr_b16 v[228:229], v9 offset:24576\n\t"
      "s_add_u32 s50, s40, %[sXo]\n\t"
      "buffer_load_dwordx4 v[196:199], %[vXsrc], %[srdWS], s40 offen offset:256 sc1\n\t"
      "v_mfma_f32_16x16x32_bf16 v[28:31], v[220:223], v[144:147], v[28:31]\n\t"
      "ds_read_b64_tr_b16 v[230:231], v9 offset:26624\n\t"
      "v_mfma_f32_16x16x32_bf16 v[44:47], v[220:223], v[148:151], v[44:47]\n\t"
      "ds_read_b64_tr_b16 v[232:233], v10 offset:24576\n\t"
      "buffer_load_dwordx4 v[200:203], %[vXsrc], %[srdWS], s50 offen offset:0 sc1\n\t"
      "v_mfma_f32_16x16x32_bf16 v[40:43], v[216:219], v[148:151], v[40:43]\n\t"
      "ds_read_b64_tr_b16 v[234:235], v10 offset:26624\n\t"
      "v_mfma_f32_16x16x32_bf16 v[36:39], v[212:215], v[148:151], v[36:39]\n\t"
      "ds_read_b64_tr_b16 v[236:237], v11 offset:24576\n\t"
      "buffer_load_dwordx4 v[204:207], %[vXsrc], %[srdWS], s50 offen offset:256 sc1\n\t"
      "s_add_u32 s40, s40, 4096\n\t"
      "v_mfma_f32_16x16x32_bf16 v[32:35], v[208:211], v[148:151], v[32:35]\n\t"
      "ds_read_b64_tr_b16 v[238:239], v11 offset:26624\n\t"
      "v_mfma_f32_16x16x32_bf16 v[48:51], v[208:211], v[152:155], v[48:51]\n\t"
      "s_add_u32 m0, s42, 98304\n\t"
      "s_add_u32 s43, s41, 15360\n\t"
      "buffer_load_dwordx4 %[vWsrc], s[44:47], s41 offen lds\n\t"
      "v_mfma_f32_16x16x32_bf16 v[52:55], v[212:215], v[152:155], v[52:55]\n\t"
      "v_mfma_f32_16x16x32_bf16 v[56:59], v[216:219], v[152:155], v[56:59]\n\t"
      "buffer_load_dwordx4 %[vWsrc], s[44:47], s43 offen offset:1024 lds\n\t"
      "s_add_u32 s41, s41, 131072\n\t"
      "v_mfma_f32_16x16x32_bf16 v[60:63], v[220:223], v[152:155], v[60:63]\n\t"
      "v_mfma_f32_16x16x32_bf16 v[76:79], v[220:223], v[156:159], v[76:79]\n\t"
      "v_mfma_f32_16x16x32_bf16 v[72:75], v[216:219], v[156:159], v[72:75]\n\t"
      "v_mfma_f32_16x16x32_bf16 v[68:71], v[212:215], v[156:159], v[68:71]\n\t"
      "v_mfma_f32_16x16x32_bf16 v[64:67], v[208:211], v[156:159], v[64:67]\n\t"
      "s_waitcnt vmcnt(24) lgkmcnt(0)\n\t"
      "s_barrier\n\t"
      "s_waitcnt vmcnt(20) lgkmcnt(0)\n\t"
      "v_mfma_f32_16x16x32_bf16 v[16:19], v[224:227], v[160:163], v[16:19]\n\t"
      "ds_read_b64_tr_b16 v[208:209], v8 offset:32768\n\t"
      "buffer_load_dwordx4 v[80:83], %[vXsrc], %[srdWS], s40 offen offset:0 sc1\n\t"
      "v_mfma_f32_16x16x32_bf16 v[20:23], v[228:231], v[160:163], v[20:23]\n\t"
      "ds_read_b64_tr_b16 v[210:211], v8 offset:34816\n\t"
      "v_mfma_f32_16x16x32_bf16 v[24:27], v[232:235], v[160:163], v[24:27]\n\t"
      "ds_read_b64_tr_b16 v[212:213], v9 offset:32768\n\t"
      "s_add_u32 s50, s40, %[sXo]\n\t"
      "buffer_load_dwordx4 v[84:87], %[vXsrc], %[srdWS], s40 offen offset:256 sc1\n\t"
      "v_mfma_f32_16x16x32_bf16 v[28:31], v[236:239], v[160:163], v[28:31]\n\t"
      "ds_read_b64_tr_b16 v[214:215], v9 offset:34816\n\t"
      "v_mfma_f32_16x16x32_bf16 v[44:47], v[236:239], v[164:167], v[44:47]\n\t"
      "ds_read_b64_tr_b16 v[216:217], v10 offset:32768\n\t"
      "buffer_load_dwordx4 v[88:91], %[vXsrc], %[srdWS], s50 offen offset:0 sc1\n\t"
      "v_mfma_f32_16x16x32_bf16 v[40:43], v[232:235], v[164:167], v[40:43]\n\t"
      "ds_read_b64_tr_b16 v[218:219], v10 offset:34816\n\t"
      "v_mfma_f32_16x16x32_bf16 v[36:39], v[228:231], v[164:167], v[36:39]\n\t"
      "ds_read_b64_tr_b16 v[220:221], v11 offset:32768\n\t"
      "buffer_load_dwordx4 v[92:95], %[vXsrc], %[srdWS], s50 offen offset:256 sc1\n\t"
      "s_add_u32 s40, s40, 4096\n\t"
      "v_mfma_f32_16x16x32_bf16 v[32:35], v[224:227], v[164:167], v[32:35]\n\t"
      "ds_read_b64_tr_b16 v[222:223], v11 offset:34816\n\t"
      "v_mfma_f32_16x16x32_bf16 v[48:51], v[224:227], v[168:171], v[48:51]\n\t"
      "s_add_u32 m0, s42, 106496\n\t"
      "s_add_u32 s43, s41, 15360\n\t"
      "buffer_load_dwordx4 %[vWsrc], s[44:47], s41 offen lds\n\t"
      "v_mfma_f32_16x16x32_bf16 v[52:55], v[228:231], v[168:171], v[52:55]\n\t"
      "v_mfma_f32_16x16x32_bf16 v[56:59], v[232:235], v[168:171], v[56:59]\n\t"
      "buffer_load_dwordx4 %[vWsrc], s[44:47], s43 offen offset:1024 lds\n\t"
      "s_add_u32 s41, s41, 131072\n\t"
      "v_mfma_f32_16x16x32_bf16 v[60:63], v[236:239], v[168:171], v[60:63]\n\t"
      "v_mfma_f32_16x16x32_bf16 v[76:79], v[236:239], v[172:175], v[76:79]\n\t"
      "v_mfma_f32_16x16x32_bf16 v[72:75], v[232:235], v[172:175], v[72:75]\n\t"
      "v_mfma_f32_16x16x32_bf16 v[68:71], v[228:231], v[172:175], v[68:71]\n\t"
      "v_mfma_f32_16x16x32_bf16 v[64:67], v[224:227], v[172:175], v[64:67]\n\t"
      "s_waitcnt vmcnt(18) lgkmcnt(0)\n\t"
      "v_mfma_f32_16x16x32_bf16 v[16:19], v[208:211], v[176:179], v[16:19]\n\t"
      "ds_read_b64_tr_b16 v[224:225], v8 offset:40960\n\t"
      "buffer_load_dwordx4 v[96:99], %[vXsrc], %[srdWS], s40 offen offset:0 sc1\n\t"
      "v_mfma_f32_16x16x32_bf16 v[20:23], v[212:215], v[176:179], v[20:23]\n\t"
      "ds_read_b64_tr_b16 v[226:227], v8 offset:43008\n\t"
      "s_add_u32 s50, s40, %[sXo]\n\t"
      "buffer_load_dwordx4 v[100:103], %[vXsrc], %[srdWS], s40 offen offset:256 sc1\n\t"
      "v_mfma_f32_16x16x32_bf16 v[24:27], v[216:219], v[176:179], v[24:27]\n\t"
      "ds_read_b64_tr_b16 v[228:229], v9 offset:40960\n\t"
      "v_mfma_f32_16x16x32_bf16 v[28:31], v[220:223], v[176:179], v[28:31]\n\t"
      "ds_read_b64_tr_b16 v[230:231], v9 offset:43008\n\t"
      "buffer_load_dwordx4 v[104:107], %[vXsrc], %[srdWS], s50 offen offset:0 sc1\n\t"
      "v_mfma_f32_16x16x32_bf16 v[44:47], v[220:223], v[180:183], v[44:47]\n\t"
      "ds_read_b64_tr_b16 v[232:233], v10 offset:40960\n\t"
      "buffer_load_dwordx4 v[108:111], %[vXsrc], %[srdWS], s50 offen offset:256 sc1\n\t"
      "s_add_u32 s40, s40, 4096\n\t"
      "v_mfma_f32_16x16x32_bf16 v[40:43], v[216:219], v[180:183], v[40:43]\n\t"
      "ds_read_b64_tr_b16 v[234:235], v10 offset:43008\n\t"
      "v_mfma_f32_16x16x32_bf16 v[36:39], v[212:215], v[180:183], v[36:39]\n\t"
      "ds_read_b64_tr_b16 v[236:237], v11 offset:40960\n\t"
      "s_add_u32 m0, s42, 114688\n\t"
      "s_add_u32 s43, s41, 15360\n\t"
      "buffer_load_dwordx4 %[vWsrc], s[44:47], s41 offen lds\n\t"
      "v_mfma_f32_16x16x32_bf16 v[32:35], v[208:211], v[180:183], v[32:35]\n\t"
      "ds_read_b64_tr_b16 v[238:239], v11 offset:43008\n\t"
      "v_mfma_f32_16x16x32_bf16 v[48:51], v[208:211], v[184:187], v[48:51]\n\t"
      "buffer_load_dwordx4 %[vWsrc], s[44:47], s43 offen offset:1024 lds\n\t"
      "s_add_u32 s41, s41, 131072\n\t"
      "v_mfma_f32_16x16x32_bf16 v[52:55], v[212:215], v[184:187], v[52:55]\n\t"
      "s_lshl_b32 s43, s42, 1\n\t"
      "s_add_u32 m0, s43, 65536\n\t"
      "s_add_u32 s49, s49, 16384\n\t"
      "s_bitset0_b32 s49, 18\n\t"
      "buffer_load_dwordx4 %[vL16], %[srdWS], s49 offen offset:0 sc1 lds\n\t"
      "v_mfma_f32_16x16x32_bf16 v[56:59], v[216:219], v[184:187], v[56:59]\n\t"
      "v_mfma_f32_16x16x32_bf16 v[60:63], v[220:223], v[184:187], v[60:63]\n\t"
      "buffer_load_dwordx4 %[vL16], %[srdWS], s49 offen offset:1024 sc1 lds\n\t"
      "v_mfma_f32_16x16x32_bf16 v[76:79], v[220:223], v[188:191], v[76:79]\n\t"
      "buffer_load_dwordx4 %[vL16], %[srdWS], s49 offen offset:2048 sc1 lds\n\t"
      "v_mfma_f32_16x16x32_bf16 v[72:75], v[216:219], v[188:191], v[72:75]\n\t"
      "v_mfma_f32_16x16x32_bf16 v[68:71], v[212:215], v[188:191], v[68:71]\n\t"
      "buffer_load_dwordx4 %[vL16], %[srdWS], s49 offen offset:3072 sc1 lds\n\t"
      "v_mfma_f32_16x16x32_bf16 v[64:67], v[208:211], v[188:191], v[64:67]\n\t"
      "s_waitcnt vmcnt(18) lgkmcnt(0)\n\t"
      "v_mfma_f32_16x16x32_bf16 v[16:19], v[224:227], v[192:195], v[16:19]\n\t"
      "ds_read_b64_tr_b16 v[208:209], v8 offset:49152\n\t"
      "buffer_load_dwordx4 v[112:115], %[vXsrc], %[srdWS], s40 offen offset:0 sc1\n\t"
      "v_mfma_f32_16x16x32_bf16 v[20:23], v[228:231], v[192:195], v[20:23]\n\t"
      "ds_read_b64_tr_b16 v[210:211], v8 offset:51200\n\t"
      "v_mfma_f32_16x16x32_bf16 v[24:27], v[232:235], v[192:195], v[24:27]\n\t"
      "ds_read_b64_tr_b16 v[212:213], v9 offset:49152\n\t"
      "s_add_u32 s50, s40, %[sXo]\n\t"
      "buffer_load_dwordx4 v[116:119], %[vXsrc], %[srdWS], s40 offen offset:256 sc1\n\t"
      "v_mfma_f32_16x16x32_bf16 v[28:31], v[236:239], v[192:195], v[28:31]\n\t"
      "ds_read_b64_tr_b16 v[214:215], v9 offset:51200\n\t"
      "v_mfma_f32_16x16x32_bf16 v[44:47], v[236:239], v[196:199], v[44:47]\n\t"
      "ds_read_b64_tr_b16 v[216:217], v10 offset:49152\n\t"
      "buffer_load_dwordx4 v[120:123], %[vXsrc], %[srdWS], s50 offen offset:0 sc1\n\t"
      "v_mfma_f32_16x16x32_bf16 v[40:43], v[232:235], v[196:199], v[40:43]\n\t"
      "ds_read_b64_tr_b16 v[218:219], v10 offset:51200\n\t"
      "v_mfma_f32_16x16x32_bf16 v[36:39], v[228:231], v[196:199], v[36:39]\n\t"
      "ds_read_b64_tr_b16 v[220:221], v11 offset:49152\n\t"
      "buffer_load_dwordx4 v[124:127], %[vXsrc], %[srdWS], s50 offen offset:256 sc1\n\t"
      "s_add_u32 s40, s40, 4096\n\t"
      "v_mfma_f32_16x16x32_bf16 v[32:35], v[224:227], v[196:199], v[32:35]\n\t"
      "ds_read_b64_tr_b16 v[222:223], v11 offset:51200\n\t"
      "v_mfma_f32_16x16x32_bf16 v[48:51], v[224:227], v[200:203], v[48:51]\n\t"
      "s_add_u32 m0, s42, 122880\n\t"
      "s_add_u32 s43, s41, 15360\n\t"
      "buffer_load_dwordx4 %[vWsrc], s[44:47], s41 offen lds\n\t"
      "v_mfma_f32_16x16x32_bf16 v[52:55], v[228:231], v[200:203], v[52:55]\n\t"
      "v_mfma_f32_16x16x32_bf16 v[56:59], v[232:235], v[200:203], v[56:59]\n\t"
      "buffer_load_dwordx4 %[vWsrc], s[44:47], s43 offen offset:1024 lds\n\t"
      "s_add_u32 s41, s41, 131072\n\t"
      "v_mfma_f32_16x16x32_bf16 v[60:63], v[236:239], v[200:203], v[60:63]\n\t"
      "v_mfma_f32_16x16x32_bf16 v[76:79], v[236:239], v[204:207], v[76:79]\n\t"
      "v_mfma_f32_16x16x32_bf16 v[72:75], v[232:235], v[204:207], v[72:75]\n\t"
      "v_mfma_f32_16x16x32_bf16 v[68:71], v[228:231], v[204:207], v[68:71]\n\t"
      "v_mfma_f32_16x16x32_bf16 v[64:67], v[224:227], v[204:207], v[64:67]\n\t"
      "s_waitcnt vmcnt(18) lgkmcnt(0)\n\t"
      "v_mfma_f32_16x16x32_bf16 v[16:19], v[208:211], v[80:83], v[16:19]\n\t"
      "ds_read_b64_tr_b16 v[224:225], v8 offset:57344\n\t"
      "buffer_load_dwordx4 v[128:131], %[vXsrc], %[srdWS], s40 offen offset:0 sc1\n\t"
      "v_mfma_f32_16x16x32_bf16 v[20:23], v[212:215], v[80:83], v[20:23]\n\t"
      "ds_read_b64_tr_b16 v[226:227], v8 offset:59392\n\t"
      "v_mfma_f32_16x16x32_bf16 v[24:27], v[216:219], v[80:83], v[24:27]\n\t"
      "ds_read_b64_tr_b16 v[228:229], v9 offset:57344\n\t"
      "s_add_u32 s50, s40, %[sXo]\n\t"
      "buffer_load_dwordx4 v[132:135], %[vXsrc], %[srdWS], s40 offen offset:256 sc1\n\t"
      "v_mfma_f32_16x16x32_bf16 v[28:31], v[220:223], v[80:83], v[28:31]\n\t"
      "ds_read_b64_tr_b16 v[230:231], v9 offset:59392\n\t"
      "v_mfma_f32_16x16x32_bf16 v[44:47], v[220:223], v[84:87], v[44:47]\n\t"
      "ds_read_b64_tr_b16 v[232:233], v10 offset:57344\n\t"
      "buffer_load_dwordx4 v[136:139], %[vXsrc], %[srdWS], s50 offen offset:0 sc1\n\t"
      "v_mfma_f32_16x16x32_bf16 v[40:43], v[216:219], v[84:87], v[40:43]\n\t"
      "ds_read_b64_tr_b16 v[234:235], v10 offset:59392\n\t"
      "v_mfma_f32_16x16x32_bf16 v[36:39], v[212:215], v[84:87], v[36:39]\n\t"
      "ds_read_b64_tr_b16 v[236:237], v11 offset:57344\n\t"
      "buffer_load_dwordx4 v[140:143], %[vXsrc], %[srdWS], s50 offen offset:256 sc1\n\t"
      "s_add_u32 s40, s40, 4096\n\t"
      "v_mfma_f32_16x16x32_bf16 v[32:35], v[208:211], v[84:87], v[32:35]\n\t"
      "ds_read_b64_tr_b16 v[238:239], v11 offset:59392\n\t"
      "v_mfma_f32_16x16x32_bf16 v[48:51], v[208:211], v[88:91], v[48:51]\n\t"
      "v_mfma_f32_16x16x32_bf16 v[52:55], v[212:215], v[88:91], v[52:55]\n\t"
      "v_mfma_f32_16x16x32_bf16 v[56:59], v[216:219], v[88:91], v[56:59]\n\t"
      "v_mfma_f32_16x16x32_bf16 v[60:63], v[220:223], v[88:91], v[60:63]\n\t"
      "v_mfma_f32_16x16x32_bf16 v[76:79], v[220:223], v[92:95], v[76:79]\n\t"
      "v_mfma_f32_16x16x32_bf16 v[72:75], v[216:219], v[92:95], v[72:75]\n\t"
      "v_mfma_f32_16x16x32_bf16 v[68:71], v[212:215], v[92:95], v[68:71]\n\t"
      "v_mfma_f32_16x16x32_bf16 v[64:67], v[208:211], v[92:95], v[64:67]\n\t"
      "s_waitcnt vmcnt(22) lgkmcnt(0)\n\t"
      "s_barrier\n\t"
      "s_waitcnt vmcnt(16) lgkmcnt(0)\n\t"
      "v_mfma_f32_16x16x32_bf16 v[16:19], v[224:227], v[96:99], v[16:19]\n\t"
      "v_add_u32 v8, 4294901760, v8\n\t"
      "v_add_u32 v9, 4294901760, v9\n\t"
      "v_add_u32 v10, 4294901760, v10\n\t"
      "v_add_u32 v11, 4294901760, v11\n\t"
      "ds_read_b64_tr_b16 v[208:209], v8 offset:0\n\t"
      "buffer_load_dwordx4 v[144:147], %[vXsrc], %[srdWS], s40 offen offset:0 sc1\n\t"
      "v_mfma_f32_16x16x32_bf16 v[20:23], v[228:231], v[96:99], v[20:23]\n\t"
      "ds_read_b64_tr_b16 v[210:211], v8 offset:2048\n\t"
      "v_mfma_f32_16x16x32_bf16 v[24:27], v[232:235], v[96:99], v[24:27]\n\t"
      "ds_read_b64_tr_b16 v[212:213], v9 offset:0\n\t"
      "s_add_u32 s50, s40, %[sXo]\n\t"
      "buffer_load_dwordx4 v[148:151], %[vXsrc], %[srdWS], s40 offen offset:256 sc1\n\t"
      "v_mfma_f32_16x16x32_bf16 v[28:31], v[236:239], v[96:99], v[28:31]\n\t"
      "ds_read_b64_tr_b16 v[214:215], v9 offset:2048\n\t"
      "v_mfma_f32_16x16x32_bf16 v[44:47], v[236:239], v[100:103], v[44:47]\n\t"
      "ds_read_b64_tr_b16 v[216:217], v10 offset:0\n\t"
      "buffer_load_dwordx4 v[152:155], %[vXsrc], %[srdWS], s50 offen offset:0 sc1\n\t"
      "v_mfma_f32_16x16x32_bf16 v[40:43], v[232:235], v[100:103], v[40:43]\n\t"
      "ds_read_b64_tr_b16 v[218:219], v10 offset:2048\n\t"
      "v_mfma_f32_16x16x32_bf16 v[36:39], v[228:231], v[100:103], v[36:39]\n\t"
      "ds_read_b64_tr_b16 v[220:221], v11 offset:0\n\t"
      "buffer_load_dwordx4 v[156:159], %[vXsrc], %[srdWS], s50 offen offset:256 sc1\n\t"
      "s_add_u32 s40, s40, 4096\n\t"
      "v_mfma_f32_16x16x32_bf16 v[32:35], v[224:227], v[100:103], v[32:35]\n\t"
      "ds_read_b64_tr_b16 v[222:223], v11 offset:2048\n\t"
      "v_mfma_f32_16x16x32_bf16 v[48:51], v[224:227], v[104:107], v[48:51]\n\t"
      "s_add_u32 m0, s42, 131072\n\t"
      "s_add_u32 s43, s41, 15360\n\t"
      "buffer_load_dwordx4 %[vWsrc], s[44:47], s41 offen lds\n\t"
      "v_mfma_f32_16x16x32_bf16 v[52:55], v[228:231], v[104:107], v[52:55]\n\t"
      "v_mfma_f32_16x16x32_bf16 v[56:59], v[232:235], v[104:107], v[56:59]\n\t"
      "buffer_load_dwordx4 %[vWsrc], s[44:47], s43 offen offset:1024 lds\n\t"
      "s_add_u32 s41, s41, 131072\n\t"
      "v_mfma_f32_16x16x32_bf16 v[60:63], v[236:239], v[104:107], v[60:63]\n\t"
      "v_mfma_f32_16x16x32_bf16 v[76:79], v[236:239], v[108:111], v[76:79]\n\t"
      "s_add_u32 m0, s42, 139264\n\t"
      "s_add_u32 s43, s41, 15360\n\t"
      "buffer_load_dwordx4 %[vWsrc], s[44:47], s41 offen lds\n\t"
      "v_mfma_f32_16x16x32_bf16 v[72:75], v[232:235], v[108:111], v[72:75]\n\t"
      "v_mfma_f32_16x16x32_bf16 v[68:71], v[228:231], v[108:111], v[68:71]\n\t"
      "buffer_load_dwordx4 %[vWsrc], s[44:47], s43 offen offset:1024 lds\n\t"
      "s_add_u32 s41, s41, 131072\n\t"
      "v_mfma_f32_16x16x32_bf16 v[64:67], v[224:227], v[108:111], v[64:67]\n\t"
      "s_waitcnt vmcnt(14) lgkmcnt(0)\n\t"
      "v_mfma_f32_16x16x32_bf16 v[16:19], v[208:211], v[112:115], v[16:19]\n\t"
      "ds_read_b64_tr_b16 v[224:225], v8 offset:8192\n\t"
      "buffer_load_dwordx4 v[160:163], %[vXsrc], %[srdWS], s40 offen offset:0 sc1\n\t"
      "v_mfma_f32_16x16x32_bf16 v[20:23], v[212:215], v[112:115], v[20:23]\n\t"
      "ds_read_b64_tr_b16 v[226:227], v8 offset:10240\n\t"
      "s_add_u32 s50, s40, %[sXo]\n\t"
      "buffer_load_dwordx4 v[164:167], %[vXsrc], %[srdWS], s40 offen offset:256 sc1\n\t"
      "v_mfma_f32_16x16x32_bf16 v[24:27], v[216:219], v[112:115], v[24:27]\n\t"
      "ds_read_b64_tr_b16 v[228:229], v9 offset:8192\n\t"
      "v_mfma_f32_16x16x32_bf16 v[28:31], v[220:223], v[112:115], v[28:31]\n\t"
      "ds_read_b64_tr_b16 v[230:231], v9 offset:10240\n\t"
      "buffer_load_dwordx4 v[168:171], %[vXsrc], %[srdWS], s50 offen offset:0 sc1\n\t"
      "v_mfma_f32_16x16x32_bf16 v[44:47], v[220:223], v[116:119], v[44:47]\n\t"
      "ds_read_b64_tr_b16 v[232:233], v10 offset:8192\n\t"
      "buffer_load_dwordx4 v[172:175], %[vXsrc], %[srdWS], s50 offen offset:256 sc1\n\t"
      "s_add_u32 s40, s40, 4096\n\t"
      "v_mfma_f32_16x16x32_bf16 v[40:43], v[216:219], v[116:119], v[40:43]\n\t"
      "ds_read_b64_tr_b16 v[234:235], v10 offset:10240\n\t"
      "v_mfma_f32_16x16x32_bf16 v[36:39], v[212:215], v[116:119], v[36:39]\n\t"
      "ds_read_b64_tr_b16 v[236:237], v11 offset:8192\n\t"
      "s_add_u32 m0, s42, 147456\n\t"
      "s_add_u32 s43, s41, 15360\n\t"
      "buffer_load_dwordx4 %[vWsrc], s[44:47], s41 offen lds\n\t"
      "v_mfma_f32_16x16x32_bf16 v[32:35], v[208:211], v[116:119], v[32:35]\n\t"
      "ds_read_b64_tr_b16 v[238:239], v11 offset:10240\n\t"
      "v_mfma_f32_16x16x32_bf16 v[48:51], v[208:211], v[120:123], v[48:51]\n\t"
      "buffer_load_dwordx4 %[vWsrc], s[44:47], s43 offen offset:1024 lds\n\t"
      "s_add_u32 s41, s41, 131072\n\t"
      "v_mfma_f32_16x16x32_bf16 v[52:55], v[212:215], v[120:123], v[52:55]\n\t"
      "s_lshl_b32 s43, s42, 1\n\t"
      "s_add_u32 m0, s43, 81920\n\t"
      "s_add_u32 s49, s49, 16384\n\t"
      "s_bitset0_b32 s49, 18\n\t"
      "buffer_load_dwordx4 %[vL16], %[srdWS], s49 offen offset:0 sc1 lds\n\t"
      "v_mfma_f32_16x16x32_bf16 v[56:59], v[216:219], v[120:123], v[56:59]\n\t"
      "v_mfma_f32_16x16x32_bf16 v[60:63], v[220:223], v[120:123], v[60:63]\n\t"
      "buffer_load_dwordx4 %[vL16], %[srdWS], s49 offen offset:1024 sc1 lds\n\t"
      "v_mfma_f32_16x16x32_bf16 v[76:79], v[220:223], v[124:127], v[76:79]\n\t"
      "buffer_load_dwordx4 %[vL16], %[srdWS], s49 offen offset:2048 sc1 lds\n\t"
      "v_mfma_f32_16x16x32_bf16 v[72:75], v[216:219], v[124:127], v[72:75]\n\t"
      "v_mfma_f32_16x16x32_bf16 v[68:71], v[212:215], v[124:127], v[68:71]\n\t"
      "buffer_load_dwordx4 %[vL16], %[srdWS], s49 offen offset:3072 sc1 lds\n\t"
      "v_mfma_f32_16x16x32_bf16 v[64:67], v[208:211], v[124:127], v[64:67]\n\t"
      "s_waitcnt vmcnt(18) lgkmcnt(0)\n\t"
      "v_mfma_f32_16x16x32_bf16 v[16:19], v[224:227], v[128:131], v[16:19]\n\t"
      "ds_read_b64_tr_b16 v[208:209], v8 offset:16384\n\t"
      "buffer_load_dwordx4 v[176:179], %[vXsrc], %[srdWS], s40 offen offset:0 sc1\n\t"
      "v_mfma_f32_16x16x32_bf16 v[20:23], v[228:231], v[128:131], v[20:23]\n\t"
      "ds_read_b64_tr_b16 v[210:211], v8 offset:18432\n\t"
      "v_mfma_f32_16x16x32_bf16 v[24:27], v[232:235], v[128:131], v[24:27]\n\t"
      "ds_read_b64_tr_b16 v[212:213], v9 offset:16384\n\t"
      "s_add_u32 s50, s40, %[sXo]\n\t"
      "buffer_load_dwordx4 v[180:183], %[vXsrc], %[srdWS], s40 offen offset:256 sc1\n\t"
      "v_mfma_f32_16x16x32_bf16 v[28:31], v[236:239], v[128:131], v[28:31]\n\t"
      "ds_read_b64_tr_b16 v[214:215], v9 offset:18432\n\t"
      "v_mfma_f32_16x16x32_bf16 v[44:47], v[236:239], v[132:135], v[44:47]\n\t"
      "ds_read_b64_tr_b16 v[216:217], v10 offset:16384\n\t"
      "buffer_load_dwordx4 v[184:187], %[vXsrc], %[srdWS], s50 offen offset:0 sc1\n\t"
      "v_mfma_f32_16x16x32_bf16 v[40:43], v[232:235], v[132:135], v[40:43]\n\t"
      "ds_read_b64_tr_b16 v[218:219], v10 offset:18432\n\t"
      "v_mfma_f32_16x16x32_bf16 v[36:39], v[228:231], v[132:135], v[36:39]\n\t"
      "ds_read_b64_tr_b16 v[220:221], v11 offset:16384\n\t"
      "buffer_load_dwordx4 v[188:191], %[vXsrc], %[srdWS], s50 offen offset:256 sc1\n\t"
      "s_add_u32 s40, s40, 4096\n\t"
      "v_mfma_f32_16x16x32_bf16 v[32:35], v[224:227], v[132:135], v[32:35]\n\t"
      "ds_read_b64_tr_b16 v[222:223], v11 offset:18432\n\t"
      "v_mfma_f32_16x16x32_bf16 v[48:51], v[224:227], v[136:139], v[48:51]\n\t"
      "s_add_u32 m0, s42, 155648\n\t"
      "s_add_u32 s43, s41, 15360\n\t"
      "buffer_load_dwordx4 %[vWsrc], s[44:47], s41 offen lds\n\t"
      "v_mfma_f32_16x16x32_bf16 v[52:55], v[228:231], v[136:139], v[52:55]\n\t"
      "v_mfma_f32_16x16x32_bf16 v[56:59], v[232:235], v[136:139], v[56:59]\n\t"
      "buffer_load_dwordx4 %[vWsrc], s[44:47], s43 offen offset:1024 lds\n\t"
      "s_add_u32 s41, s41, 131072\n\t"
      "v_mfma_f32_16x16x32_bf16 v[60:63], v[236:239], v[136:139], v[60:63]\n\t"
      "v_mfma_f32_16x16x32_bf16 v[76:79], v[236:239], v[140:143], v[76:79]\n\t"
      "v_mfma_f32_16x16x32_bf16 v[72:75], v[232:235], v[140:143], v[72:75]\n\t"
      "v_mfma_f32_16x16x32_bf16 v[68:71], v[228:231], v[140:143], v[68:71]\n\t"
      "v_mfma_f32_16x16x32_bf16 v[64:67], v[224:227], v[140:143], v[64:67]\n\t"
      "s_waitcnt vmcnt(24) lgkmcnt(0)\n\t"
      "s_barrier\n\t"
      "s_waitcnt vmcnt(20) lgkmcnt(0)\n\t"
      "v_mfma_f32_16x16x32_bf16 v[16:19], v[208:211], v[144:147], v[16:19]\n\t"
      "ds_read_b64_tr_b16 v[224:225], v8 offset:24576\n\t"
      "buffer_load_dwordx4 v[192:195], %[vXsrc], %[srdWS], s40 offen offset:0 sc1\n\t"
      "v_mfma_f32_16x16x32_bf16 v[20:23], v[212:215], v[144:147], v[20:23]\n\t"
      "ds_read_b64_tr_b16 v[226:227], v8 offset:26624\n\t"
      "v_mfma_f32_16x16x32_bf16 v[24:27], v[216:219], v[144:147], v[24:27]\n\t"
      "ds_read_b64_tr_b16 v[228:229], v9 offset:24576\n\t"
      "s_add_u32 s50, s40, %[sXo]\n\t"
      "buffer_load_dwordx4 v[196:199], %[vXsrc], %[srdWS], s40 offen offset:256 sc1\n\t"
      "v_mfma_f32_16x16x32_bf16 v[28:31], v[220:223], v[144:147], v[28:31]\n\t"
      "ds_read_b64_tr_b16 v[230:231], v9 offset:26624\n\t"
      "v_mfma_f32_16x16x32_bf16 v[44:47], v[220:223], v[148:151], v[44:47]\n\t"
      "ds_read_b64_tr_b16 v[232:233], v10 offset:24576\n\t"
      "buffer_load_dwordx4 v[200:203], %[vXsrc], %[srdWS], s50 offen offset:0 sc1\n\t"
      "v_mfma_f32_16x16x32_bf16 v[40:43], v[216:219], v[148:151], v[40:43]\n\t"
      "ds_read_b64_tr_b16 v[234:235], v10 offset:26624\n\t"
      "v_mfma_f32_16x16x32_bf16 v[36:39], v[212:215], v[148:151], v[36:39]\n\t"
      "ds_read_b64_tr_b16 v[236:237], v11 offset:24576\n\t"
      "buffer_load_dwordx4 v[204:207], %[vXsrc], %[srdWS], s50 offen offset:256 sc1\n\t"
      "s_add_u32 s40, s40, 4096\n\t"
      "v_mfma_f32_16x16x32_bf16 v[32:35], v[208:211], v[148:151], v[32:35]\n\t"
      "ds_read_b64_tr_b16 v[238:239], v11 offset:26624\n\t"
      "v_mfma_f32_16x16x32_bf16 v[48:51], v[208:211], v[152:155], v[48:51]\n\t"
      "s_add_u32 m0, s42, 32768\n\t"
      "s_add_u32 s43, s41, 15360\n\t"
      "buffer_load_dwordx4 %[vWsrc], s[44:47], s41 offen lds\n\t"
      "v_mfma_f32_16x16x32_bf16 v[52:55], v[212:215], v[152:155], v[52:55]\n\t"
      "v_mfma_f32_16x16x32_bf16 v[56:59], v[216:219], v[152:155], v[56:59]\n\t"
      "buffer_load_dwordx4 %[vWsrc], s[44:47], s43 offen offset:1024 lds\n\t"
      "s_add_u32 s41, s41, 131072\n\t"
      "v_mfma_f32_16x16x32_bf16 v[60:63], v[220:223], v[152:155], v[60:63]\n\t"
      "v_mfma_f32_16x16x32_bf16 v[76:79], v[220:223], v[156:159], v[76:79]\n\t"
      "v_mfma_f32_16x16x32_bf16 v[72:75], v[216:219], v[156:159], v[72:75]\n\t"
      "v_mfma_f32_16x16x32_bf16 v[68:71], v[212:215], v[156:159], v[68:71]\n\t"
      "v_mfma_f32_16x16x32_bf16 v[64:67], v[208:211], v[156:159], v[64:67]\n\t"
      "s_waitcnt vmcnt(18) lgkmcnt(0)\n\t"
      "v_mfma_f32_16x16x32_bf16 v[16:19], v[224:227], v[160:163], v[16:19]\n\t"
      "v_add_u32 v8, 65536, v8\n\t"
      "v_add_u32 v9, 65536, v9\n\t"
      "v_add_u32 v10, 65536, v10\n\t"
      "v_add_u32 v11, 65536, v11\n\t"
      "ds_read_b64_tr_b16 v[208:209], v8 offset:0\n\t"
      "buffer_load_dwordx4 v[80:83], %[vXsrc], %[srdWS], s40 offen offset:0 sc1\n\t"
      "v_mfma_f32_16x16x32_bf16 v[20:23], v[228:231], v[160:163], v[20:23]\n\t"
      "ds_read_b64_tr_b16 v[210:211], v8 offset:2048\n\t"
      "s_add_u32 s50, s40, %[sXo]\n\t"
      "buffer_load_dwordx4 v[84:87], %[vXsrc], %[srdWS], s40 offen offset:256 sc1\n\t"
      "v_mfma_f32_16x16x32_bf16 v[24:27], v[232:235], v[160:163], v[24:27]\n\t"
      "ds_read_b64_tr_b16 v[212:213], v9 offset:0\n\t"
      "v_mfma_f32_16x16x32_bf16 v[28:31], v[236:239], v[160:163], v[28:31]\n\t"
      "ds_read_b64_tr_b16 v[214:215], v9 offset:2048\n\t"
      "buffer_load_dwordx4 v[88:91], %[vXsrc], %[srdWS], s50 offen offset:0 sc1\n\t"
      "v_mfma_f32_16x16x32_bf16 v[44:47], v[236:239], v[164:167], v[44:47]\n\t"
      "ds_read_b64_tr_b16 v[216:217], v10 offset:0\n\t"
      "buffer_load_dwordx4 v[92:95], %[vXsrc], %[srdWS], s50 offen offset:256 sc1\n\t"
      "s_add_u32 s40, s40, 4096\n\t"
      "v_mfma_f32_16x16x32_bf16 v[40:43], v[232:235], v[164:167], v[40:43]\n\t"
      "ds_read_b64_tr_b16 v[218:219], v10 offset:2048\n\t"
      "v_mfma_f32_16x16x32_bf16 v[36:39], v[228:231], v[164:167], v[36:39]\n\t"
      "ds_read_b64_tr_b16 v[220:221], v11 offset:0\n\t"
      "s_add_u32 m0, s42, 40960\n\t"
      "s_add_u32 s43, s41, 15360\n\t"
      "buffer_load_dwordx4 %[vWsrc], s[44:47], s41 offen lds\n\t"
      "v_mfma_f32_16x16x32_bf16 v[32:35], v[224:227], v[164:167], v[32:35]\n\t"
      "ds_read_b64_tr_b16 v[222:223], v11 offset:2048\n\t"
      "v_mfma_f32_16x16x32_bf16 v[48:51], v[224:227], v[168:171], v[48:51]\n\t"
      "buffer_load_dwordx4 %[vWsrc], s[44:47], s43 offen offset:1024 lds\n\t"
      "s_add_u32 s41, s41, 131072\n\t"
      "v_mfma_f32_16x16x32_bf16 v[52:55], v[228:231], v[168:171], v[52:55]\n\t"
      "s_lshl_b32 s43, s42, 1\n\t"
      "s_add_u32 m0, s43, 0\n\t"
      "s_add_u32 s49, s49, 16384\n\t"
      "s_bitset0_b32 s49, 18\n\t"
      "buffer_load_dwordx4 %[vL16], %[srdWS], s49 offen offset:0 sc1 lds\n\t"
      "v_mfma_f32_16x16x32_bf16 v[56:59], v[232:235], v[168:171], v[56:59]\n\t"
      "v_mfma_f32_16x16x32_bf16 v[60:63], v[236:239], v[168:171], v[60:63]\n\t"
      "buffer_load_dwordx4 %[vL16], %[srdWS], s49 offen offset:1024 sc1 lds\n\t"
      "v_mfma_f32_16x16x32_bf16 v[76:79], v[236:239], v[172:175], v[76:79]\n\t"
      "buffer_load_dwordx4 %[vL16], %[srdWS], s49 offen offset:2048 sc1 lds\n\t"
      "v_mfma_f32_16x16x32_bf16 v[72:75], v[232:235], v[172:175], v[72:75]\n\t"
      "v_mfma_f32_16x16x32_bf16 v[68:71], v[228:231], v[172:175], v[68:71]\n\t"
      "buffer_load_dwordx4 %[vL16], %[srdWS], s49 offen offset:3072 sc1 lds\n\t"
      "v_mfma_f32_16x16x32_bf16 v[64:67], v[224:227], v[172:175], v[64:67]\n\t"
      "s_waitcnt vmcnt(18) lgkmcnt(0)\n\t"
      "v_mfma_f32_16x16x32_bf16 v[16:19], v[208:211], v[176:179], v[16:19]\n\t"
      "ds_read_b64_tr_b16 v[224:225], v8 offset:8192\n\t"
      "buffer_load_dwordx4 v[96:99], %[vXsrc], %[srdWS], s40 offen offset:0 sc1\n\t"
      "v_mfma_f32_16x16x32_bf16 v[20:23], v[212:215], v[176:179], v[20:23]\n\t"
      "ds_read_b64_tr_b16 v[226:227], v8 offset:10240\n\t"
      "v_mfma_f32_16x16x32_bf16 v[24:27], v[216:219], v[176:179], v[24:27]\n\t"
      "ds_read_b64_tr_b16 v[228:229], v9 offset:8192\n\t"
      "s_add_u32 s50, s40, %[sXo]\n\t"
      "buffer_load_dwordx4 v[100:103], %[vXsrc], %[srdWS], s40 offen offset:256 sc1\n\t"
      "v_mfma_f32_16x16x32_bf16 v[28:31], v[220:223], v[176:179], v[28:31]\n\t"
      "ds_read_b64_tr_b16 v[230:231], v9 offset:10240\n\t"
      "v_mfma_f32_16x16x32_bf16 v[44:47], v[220:223], v[180:183], v[44:47]\n\t"
      "ds_read_b64_tr_b16 v[232:233], v10 offset:8192\n\t"
      "buffer_load_dwordx4 v[104:107], %[vXsrc], %[srdWS], s50 offen offset:0 sc1\n\t"
      "v_mfma_f32_16x16x32_bf16 v[40:43], v[216:219], v[180:183], v[40:43]\n\t"
      "ds_read_b64_tr_b16 v[234:235], v10 offset:10240\n\t"
      "v_mfma_f32_16x16x32_bf16 v[36:39], v[212:215], v[180:183], v[36:39]\n\t"
      "ds_read_b64_tr_b16 v[236:237], v11 offset:8192\n\t"
      "buffer_load_dwordx4 v[108:111], %[vXsrc], %[srdWS], s50 offen offset:256 sc1\n\t"
      "s_add_u32 s40, s40, 4096\n\t"
      "v_mfma_f32_16x16x32_bf16 v[32:35], v[208:211], v[180:183], v[32:35]\n\t"
      "ds_read_b64_tr_b16 v[238:239], v11 offset:10240\n\t"
      "v_mfma_f32_16x16x32_bf16 v[48:51], v[208:211], v[184:187], v[48:51]\n\t"
      "s_add_u32 m0, s42, 49152\n\t"
      "s_add_u32 s43, s41, 15360\n\t"
      "buffer_load_dwordx4 %[vWsrc], s[44:47], s41 offen lds\n\t"
      "v_mfma_f32_16x16x32_bf16 v[52:55], v[212:215], v[184:187], v[52:55]\n\t"
      "v_mfma_f32_16x16x32_bf16 v[56:59], v[216:219], v[184:187], v[56:59]\n\t"
      "buffer_load_dwordx4 %[vWsrc], s[44:47], s43 offen offset:1024 lds\n\t"
      "s_add_u32 s41, s41, 131072\n\t"
      "v_mfma_f32_16x16x32_bf16 v[60:63], v[220:223], v[184:187], v[60:63]\n\t"
      "v_mfma_f32_16x16x32_bf16 v[76:79], v[220:223], v[188:191], v[76:79]\n\t"
      "v_mfma_f32_16x16x32_bf16 v[72:75], v[216:219], v[188:191], v[72:75]\n\t"
      "v_mfma_f32_16x16x32_bf16 v[68:71], v[212:215], v[188:191], v[68:71]\n\t"
      "v_mfma_f32_16x16x32_bf16 v[64:67], v[208:211], v[188:191], v[64:67]\n\t"
      "s_waitcnt vmcnt(18) lgkmcnt(0)\n\t"
      "v_mfma_f32_16x16x32_bf16 v[16:19], v[224:227], v[192:195], v[16:19]\n\t"
      "ds_read_b64_tr_b16 v[208:209], v8 offset:16384\n\t"
      "buffer_load_dwordx4 v[112:115], %[vXsrc], %[srdWS], s40 offen offset:0 sc1\n\t"
      "v_mfma_f32_16x16x32_bf16 v[20:23], v[228:231], v[192:195], v[20:23]\n\t"
      "ds_read_b64_tr_b16 v[210:211], v8 offset:18432\n\t"
      "v_mfma_f32_16x16x32_bf16 v[24:27], v[232:235], v[192:195], v[24:27]\n\t"
      "ds_read_b64_tr_b16 v[212:213], v9 offset:16384\n\t"
      "s_add_u32 s50, s40, %[sXo]\n\t"
      "buffer_load_dwordx4 v[116:119], %[vXsrc], %[srdWS], s40 offen offset:256 sc1\n\t"
      "v_mfma_f32_16x16x32_bf16 v[28:31], v[236:239], v[192:195], v[28:31]\n\t"
      "ds_read_b64_tr_b16 v[214:215], v9 offset:18432\n\t"
      "v_mfma_f32_16x16x32_bf16 v[44:47], v[236:239], v[196:199], v[44:47]\n\t"
      "ds_read_b64_tr_b16 v[216:217], v10 offset:16384\n\t"
      "buffer_load_dwordx4 v[120:123], %[vXsrc], %[srdWS], s50 offen offset:0 sc1\n\t"
      "v_mfma_f32_16x16x32_bf16 v[40:43], v[232:235], v[196:199], v[40:43]\n\t"
      "ds_read_b64_tr_b16 v[218:219], v10 offset:18432\n\t"
      "v_mfma_f32_16x16x32_bf16 v[36:39], v[228:231], v[196:199], v[36:39]\n\t"
      "ds_read_b64_tr_b16 v[220:221], v11 offset:16384\n\t"
      "buffer_load_dwordx4 v[124:127], %[vXsrc], %[srdWS], s50 offen offset:256 sc1\n\t"
      "s_add_u32 s40, s40, 4096\n\t"
      "v_mfma_f32_16x16x32_bf16 v[32:35], v[224:227], v[196:199], v[32:35]\n\t"
      "ds_read_b64_tr_b16 v[222:223], v11 offset:18432\n\t"
      "v_mfma_f32_16x16x32_bf16 v[48:51], v[224:227], v[200:203], v[48:51]\n\t"
      "v_mfma_f32_16x16x32_bf16 v[52:55], v[228:231], v[200:203], v[52:55]\n\t"
      "v_mfma_f32_16x16x32_bf16 v[56:59], v[232:235], v[200:203], v[56:59]\n\t"
      "v_mfma_f32_16x16x32_bf16 v[60:63], v[236:239], v[200:203], v[60:63]\n\t"
      "v_mfma_f32_16x16x32_bf16 v[76:79], v[236:239], v[204:207], v[76:79]\n\t"
      "v_mfma_f32_16x16x32_bf16 v[72:75], v[232:235], v[204:207], v[72:75]\n\t"
      "v_mfma_f32_16x16x32_bf16 v[68:71], v[228:231], v[204:207], v[68:71]\n\t"
      "v_mfma_f32_16x16x32_bf16 v[64:67], v[224:227], v[204:207], v[64:67]\n\t"
      "s_waitcnt vmcnt(22) lgkmcnt(0)\n\t"
      "s_barrier\n\t"
      "s_waitcnt vmcnt(16) lgkmcnt(0)\n\t"
      "v_mfma_f32_16x16x32_bf16 v[16:19], v[208:211], v[80:83], v[16:19]\n\t"
      "ds_read_b64_tr_b16 v[224:225], v8 offset:24576\n\t"
      "buffer_load_dwordx4 v[128:131], %[vXsrc], %[srdWS], s40 offen offset:0 sc1\n\t"
      "v_mfma_f32_16x16x32_bf16 v[20:23], v[212:215], v[80:83], v[20:23]\n\t"
      "ds_read_b64_tr_b16 v[226:227], v8 offset:26624\n\t"
      "v_mfma_f32_16x16x32_bf16 v[24:27], v[216:219], v[80:83], v[24:27]\n\t"
      "ds_read_b64_tr_b16 v[228:229], v9 offset:24576\n\t"
      "s_add_u32 s50, s40, %[sXo]\n\t"
      "buffer_load_dwordx4 v[132:135], %[vXsrc], %[srdWS], s40 offen offset:256 sc1\n\t"
      "v_mfma_f32_16x16x32_bf16 v[28:31], v[220:223], v[80:83], v[28:31]\n\t"
      "ds_read_b64_tr_b16 v[230:231], v9 offset:26624\n\t"
      "v_mfma_f32_16x16x32_bf16 v[44:47], v[220:223], v[84:87], v[44:47]\n\t"
      "ds_read_b64_tr_b16 v[232:233], v10 offset:24576\n\t"
      "buffer_load_dwordx4 v[136:139], %[vXsrc], %[srdWS], s50 offen offset:0 sc1\n\t"
      "v_mfma_f32_16x16x32_bf16 v[40:43], v[216:219], v[84:87], v[40:43]\n\t"
      "ds_read_b64_tr_b16 v[234:235], v10 offset:26624\n\t"
      "v_mfma_f32_16x16x32_bf16 v[36:39], v[212:215], v[84:87], v[36:39]\n\t"
      "ds_read_b64_tr_b16 v[236:237], v11 offset:24576\n\t"
      "buffer_load_dwordx4 v[140:143], %[vXsrc], %[srdWS], s50 offen offset:256 sc1\n\t"
      "s_add_u32 s40, s40, 4096\n\t"
      "v_mfma_f32_16x16x32_bf16 v[32:35], v[208:211], v[84:87], v[32:35]\n\t"
      "ds_read_b64_tr_b16 v[238:239], v11 offset:26624\n\t"
      "v_mfma_f32_16x16x32_bf16 v[48:51], v[208:211], v[88:91], v[48:51]\n\t"
      "s_add_u32 m0, s42, 57344\n\t"
      "s_add_u32 s43, s41, 15360\n\t"
      "buffer_load_dwordx4 %[vWsrc], s[44:47], s41 offen lds\n\t"
      "v_mfma_f32_16x16x32_bf16 v[52:55], v[212:215], v[88:91], v[52:55]\n\t"
      "v_mfma_f32_16x16x32_bf16 v[56:59], v[216:219], v[88:91], v[56:59]\n\t"
      "buffer_load_dwordx4 %[vWsrc], s[44:47], s43 offen offset:1024 lds\n\t"
      "s_add_u32 s41, s41, 131072\n\t"
      "v_mfma_f32_16x16x32_bf16 v[60:63], v[220:223], v[88:91], v[60:63]\n\t"
      "v_mfma_f32_16x16x32_bf16 v[76:79], v[220:223], v[92:95], v[76:79]\n\t"
      "s_add_u32 m0, s42, 98304\n\t"
      "s_add_u32 s43, s41, 15360\n\t"
      "buffer_load_dwordx4 %[vWsrc], s[44:47], s41 offen lds\n\t"
      "v_mfma_f32_16x16x32_bf16 v[72:75], v[216:219], v[92:95], v[72:75]\n\t"
      "v_mfma_f32_16x16x32_bf16 v[68:71], v[212:215], v[92:95], v[68:71]\n\t"
      "buffer_load_dwordx4 %[vWsrc], s[44:47], s43 offen offset:1024 lds\n\t"
      "s_add_u32 s41, s41, 131072\n\t"
      "v_mfma_f32_16x16x32_bf16 v[64:67], v[208:211], v[92:95], v[64:67]\n\t"
      "s_waitcnt vmcnt(14) lgkmcnt(0)\n\t"
      "v_mfma_f32_16x16x32_bf16 v[16:19], v[224:227], v[96:99], v[16:19]\n\t"
      "ds_read_b64_tr_b16 v[208:209], v8 offset:32768\n\t"
      "buffer_load_dwordx4 v[144:147], %[vXsrc], %[srdWS], s40 offen offset:0 sc1\n\t"
      "v_mfma_f32_16x16x32_bf16 v[20:23], v[228:231], v[96:99], v[20:23]\n\t"
      "ds_read_b64_tr_b16 v[210:211], v8 offset:34816\n\t"
      "s_add_u32 s50, s40, %[sXo]\n\t"
      "buffer_load_dwordx4 v[148:151], %[vXsrc], %[srdWS], s40 offen offset:256 sc1\n\t"
      "v_mfma_f32_16x16x32_bf16 v[24:27], v[232:235], v[96:99], v[24:27]\n\t"
      "ds_read_b64_tr_b16 v[212:213], v9 offset:32768\n\t"
      "v_mfma_f32_16x16x32_bf16 v[28:31], v[236:239], v[96:99], v[28:31]\n\t"
      "ds_read_b64_tr_b16 v[214:215], v9 offset:34816\n\t"
      "buffer_load_dwordx4 v[152:155], %[vXsrc], %[srdWS], s50 offen offset:0 sc1\n\t"
      "v_mfma_f32_16x16x32_bf16 v[44:47], v[236:239], v[100:103], v[44:47]\n\t"
      "ds_read_b64_tr_b16 v[216:217], v10 offset:32768\n\t"
      "buffer_load_dwordx4 v[156:159], %[vXsrc], %[srdWS], s50 offen offset:256 sc1\n\t"
      "s_add_u32 s40, s40, 4096\n\t"
      "v_mfma_f32_16x16x32_bf16 v[40:43], v[232:235], v[100:103], v[40:43]\n\t"
      "ds_read_b64_tr_b16 v[218:219], v10 offset:34816\n\t"
      "v_mfma_f32_16x16x32_bf16 v[36:39], v[228:231], v[100:103], v[36:39]\n\t"
      "ds_read_b64_tr_b16 v[220:221], v11 offset:32768\n\t"
      "s_add_u32 m0, s42, 106496\n\t"
      "s_add_u32 s43, s41, 15360\n\t"
      "buffer_load_dwordx4 %[vWsrc], s[44:47], s41 offen lds\n\t"
      "v_mfma_f32_16x16x32_bf16 v[32:35], v[224:227], v[100:103], v[32:35]\n\t"
      "ds_read_b64_tr_b16 v[222:223], v11 offset:34816\n\t"
      "v_mfma_f32_16x16x32_bf16 v[48:51], v[224:227], v[104:107], v[48:51]\n\t"
      "buffer_load_dwordx4 %[vWsrc], s[44:47], s43 offen offset:1024 lds\n\t"
      "s_add_u32 s41, s41, 131072\n\t"
      "v_mfma_f32_16x16x32_bf16 v[52:55], v[228:231], v[104:107], v[52:55]\n\t"
      "s_lshl_b32 s43, s42, 1\n\t"
      "s_add_u32 m0, s43, 16384\n\t"
      "s_add_u32 s49, s49, 16384\n\t"
      "s_bitset0_b32 s49, 18\n\t"
      "buffer_load_dwordx4 %[vL16], %[srdWS], s49 offen offset:0 sc1 lds\n\t"
      "v_mfma_f32_16x16x32_bf16 v[56:59], v[232:235], v[104:107], v[56:59]\n\t"
      "v_mfma_f32_16x16x32_bf16 v[60:63], v[236:239], v[104:107], v[60:63]\n\t"
      "buffer_load_dwordx4 %[vL16], %[srdWS], s49 offen offset:1024 sc1 lds\n\t"
      "v_mfma_f32_16x16x32_bf16 v[76:79], v[236:239], v[108:111], v[76:79]\n\t"
      "buffer_load_dwordx4 %[vL16], %[srdWS], s49 offen offset:2048 sc1 lds\n\t"
      "v_mfma_f32_16x16x32_bf16 v[72:75], v[232:235], v[108:111], v[72:75]\n\t"
      "v_mfma_f32_16x16x32_bf16 v[68:71], v[228:231], v[108:111], v[68:71]\n\t"
      "buffer_load_dwordx4 %[vL16], %[srdWS], s49 offen offset:3072 sc1 lds\n\t"
      "v_mfma_f32_16x16x32_bf16 v[64:67], v[224:227], v[108:111], v[64:67]\n\t"
      "s_waitcnt vmcnt(18) lgkmcnt(0)\n\t"
      "v_mfma_f32_16x16x32_bf16 v[16:19], v[208:211], v[112:115], v[16:19]\n\t"
      "ds_read_b64_tr_b16 v[224:225], v8 offset:40960\n\t"
      "buffer_load_dwordx4 v[160:163], %[vXsrc], %[srdWS], s40 offen offset:0 sc1\n\t"
      "v_mfma_f32_16x16x32_bf16 v[20:23], v[212:215], v[112:115], v[20:23]\n\t"
      "ds_read_b64_tr_b16 v[226:227], v8 offset:43008\n\t"
      "v_mfma_f32_16x16x32_bf16 v[24:27], v[216:219], v[112:115], v[24:27]\n\t"
      "ds_read_b64_tr_b16 v[228:229], v9 offset:40960\n\t"
      "s_add_u32 s50, s40, %[sXo]\n\t"
      "buffer_load_dwordx4 v[164:167], %[vXsrc], %[srdWS], s40 offen offset:256 sc1\n\t"
      "v_mfma_f32_16x16x32_bf16 v[28:31], v[220:223], v[112:115], v[28:31]\n\t"
      "ds_read_b64_tr_b16 v[230:231], v9 offset:43008\n\t"
      "v_mfma_f32_16x16x32_bf16 v[44:47], v[220:223], v[116:119], v[44:47]\n\t"
      "ds_read_b64_tr_b16 v[232:233], v10 offset:40960\n\t"
      "buffer_load_dwordx4 v[168:171], %[vXsrc], %[srdWS], s50 offen offset:0 sc1\n\t"
      "v_mfma_f32_16x16x32_bf16 v[40:43], v[216:219], v[116:119], v[40:43]\n\t"
      "ds_read_b64_tr_b16 v[234:235], v10 offset:43008\n\t"
      "v_mfma_f32_16x16x32_bf16 v[36:39], v[212:215], v[116:119], v[36:39]\n\t"
      "ds_read_b64_tr_b16 v[236:237], v11 offset:40960\n\t"
      "buffer_load_dwordx4 v[172:175], %[vXsrc], %[srdWS], s50 offen offset:256 sc1\n\t"
      "s_add_u32 s40, s40, 4096\n\t"
      "v_mfma_f32_16x16x32_bf16 v[32:35], v[208:211], v[116:119], v[32:35]\n\t"
      "ds_read_b64_tr_b16 v[238:239], v11 offset:43008\n\t"
      "v_mfma_f32_16x16x32_bf16 v[48:51], v[208:211], v[120:123], v[48:51]\n\t"
      "s_add_u32 m0, s42, 114688\n\t"
      "s_add_u32 s43, s41, 15360\n\t"
      "buffer_load_dwordx4 %[vWsrc], s[44:47], s41 offen lds\n\t"
      "v_mfma_f32_16x16x32_bf16 v[52:55], v[212:215], v[120:123], v[52:55]\n\t"
      "v_mfma_f32_16x16x32_bf16 v[56:59], v[216:219], v[120:123], v[56:59]\n\t"
      "buffer_load_dwordx4 %[vWsrc], s[44:47], s43 offen offset:1024 lds\n\t"
      "s_add_u32 s41, s41, 131072\n\t"
      "v_mfma_f32_16x16x32_bf16 v[60:63], v[220:223], v[120:123], v[60:63]\n\t"
      "v_mfma_f32_16x16x32_bf16 v[76:79], v[220:223], v[124:127], v[76:79]\n\t"
      "v_mfma_f32_16x16x32_bf16 v[72:75], v[216:219], v[124:127], v[72:75]\n\t"
      "v_mfma_f32_16x16x32_bf16 v[68:71], v[212:215], v[124:127], v[68:71]\n\t"
      "v_mfma_f32_16x16x32_bf16 v[64:67], v[208:211], v[124:127], v[64:67]\n\t"
      "s_waitcnt vmcnt(24) lgkmcnt(0)\n\t"
      "s_barrier\n\t"
      "s_waitcnt vmcnt(20) lgkmcnt(0)\n\t"
      "v_mfma_f32_16x16x32_bf16 v[16:19], v[224:227], v[128:131], v[16:19]\n\t"
      "ds_read_b64_tr_b16 v[208:209], v8 offset:49152\n\t"
      "buffer_load_dwordx4 v[176:179], %[vXsrc], %[srdWS], s40 offen offset:0 sc1\n\t"
      "v_mfma_f32_16x16x32_bf16 v[20:23], v[228:231], v[128:131], v[20:23]\n\t"
      "ds_read_b64_tr_b16 v[210:211], v8 offset:51200\n\t"
      "v_mfma_f32_16x16x32_bf16 v[24:27], v[232:235], v[128:131], v[24:27]\n\t"
      "ds_read_b64_tr_b16 v[212:213], v9 offset:49152\n\t"
      "s_add_u32 s50, s40, %[sXo]\n\t"
      "buffer_load_dwordx4 v[180:183], %[vXsrc], %[srdWS], s40 offen offset:256 sc1\n\t"
      "v_mfma_f32_16x16x32_bf16 v[28:31], v[236:239], v[128:131], v[28:31]\n\t"
      "ds_read_b64_tr_b16 v[214:215], v9 offset:51200\n\t"
      "v_mfma_f32_16x16x32_bf16 v[44:47], v[236:239], v[132:135], v[44:47]\n\t"
      "ds_read_b64_tr_b16 v[216:217], v10 offset:49152\n\t"
      "buffer_load_dwordx4 v[184:187], %[vXsrc], %[srdWS], s50 offen offset:0 sc1\n\t"
      "v_mfma_f32_16x16x32_bf16 v[40:43], v[232:235], v[132:135], v[40:43]\n\t"
      "ds_read_b64_tr_b16 v[218:219], v10 offset:51200\n\t"
      "v_mfma_f32_16x16x32_bf16 v[36:39], v[228:231], v[132:135], v[36:39]\n\t"
      "ds_read_b64_tr_b16 v[220:221], v11 offset:49152\n\t"
      "buffer_load_dwordx4 v[188:191], %[vXsrc], %[srdWS], s50 offen offset:256 sc1\n\t"
      "s_add_u32 s40, s40, 4096\n\t"
      "v_mfma_f32_16x16x32_bf16 v[32:35], v[224:227], v[132:135], v[32:35]\n\t"
      "ds_read_b64_tr_b16 v[222:223], v11 offset:51200\n\t"
      "v_mfma_f32_16x16x32_bf16 v[48:51], v[224:227], v[136:139], v[48:51]\n\t"
      "s_add_u32 m0, s42, 122880\n\t"
      "s_add_u32 s43, s41, 15360\n\t"
      "buffer_load_dwordx4 %[vWsrc], s[44:47], s41 offen lds\n\t"
      "v_mfma_f32_16x16x32_bf16 v[52:55], v[228:231], v[136:139], v[52:55]\n\t"
      "v_mfma_f32_16x16x32_bf16 v[56:59], v[232:235], v[136:139], v[56:59]\n\t"
      "buffer_load_dwordx4 %[vWsrc], s[44:47], s43 offen offset:1024 lds\n\t"
      "s_add_u32 s41, s41, 131072\n\t"
      "v_mfma_f32_16x16x32_bf16 v[60:63], v[236:239], v[136:139], v[60:63]\n\t"
      "v_mfma_f32_16x16x32_bf16 v[76:79], v[236:239], v[140:143], v[76:79]\n\t"
      "v_mfma_f32_16x16x32_bf16 v[72:75], v[232:235], v[140:143], v[72:75]\n\t"
      "v_mfma_f32_16x16x32_bf16 v[68:71], v[228:231], v[140:143], v[68:71]\n\t"
      "v_mfma_f32_16x16x32_bf16 v[64:67], v[224:227], v[140:143], v[64:67]\n\t"
      "s_waitcnt vmcnt(18) lgkmcnt(0)\n\t"
      "v_mfma_f32_16x16x32_bf16 v[16:19], v[208:211], v[144:147], v[16:19]\n\t"
      "ds_read_b64_tr_b16 v[224:225], v8 offset:57344\n\t"
      "buffer_load_dwordx4 v[192:195], %[vXsrc], %[srdWS], s40 offen offset:0 sc1\n\t"
      "v_mfma_f32_16x16x32_bf16 v[20:23], v[212:215], v[144:147], v[20:23]\n\t"
      "ds_read_b64_tr_b16 v[226:227], v8 offset:59392\n\t"
      "s_add_u32 s50, s40, %[sXo]\n\t"
      "buffer_load_dwordx4 v[196:199], %[vXsrc], %[srdWS], s40 offen offset:256 sc1\n\t"
      "v_mfma_f32_16x16x32_bf16 v[24:27], v[216:219], v[144:147], v[24:27]\n\t"
      "ds_read_b64_tr_b16 v[228:229], v9 offset:57344\n\t"
      "v_mfma_f32_16x16x32_bf16 v[28:31], v[220:223], v[144:147], v[28:31]\n\t"
      "ds_read_b64_tr_b16 v[230:231], v9 offset:59392\n\t"
      "buffer_load_dwordx4 v[200:203], %[vXsrc], %[srdWS], s50 offen offset:0 sc1\n\t"
      "v_mfma_f32_16x16x32_bf16 v[44:47], v[220:223], v[148:151], v[44:47]\n\t"
      "ds_read_b64_tr_b16 v[232:233], v10 offset:57344\n\t"
      "buffer_load_dwordx4 v[204:207], %[vXsrc], %[srdWS], s50 offen offset:256 sc1\n\t"
      "s_add_u32 s40, s40, 4096\n\t"
      "v_mfma_f32_16x16x32_bf16 v[40:43], v[216:219], v[148:151], v[40:43]\n\t"
      "ds_read_b64_tr_b16 v[234:235], v10 offset:59392\n\t"
      "v_mfma_f32_16x16x32_bf16 v[36:39], v[212:215], v[148:151], v[36:39]\n\t"
      "ds_read_b64_tr_b16 v[236:237], v11 offset:57344\n\t"
      "s_add_u32 m0, s42, 131072\n\t"
      "s_add_u32 s43, s41, 15360\n\t"
      "buffer_load_dwordx4 %[vWsrc], s[44:47], s41 offen lds\n\t"
      "v_mfma_f32_16x16x32_bf16 v[32:35], v[208:211], v[148:151], v[32:35]\n\t"
      "ds_read_b64_tr_b16 v[238:239], v11 offset:59392\n\t"
      "v_mfma_f32_16x16x32_bf16 v[48:51], v[208:211], v[152:155], v[48:51]\n\t"
      "buffer_load_dwordx4 %[vWsrc], s[44:47], s43 offen offset:1024 lds\n\t"
      "s_add_u32 s41, s41, 131072\n\t"
      "v_mfma_f32_16x16x32_bf16 v[52:55], v[212:215], v[152:155], v[52:55]\n\t"
      "s_lshl_b32 s43, s42, 1\n\t"
      "s_add_u32 m0, s43, 65536\n\t"
      "s_add_u32 s49, s49, 16384\n\t"
      "s_bitset0_b32 s49, 18\n\t"
      "buffer_load_dwordx4 %[vL16], %[srdWS], s49 offen offset:0 sc1 lds\n\t"
      "v_mfma_f32_16x16x32_bf16 v[56:59], v[216:219], v[152:155], v[56:59]\n\t"
      "v_mfma_f32_16x16x32_bf16 v[60:63], v[220:223], v[152:155], v[60:63]\n\t"
      "buffer_load_dwordx4 %[vL16], %[srdWS], s49 offen offset:1024 sc1 lds\n\t"
      "v_mfma_f32_16x16x32_bf16 v[76:79], v[220:223], v[156:159], v[76:79]\n\t"
      "buffer_load_dwordx4 %[vL16], %[srdWS], s49 offen offset:2048 sc1 lds\n\t"
      "v_mfma_f32_16x16x32_bf16 v[72:75], v[216:219], v[156:159], v[72:75]\n\t"
      "v_mfma_f32_16x16x32_bf16 v[68:71], v[212:215], v[156:159], v[68:71]\n\t"
      "buffer_load_dwordx4 %[vL16], %[srdWS], s49 offen offset:3072 sc1 lds\n\t"
      "v_mfma_f32_16x16x32_bf16 v[64:67], v[208:211], v[156:159], v[64:67]\n\t"
      "s_waitcnt vmcnt(18) lgkmcnt(0)\n\t"
      "v_mfma_f32_16x16x32_bf16 v[16:19], v[224:227], v[160:163], v[16:19]\n\t"
      "v_add_u32 v8, 4294901760, v8\n\t"
      "v_add_u32 v9, 4294901760, v9\n\t"
      "v_add_u32 v10, 4294901760, v10\n\t"
      "v_add_u32 v11, 4294901760, v11\n\t"
      "ds_read_b64_tr_b16 v[208:209], v8 offset:0\n\t"
      "buffer_load_dwordx4 v[80:83], %[vXsrc], %[srdWS], s40 offen offset:0 sc1\n\t"
      "v_mfma_f32_16x16x32_bf16 v[20:23], v[228:231], v[160:163], v[20:23]\n\t"
      "ds_read_b64_tr_b16 v[210:211], v8 offset:2048\n\t"
      "v_mfma_f32_16x16x32_bf16 v[24:27], v[232:235], v[160:163], v[24:27]\n\t"
      "ds_read_b64_tr_b16 v[212:213], v9 offset:0\n\t"
      "s_add_u32 s50, s40, %[sXo]\n\t"
      "buffer_load_dwordx4 v[84:87], %[vXsrc], %[srdWS], s40 offen offset:256 sc1\n\t"
      "v_mfma_f32_16x16x32_bf16 v[28:31], v[236:239], v[160:163], v[28:31]\n\t"
      "ds_read_b64_tr_b16 v[214:215], v9 offset:2048\n\t"
      "v_mfma_f32_16x16x32_bf16 v[44:47], v[236:239], v[164:167], v[44:47]\n\t"
      "ds_read_b64_tr_b16 v[216:217], v10 offset:0\n\t"
      "buffer_load_dwordx4 v[88:91], %[vXsrc], %[srdWS], s50 offen offset:0 sc1\n\t"
      "v_mfma_f32_16x16x32_bf16 v[40:43], v[232:235], v[164:167], v[40:43]\n\t"
      "ds_read_b64_tr_b16 v[218:219], v10 offset:2048\n\t"
      "v_mfma_f32_16x16x32_bf16 v[36:39], v[228:231], v[164:167], v[36:39]\n\t"
      "ds_read_b64_tr_b16 v[220:221], v11 offset:0\n\t"
      "buffer_load_dwordx4 v[92:95], %[vXsrc], %[srdWS], s50 offen offset:256 sc1\n\t"
      "s_add_u32 s40, s40, 4096\n\t"
      "v_mfma_f32_16x16x32_bf16 v[32:35], v[224:227], v[164:167], v[32:35]\n\t"
      "ds_read_b64_tr_b16 v[222:223], v11 offset:2048\n\t"
      "v_mfma_f32_16x16x32_bf16 v[48:51], v[224:227], v[168:171], v[48:51]\n\t"
      "s_add_u32 m0, s42, 139264\n\t"
      "s_add_u32 s43, s41, 15360\n\t"
      "buffer_load_dwordx4 %[vWsrc], s[44:47], s41 offen lds\n\t"
      "v_mfma_f32_16x16x32_bf16 v[52:55], v[228:231], v[168:171], v[52:55]\n\t"
      "v_mfma_f32_16x16x32_bf16 v[56:59], v[232:235], v[168:171], v[56:59]\n\t"
      "buffer_load_dwordx4 %[vWsrc], s[44:47], s43 offen offset:1024 lds\n\t"
      "s_add_u32 s41, s41, 131072\n\t"
      "v_mfma_f32_16x16x32_bf16 v[60:63], v[236:239], v[168:171], v[60:63]\n\t"
      "v_mfma_f32_16x16x32_bf16 v[76:79], v[236:239], v[172:175], v[76:79]\n\t"
      "v_mfma_f32_16x16x32_bf16 v[72:75], v[232:235], v[172:175], v[72:75]\n\t"
      "v_mfma_f32_16x16x32_bf16 v[68:71], v[228:231], v[172:175], v[68:71]\n\t"
      "v_mfma_f32_16x16x32_bf16 v[64:67], v[224:227], v[172:175], v[64:67]\n\t"
      "s_waitcnt vmcnt(18) lgkmcnt(0)\n\t"
      "v_mfma_f32_16x16x32_bf16 v[16:19], v[208:211], v[176:179], v[16:19]\n\t"
      "ds_read_b64_tr_b16 v[224:225], v8 offset:8192\n\t"
      "buffer_load_dwordx4 v[96:99], %[vXsrc], %[srdWS], s40 offen offset:0 sc1\n\t"
      "v_mfma_f32_16x16x32_bf16 v[20:23], v[212:215], v[176:179], v[20:23]\n\t"
      "ds_read_b64_tr_b16 v[226:227], v8 offset:10240\n\t"
      "v_mfma_f32_16x16x32_bf16 v[24:27], v[216:219], v[176:179], v[24:27]\n\t"
      "ds_read_b64_tr_b16 v[228:229], v9 offset:8192\n\t"
      "s_add_u32 s50, s40, %[sXo]\n\t"
      "buffer_load_dwordx4 v[100:103], %[vXsrc], %[srdWS], s40 offen offset:256 sc1\n\t"
      "v_mfma_f32_16x16x32_bf16 v[28:31], v[220:223], v[176:179], v[28:31]\n\t"
      "ds_read_b64_tr_b16 v[230:231], v9 offset:10240\n\t"
      "v_mfma_f32_16x16x32_bf16 v[44:47], v[220:223], v[180:183], v[44:47]\n\t"
      "ds_read_b64_tr_b16 v[232:233], v10 offset:8192\n\t"
      "buffer_load_dwordx4 v[104:107], %[vXsrc], %[srdWS], s50 offen offset:0 sc1\n\t"
      "v_mfma_f32_16x16x32_bf16 v[40:43], v[216:219], v[180:183], v[40:43]\n\t"
      "ds_read_b64_tr_b16 v[234:235], v10 offset:10240\n\t"
      "v_mfma_f32_16x16x32_bf16 v[36:39], v[212:215], v[180:183], v[36:39]\n\t"
      "ds_read_b64_tr_b16 v[236:237], v11 offset:8192\n\t"
      "buffer_load_dwordx4 v[108:111], %[vXsrc], %[srdWS], s50 offen offset:256 sc1\n\t"
      "s_add_u32 s40, s40, 4096\n\t"
      "v_mfma_f32_16x16x32_bf16 v[32:35], v[208:211], v[180:183], v[32:35]\n\t"
      "ds_read_b64_tr_b16 v[238:239], v11 offset:10240\n\t"
      "v_mfma_f32_16x16x32_bf16 v[48:51], v[208:211], v[184:187], v[48:51]\n\t"
      "v_mfma_f32_16x16x32_bf16 v[52:55], v[212:215], v[184:187], v[52:55]\n\t"
      "v_mfma_f32_16x16x32_bf16 v[56:59], v[216:219], v[184:187], v[56:59]\n\t"
      "v_mfma_f32_16x16x32_bf16 v[60:63], v[220:223], v[184:187], v[60:63]\n\t"
      "v_mfma_f32_16x16x32_bf16 v[76:79], v[220:223], v[188:191], v[76:79]\n\t"
      "v_mfma_f32_16x16x32_bf16 v[72:75], v[216:219], v[188:191], v[72:75]\n\t"
      "v_mfma_f32_16x16x32_bf16 v[68:71], v[212:215], v[188:191], v[68:71]\n\t"
      "v_mfma_f32_16x16x32_bf16 v[64:67], v[208:211], v[188:191], v[64:67]\n\t"
      "s_waitcnt vmcnt(22) lgkmcnt(0)\n\t"
      "s_barrier\n\t"
      "s_waitcnt vmcnt(16) lgkmcnt(0)\n\t"
      "v_mfma_f32_16x16x32_bf16 v[16:19], v[224:227], v[192:195], v[16:19]\n\t"
      "ds_read_b64_tr_b16 v[208:209], v8 offset:16384\n\t"
      "buffer_load_dwordx4 v[112:115], %[vXsrc], %[srdWS], s40 offen offset:0 sc1\n\t"
      "v_mfma_f32_16x16x32_bf16 v[20:23], v[228:231], v[192:195], v[20:23]\n\t"
      "ds_read_b64_tr_b16 v[210:211], v8 offset:18432\n\t"
      "v_mfma_f32_16x16x32_bf16 v[24:27], v[232:235], v[192:195], v[24:27]\n\t"
      "ds_read_b64_tr_b16 v[212:213], v9 offset:16384\n\t"
      "s_add_u32 s50, s40, %[sXo]\n\t"
      "buffer_load_dwordx4 v[116:119], %[vXsrc], %[srdWS], s40 offen offset:256 sc1\n\t"
      "v_mfma_f32_16x16x32_bf16 v[28:31], v[236:239], v[192:195], v[28:31]\n\t"
      "ds_read_b64_tr_b16 v[214:215], v9 offset:18432\n\t"
      "v_mfma_f32_16x16x32_bf16 v[44:47], v[236:239], v[196:199], v[44:47]\n\t"
      "ds_read_b64_tr_b16 v[216:217], v10 offset:16384\n\t"
      "buffer_load_dwordx4 v[120:123], %[vXsrc], %[srdWS], s50 offen offset:0 sc1\n\t"
      "v_mfma_f32_16x16x32_bf16 v[40:43], v[232:235], v[196:199], v[40:43]\n\t"
      "ds_read_b64_tr_b16 v[218:219], v10 offset:18432\n\t"
      "v_mfma_f32_16x16x32_bf16 v[36:39], v[228:231], v[196:199], v[36:39]\n\t"
      "ds_read_b64_tr_b16 v[220:221], v11 offset:16384\n\t"
      "buffer_load_dwordx4 v[124:127], %[vXsrc], %[srdWS], s50 offen offset:256 sc1\n\t"
      "s_add_u32 s40, s40, 4096\n\t"
      "v_mfma_f32_16x16x32_bf16 v[32:35], v[224:227], v[196:199], v[32:35]\n\t"
      "ds_read_b64_tr_b16 v[222:223], v11 offset:18432\n\t"
      "v_mfma_f32_16x16x32_bf16 v[48:51], v[224:227], v[200:203], v[48:51]\n\t"
      "s_add_u32 m0, s42, 147456\n\t"
      "s_add_u32 s43, s41, 15360\n\t"
      "buffer_load_dwordx4 %[vWsrc], s[44:47], s41 offen lds\n\t"
      "v_mfma_f32_16x16x32_bf16 v[52:55], v[228:231], v[200:203], v[52:55]\n\t"
      "v_mfma_f32_16x16x32_bf16 v[56:59], v[232:235], v[200:203], v[56:59]\n\t"
      "buffer_load_dwordx4 %[vWsrc], s[44:47], s43 offen offset:1024 lds\n\t"
      "s_add_u32 s41, s41, 131072\n\t"
      "v_mfma_f32_16x16x32_bf16 v[60:63], v[236:239], v[200:203], v[60:63]\n\t"
      "v_mfma_f32_16x16x32_bf16 v[76:79], v[236:239], v[204:207], v[76:79]\n\t"
      "s_add_u32 m0, s42, 155648\n\t"
      "s_add_u32 s43, s41, 15360\n\t"
      "buffer_load_dwordx4 %[vWsrc], s[44:47], s41 offen lds\n\t"
      "v_mfma_f32_16x16x32_bf16 v[72:75], v[232:235], v[204:207], v[72:75]\n\t"
      "v_mfma_f32_16x16x32_bf16 v[68:71], v[228:231], v[204:207], v[68:71]\n\t"
      "buffer_load_dwordx4 %[vWsrc], s[44:47], s43 offen offset:1024 lds\n\t"
      "s_add_u32 s41, s41, 131072\n\t"
      "v_mfma_f32_16x16x32_bf16 v[64:67], v[224:227], v[204:207], v[64:67]\n\t"
      "s_waitcnt vmcnt(14) lgkmcnt(0)\n\t"
      "v_mfma_f32_16x16x32_bf16 v[16:19], v[208:211], v[80:83], v[16:19]\n\t"
      "ds_read_b64_tr_b16 v[224:225], v8 offset:24576\n\t"
      "buffer_load_dwordx4 v[128:131], %[vXsrc], %[srdWS], s40 offen offset:0 sc1\n\t"
      "v_mfma_f32_16x16x32_bf16 v[20:23], v[212:215], v[80:83], v[20:23]\n\t"
      "ds_read_b64_tr_b16 v[226:227], v8 offset:26624\n\t"
      "s_add_u32 s50, s40, %[sXo]\n\t"
      "buffer_load_dwordx4 v[132:135], %[vXsrc], %[srdWS], s40 offen offset:256 sc1\n\t"
      "v_mfma_f32_16x16x32_bf16 v[24:27], v[216:219], v[80:83], v[24:27]\n\t"
      "ds_read_b64_tr_b16 v[228:229], v9 offset:24576\n\t"
      "v_mfma_f32_16x16x32_bf16 v[28:31], v[220:223], v[80:83], v[28:31]\n\t"
      "ds_read_b64_tr_b16 v[230:231], v9 offset:26624\n\t"
      "buffer_load_dwordx4 v[136:139], %[vXsrc], %[srdWS], s50 offen offset:0 sc1\n\t"
      "v_mfma_f32_16x16x32_bf16 v[44:47], v[220:223], v[84:87], v[44:47]\n\t"
      "ds_read_b64_tr_b16 v[232:233], v10 offset:24576\n\t"
      "buffer_load_dwordx4 v[140:143], %[vXsrc], %[srdWS], s50 offen offset:256 sc1\n\t"
      "s_add_u32 s40, s40, 4096\n\t"
      "v_mfma_f32_16x16x32_bf16 v[40:43], v[216:219], v[84:87], v[40:43]\n\t"
      "ds_read_b64_tr_b16 v[234:235], v10 offset:26624\n\t"
      "v_mfma_f32_16x16x32_bf16 v[36:39], v[212:215], v[84:87], v[36:39]\n\t"
      "ds_read_b64_tr_b16 v[236:237], v11 offset:24576\n\t"
      "s_add_u32 m0, s42, 32768\n\t"
      "s_add_u32 s43, s41, 15360\n\t"
      "buffer_load_dwordx4 %[vWsrc], s[44:47], s41 offen lds\n\t"
      "v_mfma_f32_16x16x32_bf16 v[32:35], v[208:211], v[84:87], v[32:35]\n\t"
      "ds_read_b64_tr_b16 v[238:239], v11 offset:26624\n\t"
      "v_mfma_f32_16x16x32_bf16 v[48:51], v[208:211], v[88:91], v[48:51]\n\t"
      "buffer_load_dwordx4 %[vWsrc], s[44:47], s43 offen offset:1024 lds\n\t"
      "s_add_u32 s41, s41, 131072\n\t"
      "v_mfma_f32_16x16x32_bf16 v[52:55], v[212:215], v[88:91], v[52:55]\n\t"
      "s_lshl_b32 s43, s42, 1\n\t"
      "s_add_u32 m0, s43, 81920\n\t"
      "s_add_u32 s49, s49, 16384\n\t"
      "s_bitset0_b32 s49, 18\n\t"
      "buffer_load_dwordx4 %[vL16], %[srdWS], s49 offen offset:0 sc1 lds\n\t"
      "v_mfma_f32_16x16x32_bf16 v[56:59], v[216:219], v[88:91], v[56:59]\n\t"
      "v_mfma_f32_16x16x32_bf16 v[60:63], v[220:223], v[88:91], v[60:63]\n\t"
      "buffer_load_dwordx4 %[vL16], %[srdWS], s49 offen offset:1024 sc1 lds\n\t"
      "v_mfma_f32_16x16x32_bf16 v[76:79], v[220:223], v[92:95], v[76:79]\n\t"
      "buffer_load_dwordx4 %[vL16], %[srdWS], s49 offen offset:2048 sc1 lds\n\t"
      "v_mfma_f32_16x16x32_bf16 v[72:75], v[216:219], v[92:95], v[72:75]\n\t"
      "v_mfma_f32_16x16x32_bf16 v[68:71], v[212:215], v[92:95], v[68:71]\n\t"
      "buffer_load_dwordx4 %[vL16], %[srdWS], s49 offen offset:3072 sc1 lds\n\t"
      "v_mfma_f32_16x16x32_bf16 v[64:67], v[208:211], v[92:95], v[64:67]\n\t"
      "s_waitcnt vmcnt(18) lgkmcnt(0)\n\t"
      "v_mfma_f32_16x16x32_bf16 v[16:19], v[224:227], v[96:99], v[16:19]\n\t"
      "v_add_u32 v8, 65536, v8\n\t"
      "v_add_u32 v9, 65536, v9\n\t"
      "v_add_u32 v10, 65536, v10\n\t"
      "v_add_u32 v11, 65536, v11\n\t"
      "ds_read_b64_tr_b16 v[208:209], v8 offset:0\n\t"
      "buffer_load_dwordx4 v[144:147], %[vXsrc], %[srdWS], s40 offen offset:0 sc1\n\t"
      "v_mfma_f32_16x16x32_bf16 v[20:23], v[228:231], v[96:99], v[20:23]\n\t"
      "ds_read_b64_tr_b16 v[210:211], v8 offset:2048\n\t"
      "s_add_u32 s50, s40, %[sXo]\n\t"
      "buffer_load_dwordx4 v[148:151], %[vXsrc], %[srdWS], s40 offen offset:256 sc1\n\t"
      "v_mfma_f32_16x16x32_bf16 v[24:27], v[232:235], v[96:99], v[24:27]\n\t"
      "ds_read_b64_tr_b16 v[212:213], v9 offset:0\n\t"
      "v_mfma_f32_16x16x32_bf16 v[28:31], v[236:239], v[96:99], v[28:31]\n\t"
      "ds_read_b64_tr_b16 v[214:215], v9 offset:2048\n\t"
      "buffer_load_dwordx4 v[152:155], %[vXsrc], %[srdWS], s50 offen offset:0 sc1\n\t"
      "v_mfma_f32_16x16x32_bf16 v[44:47], v[236:239], v[100:103], v[44:47]\n\t"
      "ds_read_b64_tr_b16 v[216:217], v10 offset:0\n\t"
      "buffer_load_dwordx4 v[156:159], %[vXsrc], %[srdWS], s50 offen offset:256 sc1\n\t"
      "s_add_u32 s40, s40, 4096\n\t"
      "v_mfma_f32_16x16x32_bf16 v[40:43], v[232:235], v[100:103], v[40:43]\n\t"
      "ds_read_b64_tr_b16 v[218:219], v10 offset:2048\n\t"
      "v_mfma_f32_16x16x32_bf16 v[36:39], v[228:231], v[100:103], v[36:39]\n\t"
      "ds_read_b64_tr_b16 v[220:221], v11 offset:0\n\t"
      "buffer_load_dwordx4 v[160:163], %[vXsrc], %[srdWS], s40 offen offset:0 sc1\n\t"
      "v_mfma_f32_16x16x32_bf16 v[32:35], v[224:227], v[100:103], v[32:35]\n\t"
      "ds_read_b64_tr_b16 v[222:223], v11 offset:2048\n\t"
      "v_mfma_f32_16x16x32_bf16 v[48:51], v[224:227], v[104:107], v[48:51]\n\t"
      "s_add_u32 s50, s40, %[sXo]\n\t"
      "buffer_load_dwordx4 v[164:167], %[vXsrc], %[srdWS], s40 offen offset:256 sc1\n\t"
      "v_mfma_f32_16x16x32_bf16 v[52:55], v[228:231], v[104:107], v[52:55]\n\t"
      "buffer_load_dwordx4 v[168:171], %[vXsrc], %[srdWS], s50 offen offset:0 sc1\n\t"
      "v_mfma_f32_16x16x32_bf16 v[56:59], v[232:235], v[104:107], v[56:59]\n\t"
      "v_mfma_f32_16x16x32_bf16 v[60:63], v[236:239], v[104:107], v[60:63]\n\t"
      "buffer_load_dwordx4 v[172:175], %[vXsrc], %[srdWS], s50 offen offset:256 sc1\n\t"
      "s_add_u32 s40, s40, 4096\n\t"
      "v_mfma_f32_16x16x32_bf16 v[76:79], v[236:239], v[108:111], v[76:79]\n\t"
      "s_add_u32 m0, s42, 40960\n\t"
      "s_add_u32 s43, s41, 15360\n\t"
      "buffer_load_dwordx4 %[vWsrc], s[44:47], s41 offen lds\n\t"
      "v_mfma_f32_16x16x32_bf16 v[72:75], v[232:235], v[108:111], v[72:75]\n\t"
      "v_mfma_f32_16x16x32_bf16 v[68:71], v[228:231], v[108:111], v[68:71]\n\t"
      "buffer_load_dwordx4 %[vWsrc], s[44:47], s43 offen offset:1024 lds\n\t"
      "s_add_u32 s41, s41, 131072\n\t"
      "v_mfma_f32_16x16x32_bf16 v[64:67], v[224:227], v[108:111], v[64:67]\n\t"
      "s_waitcnt vmcnt(28) lgkmcnt(0)\n\t"
      "s_barrier\n\t"
      "s_waitcnt vmcnt(24) lgkmcnt(0)\n\t"
      "v_mfma_f32_16x16x32_bf16 v[16:19], v[208:211], v[112:115], v[16:19]\n\t"
      "ds_read_b64_tr_b16 v[224:225], v8 offset:8192\n\t"
      "buffer_load_dwordx4 v[176:179], %[vXsrc], %[srdWS], s40 offen offset:0 sc1\n\t"
      "v_mfma_f32_16x16x32_bf16 v[20:23], v[212:215], v[112:115], v[20:23]\n\t"
      "ds_read_b64_tr_b16 v[226:227], v8 offset:10240\n\t"
      "s_add_u32 s50, s40, %[sXo]\n\t"
      "buffer_load_dwordx4 v[180:183], %[vXsrc], %[srdWS], s40 offen offset:256 sc1\n\t"
      "v_mfma_f32_16x16x32_bf16 v[24:27], v[216:219], v[112:115], v[24:27]\n\t"
      "ds_read_b64_tr_b16 v[228:229], v9 offset:8192\n\t"
      "buffer_load_dwordx4 v[184:187], %[vXsrc], %[srdWS], s50 offen offset:0 sc1\n\t"
      "v_mfma_f32_16x16x32_bf16 v[28:31], v[220:223], v[112:115], v[28:31]\n\t"
      "ds_read_b64_tr_b16 v[230:231], v9 offset:10240\n\t"
      "v_mfma_f32_16x16x32_bf16 v[44:47], v[220:223], v[116:119], v[44:47]\n\t"
      "ds_read_b64_tr_b16 v[232:233], v10 offset:8192\n\t"
      "buffer_load_dwordx4 v[188:191], %[vXsrc], %[srdWS], s50 offen offset:256 sc1\n\t"
      "s_add_u32 s40, s40, 4096\n\t"
      "v_mfma_f32_16x16x32_bf16 v[40:43], v[216:219], v[116:119], v[40:43]\n\t"
      "ds_read_b64_tr_b16 v[234:235], v10 offset:10240\n\t"
      "buffer_load_dwordx4 v[192:195], %[vXsrc], %[srdWS], s40 offen offset:0 sc1\n\t"
      "v_mfma_f32_16x16x32_bf16 v[36:39], v[212:215], v[116:119], v[36:39]\n\t"
      "ds_read_b64_tr_b16 v[236:237], v11 offset:8192\n\t"
      "s_add_u32 s50, s40, %[sXo]\n\t"
      "buffer_load_dwordx4 v[196:199], %[vXsrc], %[srdWS], s40 offen offset:256 sc1\n\t"
      "v_mfma_f32_16x16x32_bf16 v[32:35], v[208:211], v[116:119], v[32:35]\n\t"
      "ds_read_b64_tr_b16 v[238:239], v11 offset:10240\n\t"
      "v_mfma_f32_16x16x32_bf16 v[48:51], v[208:211], v[120:123], v[48:51]\n\t"
      "buffer_load_dwordx4 v[200:203], %[vXsrc], %[srdWS], s50 offen offset:0 sc1\n\t"
      "v_mfma_f32_16x16x32_bf16 v[52:55], v[212:215], v[120:123], v[52:55]\n\t"
      "buffer_load_dwordx4 v[204:207], %[vXsrc], %[srdWS], s50 offen offset:256 sc1\n\t"
      "s_add_u32 s40, s40, 4096\n\t"
      "v_mfma_f32_16x16x32_bf16 v[56:59], v[216:219], v[120:123], v[56:59]\n\t"
      "s_add_u32 m0, s42, 49152\n\t"
      "s_add_u32 s43, s41, 15360\n\t"
      "buffer_load_dwordx4 %[vWsrc], s[44:47], s41 offen lds\n\t"
      "v_mfma_f32_16x16x32_bf16 v[60:63], v[220:223], v[120:123], v[60:63]\n\t"
      "v_mfma_f32_16x16x32_bf16 v[76:79], v[220:223], v[124:127], v[76:79]\n\t"
      "buffer_load_dwordx4 %[vWsrc], s[44:47], s43 offen offset:1024 lds\n\t"
      "s_add_u32 s41, s41, 131072\n\t"
      "v_mfma_f32_16x16x32_bf16 v[72:75], v[216:219], v[124:127], v[72:75]\n\t"
      "s_add_u32 m0, s42, 57344\n\t"
      "s_add_u32 s43, s41, 15360\n\t"
      "buffer_load_dwordx4 %[vWsrc], s[44:47], s41 offen lds\n\t"
      "v_mfma_f32_16x16x32_bf16 v[68:71], v[212:215], v[124:127], v[68:71]\n\t"
      "buffer_load_dwordx4 %[vWsrc], s[44:47], s43 offen offset:1024 lds\n\t"
      "s_add_u32 s41, s41, 131072\n\t"
      "v_mfma_f32_16x16x32_bf16 v[64:67], v[208:211], v[124:127], v[64:67]\n\t"
      "s_waitcnt vmcnt(28) lgkmcnt(0)\n\t"
      "v_mfma_f32_16x16x32_bf16 v[16:19], v[224:227], v[128:131], v[16:19]\n\t"
      "ds_read_b64_tr_b16 v[208:209], v8 offset:16384\n\t"
      "buffer_load_dwordx4 v[80:83], %[vXsrc], %[srdWS], s40 offen offset:0 sc1\n\t"
      "v_mfma_f32_16x16x32_bf16 v[20:23], v[228:231], v[128:131], v[20:23]\n\t"
      "ds_read_b64_tr_b16 v[210:211], v8 offset:18432\n\t"
      "s_add_u32 s50, s40, %[sXo]\n\t"
      "buffer_load_dwordx4 v[84:87], %[vXsrc], %[srdWS], s40 offen offset:256 sc1\n\t"
      "v_mfma_f32_16x16x32_bf16 v[24:27], v[232:235], v[128:131], v[24:27]\n\t"
      "ds_read_b64_tr_b16 v[212:213], v9 offset:16384\n\t"
      "v_mfma_f32_16x16x32_bf16 v[28:31], v[236:239], v[128:131], v[28:31]\n\t"
      "ds_read_b64_tr_b16 v[214:215], v9 offset:18432\n\t"
      "buffer_load_dwordx4 v[88:91], %[vXsrc], %[srdWS], s50 offen offset:0 sc1\n\t"
      "v_mfma_f32_16x16x32_bf16 v[44:47], v[236:239], v[132:135], v[44:47]\n\t"
      "ds_read_b64_tr_b16 v[216:217], v10 offset:16384\n\t"
      "buffer_load_dwordx4 v[92:95], %[vXsrc], %[srdWS], s50 offen offset:256 sc1\n\t"
      "s_add_u32 s40, s40, 4096\n\t"
      "v_mfma_f32_16x16x32_bf16 v[40:43], v[232:235], v[132:135], v[40:43]\n\t"
      "ds_read_b64_tr_b16 v[218:219], v10 offset:18432\n\t"
      "v_mfma_f32_16x16x32_bf16 v[36:39], v[228:231], v[132:135], v[36:39]\n\t"
      "ds_read_b64_tr_b16 v[220:221], v11 offset:16384\n\t"
      "buffer_load_dwordx4 v[96:99], %[vXsrc], %[srdWS], s40 offen offset:0 sc1\n\t"
      "v_mfma_f32_16x16x32_bf16 v[32:35], v[224:227], v[132:135], v[32:35]\n\t"
      "ds_read_b64_tr_b16 v[222:223], v11 offset:18432\n\t"
      "v_mfma_f32_16x16x32_bf16 v[48:51], v[224:227], v[136:139], v[48:51]\n\t"
      "s_add_u32 s50, s40, %[sXo]\n\t"
      "buffer_load_dwordx4 v[100:103], %[vXsrc], %[srdWS], s40 offen offset:256 sc1\n\t"
      "v_mfma_f32_16x16x32_bf16 v[52:55], v[228:231], v[136:139], v[52:55]\n\t"
      "buffer_load_dwordx4 v[104:107], %[vXsrc], %[srdWS], s50 offen offset:0 sc1\n\t"
      "v_mfma_f32_16x16x32_bf16 v[56:59], v[232:235], v[136:139], v[56:59]\n\t"
      "v_mfma_f32_16x16x32_bf16 v[60:63], v[236:239], v[136:139], v[60:63]\n\t"
      "buffer_load_dwordx4 v[108:111], %[vXsrc], %[srdWS], s50 offen offset:256 sc1\n\t"
      "s_add_u32 s40, s40, 4096\n\t"
      "v_mfma_f32_16x16x32_bf16 v[76:79], v[236:239], v[140:143], v[76:79]\n\t"
      "s_add_u32 m0, s42, 98304\n\t"
      "s_add_u32 s43, s41, 15360\n\t"
      "buffer_load_dwordx4 %[vWsrc], s[44:47], s41 offen lds\n\t"
      "v_mfma_f32_16x16x32_bf16 v[72:75], v[232:235], v[140:143], v[72:75]\n\t"
      "v_mfma_f32_16x16x32_bf16 v[68:71], v[228:231], v[140:143], v[68:71]\n\t"
      "buffer_load_dwordx4 %[vWsrc], s[44:47], s43 offen offset:1024 lds\n\t"
      "s_add_u32 s41, s41, 131072\n\t"
      "v_mfma_f32_16x16x32_bf16 v[64:67], v[224:227], v[140:143], v[64:67]\n\t"
      "s_waitcnt vmcnt(28) lgkmcnt(0)\n\t"
      "v_mfma_f32_16x16x32_bf16 v[16:19], v[208:211], v[144:147], v[16:19]\n\t"
      "ds_read_b64_tr_b16 v[224:225], v8 offset:24576\n\t"
      "buffer_load_dwordx4 v[112:115], %[vXsrc], %[srdWS], s40 offen offset:0 sc1\n\t"
      "v_mfma_f32_16x16x32_bf16 v[20:23], v[212:215], v[144:147], v[20:23]\n\t"
      "ds_read_b64_tr_b16 v[226:227], v8 offset:26624\n\t"
      "s_add_u32 s50, s40, %[sXo]\n\t"
      "buffer_load_dwordx4 v[116:119], %[vXsrc], %[srdWS], s40 offen offset:256 sc1\n\t"
      "v_mfma_f32_16x16x32_bf16 v[24:27], v[216:219], v[144:147], v[24:27]\n\t"
      "ds_read_b64_tr_b16 v[228:229], v9 offset:24576\n\t"
      "v_mfma_f32_16x16x32_bf16 v[28:31], v[220:223], v[144:147], v[28:31]\n\t"
      "ds_read_b64_tr_b16 v[230:231], v9 offset:26624\n\t"
      "buffer_load_dwordx4 v[120:123], %[vXsrc], %[srdWS], s50 offen offset:0 sc1\n\t"
      "v_mfma_f32_16x16x32_bf16 v[44:47], v[220:223], v[148:151], v[44:47]\n\t"
      "ds_read_b64_tr_b16 v[232:233], v10 offset:24576\n\t"
      "buffer_load_dwordx4 v[124:127], %[vXsrc], %[srdWS], s50 offen offset:256 sc1\n\t"
      "s_add_u32 s40, s40, 4096\n\t"
      "v_mfma_f32_16x16x32_bf16 v[40:43], v[216:219], v[148:151], v[40:43]\n\t"
      "ds_read_b64_tr_b16 v[234:235], v10 offset:26624\n\t"
      "v_mfma_f32_16x16x32_bf16 v[36:39], v[212:215], v[148:151], v[36:39]\n\t"
      "ds_read_b64_tr_b16 v[236:237], v11 offset:24576\n\t"
      "buffer_load_dwordx4 v[128:131], %[vXsrc], %[srdWS], s40 offen offset:0 sc1\n\t"
      "v_mfma_f32_16x16x32_bf16 v[32:35], v[208:211], v[148:151], v[32:35]\n\t"
      "ds_read_b64_tr_b16 v[238:239], v11 offset:26624\n\t"
      "v_mfma_f32_16x16x32_bf16 v[48:51], v[208:211], v[152:155], v[48:51]\n\t"
      "s_add_u32 s50, s40, %[sXo]\n\t"
      "buffer_load_dwordx4 v[132:135], %[vXsrc], %[srdWS], s40 offen offset:256 sc1\n\t"
      "v_mfma_f32_16x16x32_bf16 v[52:55], v[212:215], v[152:155], v[52:55]\n\t"
      "buffer_load_dwordx4 v[136:139], %[vXsrc], %[srdWS], s50 offen offset:0 sc1\n\t"
      "v_mfma_f32_16x16x32_bf16 v[56:59], v[216:219], v[152:155], v[56:59]\n\t"
      "v_mfma_f32_16x16x32_bf16 v[60:63], v[220:223], v[152:155], v[60:63]\n\t"
      "buffer_load_dwordx4 v[140:143], %[vXsrc], %[srdWS], s50 offen offset:256 sc1\n\t"
      "s_add_u32 s40, s40, 4096\n\t"
      "v_mfma_f32_16x16x32_bf16 v[76:79], v[220:223], v[156:159], v[76:79]\n\t"
      "s_add_u32 m0, s42, 0\n\t"
      "s_add_u32 s43, s41, 15360\n\t"
      "buffer_load_dwordx4 %[vWsrc], s[44:47], s41 offen lds\n\t"
      "v_mfma_f32_16x16x32_bf16 v[72:75], v[216:219], v[156:159], v[72:75]\n\t"
      "v_mfma_f32_16x16x32_bf16 v[68:71], v[212:215], v[156:159], v[68:71]\n\t"
      "buffer_load_dwordx4 %[vWsrc], s[44:47], s43 offen offset:1024 lds\n\t"
      "s_add_u32 s41, s41, 131072\n\t"
      "v_mfma_f32_16x16x32_bf16 v[64:67], v[208:211], v[156:159], v[64:67]\n\t"
      "s_waitcnt vmcnt(34) lgkmcnt(0)\n\t"
      "v_mfma_f32_16x16x32_bf16 v[16:19], v[224:227], v[160:163], v[16:19]\n\t"
      "ds_read_b64_tr_b16 v[208:209], v8 offset:32768\n\t"
      "buffer_load_dwordx4 v[144:147], %[vXsrc], %[srdWS], s40 offen offset:0 sc1\n\t"
      "v_mfma_f32_16x16x32_bf16 v[20:23], v[228:231], v[160:163], v[20:23]\n\t"
      "ds_read_b64_tr_b16 v[210:211], v8 offset:34816\n\t"
      "v_mfma_f32_16x16x32_bf16 v[24:27], v[232:235], v[160:163], v[24:27]\n\t"
      "ds_read_b64_tr_b16 v[212:213], v9 offset:32768\n\t"
      "s_add_u32 s50, s40, %[sXo]\n\t"
      "buffer_load_dwordx4 v[148:151], %[vXsrc], %[srdWS], s40 offen offset:256 sc1\n\t"
      "v_mfma_f32_16x16x32_bf16 v[28:31], v[236:239], v[160:163], v[28:31]\n\t"
      "ds_read_b64_tr_b16 v[214:215], v9 offset:34816\n\t"
      "v_mfma_f32_16x16x32_bf16 v[44:47], v[236:239], v[164:167], v[44:47]\n\t"
      "ds_read_b64_tr_b16 v[216:217], v10 offset:32768\n\t"
      "buffer_load_dwordx4 v[152:155], %[vXsrc], %[srdWS], s50 offen offset:0 sc1\n\t"
      "v_mfma_f32_16x16x32_bf16 v[40:43], v[232:235], v[164:167], v[40:43]\n\t"
      "ds_read_b64_tr_b16 v[218:219], v10 offset:34816\n\t"
      "v_mfma_f32_16x16x32_bf16 v[36:39], v[228:231], v[164:167], v[36:39]\n\t"
      "ds_read_b64_tr_b16 v[220:221], v11 offset:32768\n\t"
      "buffer_load_dwordx4 v[156:159], %[vXsrc], %[srdWS], s50 offen offset:256 sc1\n\t"
      "s_add_u32 s40, s40, 4096\n\t"
      "v_mfma_f32_16x16x32_bf16 v[32:35], v[224:227], v[164:167], v[32:35]\n\t"
      "ds_read_b64_tr_b16 v[222:223], v11 offset:34816\n\t"
      "v_mfma_f32_16x16x32_bf16 v[48:51], v[224:227], v[168:171], v[48:51]\n\t"
      "s_add_u32 m0, s42, 8192\n\t"
      "s_add_u32 s43, s41, 15360\n\t"
      "buffer_load_dwordx4 %[vWsrc], s[44:47], s41 offen lds\n\t"
      "v_mfma_f32_16x16x32_bf16 v[52:55], v[228:231], v[168:171], v[52:55]\n\t"
      "v_mfma_f32_16x16x32_bf16 v[56:59], v[232:235], v[168:171], v[56:59]\n\t"
      "buffer_load_dwordx4 %[vWsrc], s[44:47], s43 offen offset:1024 lds\n\t"
      "s_add_u32 s41, s41, 131072\n\t"
      "v_mfma_f32_16x16x32_bf16 v[60:63], v[236:239], v[168:171], v[60:63]\n\t"
      "v_mfma_f32_16x16x32_bf16 v[76:79], v[236:239], v[172:175], v[76:79]\n\t"
      "v_mfma_f32_16x16x32_bf16 v[72:75], v[232:235], v[172:175], v[72:75]\n\t"
      "v_mfma_f32_16x16x32_bf16 v[68:71], v[228:231], v[172:175], v[68:71]\n\t"
      "v_mfma_f32_16x16x32_bf16 v[64:67], v[224:227], v[172:175], v[64:67]\n\t"
      "s_waitcnt vmcnt(40) lgkmcnt(0)\n\t"
      "s_barrier\n\t"
      "s_waitcnt vmcnt(34) lgkmcnt(0)\n\t"
      "v_mfma_f32_16x16x32_bf16 v[16:19], v[208:211], v[176:179], v[16:19]\n\t"
      "ds_read_b64_tr_b16 v[224:225], v8 offset:40960\n\t"
      "buffer_load_dwordx4 v[160:163], %[vXsrc], %[srdWS], s40 offen offset:0 sc1\n\t"
      "v_mfma_f32_16x16x32_bf16 v[20:23], v[212:215], v[176:179], v[20:23]\n\t"
      "ds_read_b64_tr_b16 v[226:227], v8 offset:43008\n\t"
      "v_mfma_f32_16x16x32_bf16 v[24:27], v[216:219], v[176:179], v[24:27]\n\t"
      "ds_read_b64_tr_b16 v[228:229], v9 offset:40960\n\t"
      "s_add_u32 s50, s40, %[sXo]\n\t"
      "buffer_load_dwordx4 v[164:167], %[vXsrc], %[srdWS], s40 offen offset:256 sc1\n\t"
      "v_mfma_f32_16x16x32_bf16 v[28:31], v[220:223], v[176:179], v[28:31]\n\t"
      "ds_read_b64_tr_b16 v[230:231], v9 offset:43008\n\t"
      "v_mfma_f32_16x16x32_bf16 v[44:47], v[220:223], v[180:183], v[44:47]\n\t"
      "ds_read_b64_tr_b16 v[232:233], v10 offset:40960\n\t"
      "buffer_load_dwordx4 v[168:171], %[vXsrc], %[srdWS], s50 offen offset:0 sc1\n\t"
      "v_mfma_f32_16x16x32_bf16 v[40:43], v[216:219], v[180:183], v[40:43]\n\t"
      "ds_read_b64_tr_b16 v[234:235], v10 offset:43008\n\t"
      "v_mfma_f32_16x16x32_bf16 v[36:39], v[212:215], v[180:183], v[36:39]\n\t"
      "ds_read_b64_tr_b16 v[236:237], v11 offset:40960\n\t"
      "buffer_load_dwordx4 v[172:175], %[vXsrc], %[srdWS], s50 offen offset:256 sc1\n\t"
      "s_add_u32 s40, s40, 4096\n\t"
      "v_mfma_f32_16x16x32_bf16 v[32:35], v[208:211], v[180:183], v[32:35]\n\t"
      "ds_read_b64_tr_b16 v[238:239], v11 offset:43008\n\t"
      "v_mfma_f32_16x16x32_bf16 v[48:51], v[208:211], v[184:187], v[48:51]\n\t"
      "s_add_u32 m0, s42, 106496\n\t"
      "s_add_u32 s43, s41, 15360\n\t"
      "buffer_load_dwordx4 %[vWsrc], s[44:47], s41 offen lds\n\t"
      "v_mfma_f32_16x16x32_bf16 v[52:55], v[212:215], v[184:187], v[52:55]\n\t"
      "v_mfma_f32_16x16x32_bf16 v[56:59], v[216:219], v[184:187], v[56:59]\n\t"
      "buffer_load_dwordx4 %[vWsrc], s[44:47], s43 offen offset:1024 lds\n\t"
      "s_add_u32 s41, s41, 131072\n\t"
      "v_mfma_f32_16x16x32_bf16 v[60:63], v[220:223], v[184:187], v[60:63]\n\t"
      "v_mfma_f32_16x16x32_bf16 v[76:79], v[220:223], v[188:191], v[76:79]\n\t"
      "s_mov_b32 s44, %[wolo]\n\t"
      "s_mov_b32 s45, %[wohi]\n\t"
      "s_mov_b32 s46, 2097152\n\t"
      "s_mov_b32 s41, %[sW0]\n\t"
      "s_add_u32 m0, s42, 114688\n\t"
      "s_add_u32 s43, s41, 15360\n\t"
      "buffer_load_dwordx4 %[vWsrc], s[44:47], s41 offen lds\n\t"
      "v_mfma_f32_16x16x32_bf16 v[72:75], v[216:219], v[188:191], v[72:75]\n\t"
      "v_mfma_f32_16x16x32_bf16 v[68:71], v[212:215], v[188:191], v[68:71]\n\t"
      "buffer_load_dwordx4 %[vWsrc], s[44:47], s43 offen offset:1024 lds\n\t"
      "s_add_u32 s41, s41, 131072\n\t"
      "v_mfma_f32_16x16x32_bf16 v[64:67], v[208:211], v[188:191], v[64:67]\n\t"
      "s_waitcnt vmcnt(38) lgkmcnt(0)\n\t"
      "v_mfma_f32_16x16x32_bf16 v[16:19], v[224:227], v[192:195], v[16:19]\n\t"
      "ds_read_b64_tr_b16 v[208:209], v8 offset:49152\n\t"
      "buffer_load_dwordx4 v[176:179], %[vXsrc], %[srdWS], s40 offen offset:0 sc1\n\t"
      "v_mfma_f32_16x16x32_bf16 v[20:23], v[228:231], v[192:195], v[20:23]\n\t"
      "ds_read_b64_tr_b16 v[210:211], v8 offset:51200\n\t"
      "v_mfma_f32_16x16x32_bf16 v[24:27], v[232:235], v[192:195], v[24:27]\n\t"
      "ds_read_b64_tr_b16 v[212:213], v9 offset:49152\n\t"
      "s_add_u32 s50, s40, %[sXo]\n\t"
      "buffer_load_dwordx4 v[180:183], %[vXsrc], %[srdWS], s40 offen offset:256 sc1\n\t"
      "v_mfma_f32_16x16x32_bf16 v[28:31], v[236:239], v[192:195], v[28:31]\n\t"
      "ds_read_b64_tr_b16 v[214:215], v9 offset:51200\n\t"
      "v_mfma_f32_16x16x32_bf16 v[44:47], v[236:239], v[196:199], v[44:47]\n\t"
      "ds_read_b64_tr_b16 v[216:217], v10 offset:49152\n\t"
      "buffer_load_dwordx4 v[184:187], %[vXsrc], %[srdWS], s50 offen offset:0 sc1\n\t"
      "v_mfma_f32_16x16x32_bf16 v[40:43], v[232:235], v[196:199], v[40:43]\n\t"
      "ds_read_b64_tr_b16 v[218:219], v10 offset:51200\n\t"
      "v_mfma_f32_16x16x32_bf16 v[36:39], v[228:231], v[196:199], v[36:39]\n\t"
      "ds_read_b64_tr_b16 v[220:221], v11 offset:49152\n\t"
      "buffer_load_dwordx4 v[188:191], %[vXsrc], %[srdWS], s50 offen offset:256 sc1\n\t"
      "s_add_u32 s40, s40, 4096\n\t"
      "v_mfma_f32_16x16x32_bf16 v[32:35], v[224:227], v[196:199], v[32:35]\n\t"
      "ds_read_b64_tr_b16 v[222:223], v11 offset:51200\n\t"
      "v_mfma_f32_16x16x32_bf16 v[48:51], v[224:227], v[200:203], v[48:51]\n\t"
      "s_add_u32 m0, s42, 122880\n\t"
      "s_add_u32 s43, s41, 15360\n\t"
      "buffer_load_dwordx4 %[vWsrc], s[44:47], s41 offen lds\n\t"
      "v_mfma_f32_16x16x32_bf16 v[52:55], v[228:231], v[200:203], v[52:55]\n\t"
      "v_mfma_f32_16x16x32_bf16 v[56:59], v[232:235], v[200:203], v[56:59]\n\t"
      "buffer_load_dwordx4 %[vWsrc], s[44:47], s43 offen offset:1024 lds\n\t"
      "s_add_u32 s41, s41, 131072\n\t"
      "v_mfma_f32_16x16x32_bf16 v[60:63], v[236:239], v[200:203], v[60:63]\n\t"
      "v_mfma_f32_16x16x32_bf16 v[76:79], v[236:239], v[204:207], v[76:79]\n\t"
      "s_add_u32 m0, s42, 131072\n\t"
      "s_add_u32 s43, s41, 15360\n\t"
      "buffer_load_dwordx4 %[vWsrc], s[44:47], s41 offen lds\n\t"
      "v_mfma_f32_16x16x32_bf16 v[72:75], v[232:235], v[204:207], v[72:75]\n\t"
      "v_mfma_f32_16x16x32_bf16 v[68:71], v[228:231], v[204:207], v[68:71]\n\t"
      "buffer_load_dwordx4 %[vWsrc], s[44:47], s43 offen offset:1024 lds\n\t"
      "s_add_u32 s41, s41, 131072\n\t"
      "v_mfma_f32_16x16x32_bf16 v[64:67], v[224:227], v[204:207], v[64:67]\n\t"
      "s_waitcnt vmcnt(38) lgkmcnt(0)\n\t"
      "v_mfma_f32_16x16x32_bf16 v[16:19], v[208:211], v[80:83], v[16:19]\n\t"
      "ds_read_b64_tr_b16 v[224:225], v8 offset:57344\n\t"
      "buffer_load_dwordx4 v[192:195], %[vXsrc], %[srdWS], s40 offen offset:0 sc1\n\t"
      "v_mfma_f32_16x16x32_bf16 v[20:23], v[212:215], v[80:83], v[20:23]\n\t"
      "ds_read_b64_tr_b16 v[226:227], v8 offset:59392\n\t"
      "v_mfma_f32_16x16x32_bf16 v[24:27], v[216:219], v[80:83], v[24:27]\n\t"
      "ds_read_b64_tr_b16 v[228:229], v9 offset:57344\n\t"
      "s_add_u32 s50, s40, %[sXo]\n\t"
      "buffer_load_dwordx4 v[196:199], %[vXsrc], %[srdWS], s40 offen offset:256 sc1\n\t"
      "v_mfma_f32_16x16x32_bf16 v[28:31], v[220:223], v[80:83], v[28:31]\n\t"
      "ds_read_b64_tr_b16 v[230:231], v9 offset:59392\n\t"
      "v_mfma_f32_16x16x32_bf16 v[44:47], v[220:223], v[84:87], v[44:47]\n\t"
      "ds_read_b64_tr_b16 v[232:233], v10 offset:57344\n\t"
      "buffer_load_dwordx4 v[200:203], %[vXsrc], %[srdWS], s50 offen offset:0 sc1\n\t"
      "v_mfma_f32_16x16x32_bf16 v[40:43], v[216:219], v[84:87], v[40:43]\n\t"
      "ds_read_b64_tr_b16 v[234:235], v10 offset:59392\n\t"
      "v_mfma_f32_16x16x32_bf16 v[36:39], v[212:215], v[84:87], v[36:39]\n\t"
      "ds_read_b64_tr_b16 v[236:237], v11 offset:57344\n\t"
      "buffer_load_dwordx4 v[204:207], %[vXsrc], %[srdWS], s50 offen offset:256 sc1\n\t"
      "s_add_u32 s40, s40, 4096\n\t"
      "v_mfma_f32_16x16x32_bf16 v[32:35], v[208:211], v[84:87], v[32:35]\n\t"
      "ds_read_b64_tr_b16 v[238:239], v11 offset:59392\n\t"
      "v_mfma_f32_16x16x32_bf16 v[48:51], v[208:211], v[88:91], v[48:51]\n\t"
      "s_add_u32 m0, s42, 16384\n\t"
      "s_add_u32 s43, s41, 15360\n\t"
      "buffer_load_dwordx4 %[vWsrc], s[44:47], s41 offen lds\n\t"
      "v_mfma_f32_16x16x32_bf16 v[52:55], v[212:215], v[88:91], v[52:55]\n\t"
      "v_mfma_f32_16x16x32_bf16 v[56:59], v[216:219], v[88:91], v[56:59]\n\t"
      "buffer_load_dwordx4 %[vWsrc], s[44:47], s43 offen offset:1024 lds\n\t"
      "s_add_u32 s41, s41, 131072\n\t"
      "v_mfma_f32_16x16x32_bf16 v[60:63], v[220:223], v[88:91], v[60:63]\n\t"
      "v_mfma_f32_16x16x32_bf16 v[76:79], v[220:223], v[92:95], v[76:79]\n\t"
      "s_add_u32 m0, s42, 24576\n\t"
      "s_add_u32 s43, s41, 15360\n\t"
      "buffer_load_dwordx4 %[vWsrc], s[44:47], s41 offen lds\n\t"
      "v_mfma_f32_16x16x32_bf16 v[72:75], v[216:219], v[92:95], v[72:75]\n\t"
      "v_mfma_f32_16x16x32_bf16 v[68:71], v[212:215], v[92:95], v[68:71]\n\t"
      "buffer_load_dwordx4 %[vWsrc], s[44:47], s43 offen offset:1024 lds\n\t"
      "s_add_u32 s41, s41, 131072\n\t"
      "v_mfma_f32_16x16x32_bf16 v[64:67], v[208:211], v[92:95], v[64:67]\n\t"
      "s_waitcnt vmcnt(10) lgkmcnt(0)\n\t"
      "s_barrier\n\t"
      "s_waitcnt vmcnt(10) lgkmcnt(0)\n\t"
      "v_mfma_f32_16x16x32_bf16 v[16:19], v[224:227], v[96:99], v[16:19]\n\t"
      "v_add_u32 v8, 4294901760, v8\n\t"
      "v_add_u32 v9, 4294901760, v9\n\t"
      "v_add_u32 v10, 4294901760, v10\n\t"
      "v_add_u32 v11, 4294901760, v11\n\t"
      "ds_read_b64_tr_b16 v[208:209], v8 offset:0\n\t"
      "buffer_load_dwordx4 v[80:83], %[vXsrc], %[srdWS], s40 offen offset:0 sc1\n\t"
      "v_mfma_f32_16x16x32_bf16 v[20:23], v[228:231], v[96:99], v[20:23]\n\t"
      "ds_read_b64_tr_b16 v[210:211], v8 offset:2048\n\t"
      "s_add_u32 s50, s40, %[sXo]\n\t"
      "buffer_load_dwordx4 v[84:87], %[vXsrc], %[srdWS], s40 offen offset:256 sc1\n\t"
      "v_mfma_f32_16x16x32_bf16 v[24:27], v[232:235], v[96:99], v[24:27]\n\t"
      "ds_read_b64_tr_b16 v[212:213], v9 offset:0\n\t"
      "v_mfma_f32_16x16x32_bf16 v[28:31], v[236:239], v[96:99], v[28:31]\n\t"
      "ds_read_b64_tr_b16 v[214:215], v9 offset:2048\n\t"
      "buffer_load_dwordx4 v[88:91], %[vXsrc], %[srdWS], s50 offen offset:0 sc1\n\t"
      "v_mfma_f32_16x16x32_bf16 v[44:47], v[236:239], v[100:103], v[44:47]\n\t"
      "ds_read_b64_tr_b16 v[216:217], v10 offset:0\n\t"
      "buffer_load_dwordx4 v[92:95], %[vXsrc], %[srdWS], s50 offen offset:256 sc1\n\t"
      "s_add_u32 s40, s40, 4096\n\t"
      "v_mfma_f32_16x16x32_bf16 v[40:43], v[232:235], v[100:103], v[40:43]\n\t"
      "ds_read_b64_tr_b16 v[218:219], v10 offset:2048\n\t"
      "v_mfma_f32_16x16x32_bf16 v[36:39], v[228:231], v[100:103], v[36:39]\n\t"
      "ds_read_b64_tr_b16 v[220:221], v11 offset:0\n\t"
      "s_add_u32 m0, s42, 139264\n\t"
      "s_add_u32 s43, s41, 15360\n\t"
      "buffer_load_dwordx4 %[vWsrc], s[44:47], s41 offen lds\n\t"
      "v_mfma_f32_16x16x32_bf16 v[32:35], v[224:227], v[100:103], v[32:35]\n\t"
      "ds_read_b64_tr_b16 v[222:223], v11 offset:2048\n\t"
      "v_mfma_f32_16x16x32_bf16 v[48:51], v[224:227], v[104:107], v[48:51]\n\t"
      "buffer_load_dwordx4 %[vWsrc], s[44:47], s43 offen offset:1024 lds\n\t"
      "s_add_u32 s41, s41, 131072\n\t"
      "v_mfma_f32_16x16x32_bf16 v[52:55], v[228:231], v[104:107], v[52:55]\n\t"
      "s_add_u32 m0, s42, 147456\n\t"
      "s_add_u32 s43, s41, 15360\n\t"
      "buffer_load_dwordx4 %[vWsrc], s[44:47], s41 offen lds\n\t"
      "v_mfma_f32_16x16x32_bf16 v[56:59], v[232:235], v[104:107], v[56:59]\n\t"
      "v_mfma_f32_16x16x32_bf16 v[60:63], v[236:239], v[104:107], v[60:63]\n\t"
      "buffer_load_dwordx4 %[vWsrc], s[44:47], s43 offen offset:1024 lds\n\t"
      "s_add_u32 s41, s41, 131072\n\t"
      "v_mfma_f32_16x16x32_bf16 v[76:79], v[236:239], v[108:111], v[76:79]\n\t"
      "s_add_u32 m0, s42, 155648\n\t"
      "s_add_u32 s43, s41, 15360\n\t"
      "buffer_load_dwordx4 %[vWsrc], s[44:47], s41 offen lds\n\t"
      "v_mfma_f32_16x16x32_bf16 v[72:75], v[232:235], v[108:111], v[72:75]\n\t"
      "v_mfma_f32_16x16x32_bf16 v[68:71], v[228:231], v[108:111], v[68:71]\n\t"
      "buffer_load_dwordx4 %[vWsrc], s[44:47], s43 offen offset:1024 lds\n\t"
      "s_add_u32 s41, s41, 131072\n\t"
      "v_mfma_f32_16x16x32_bf16 v[64:67], v[224:227], v[108:111], v[64:67]\n\t"
      "s_waitcnt vmcnt(20) lgkmcnt(0)\n\t"
      "v_mfma_f32_16x16x32_bf16 v[16:19], v[208:211], v[112:115], v[16:19]\n\t"
      "ds_read_b64_tr_b16 v[224:225], v8 offset:8192\n\t"
      "buffer_load_dwordx4 v[96:99], %[vXsrc], %[srdWS], s40 offen offset:0 sc1\n\t"
      "v_mfma_f32_16x16x32_bf16 v[20:23], v[212:215], v[112:115], v[20:23]\n\t"
      "ds_read_b64_tr_b16 v[226:227], v8 offset:10240\n\t"
      "v_mfma_f32_16x16x32_bf16 v[24:27], v[216:219], v[112:115], v[24:27]\n\t"
      "ds_read_b64_tr_b16 v[228:229], v9 offset:8192\n\t"
      "s_add_u32 s50, s40, %[sXo]\n\t"
      "buffer_load_dwordx4 v[100:103], %[vXsrc], %[srdWS], s40 offen offset:256 sc1\n\t"
      "v_mfma_f32_16x16x32_bf16 v[28:31], v[220:223], v[112:115], v[28:31]\n\t"
      "ds_read_b64_tr_b16 v[230:231], v9 offset:10240\n\t"
      "v_mfma_f32_16x16x32_bf16 v[44:47], v[220:223], v[116:119], v[44:47]\n\t"
      "ds_read_b64_tr_b16 v[232:233], v10 offset:8192\n\t"
      "buffer_load_dwordx4 v[104:107], %[vXsrc], %[srdWS], s50 offen offset:0 sc1\n\t"
      "v_mfma_f32_16x16x32_bf16 v[40:43], v[216:219], v[116:119], v[40:43]\n\t"
      "ds_read_b64_tr_b16 v[234:235], v10 offset:10240\n\t"
      "v_mfma_f32_16x16x32_bf16 v[36:39], v[212:215], v[116:119], v[36:39]\n\t"
      "ds_read_b64_tr_b16 v[236:237], v11 offset:8192\n\t"
      "buffer_load_dwordx4 v[108:111], %[vXsrc], %[srdWS], s50 offen offset:256 sc1\n\t"
      "s_add_u32 s40, s40, 4096\n\t"
      "v_mfma_f32_16x16x32_bf16 v[32:35], v[208:211], v[116:119], v[32:35]\n\t"
      "ds_read_b64_tr_b16 v[238:239], v11 offset:10240\n\t"
      "v_mfma_f32_16x16x32_bf16 v[48:51], v[208:211], v[120:123], v[48:51]\n\t"
      "s_add_u32 m0, s42, 65536\n\t"
      "s_add_u32 s43, s41, 15360\n\t"
      "buffer_load_dwordx4 %[vWsrc], s[44:47], s41 offen lds\n\t"
      "v_mfma_f32_16x16x32_bf16 v[52:55], v[212:215], v[120:123], v[52:55]\n\t"
      "v_mfma_f32_16x16x32_bf16 v[56:59], v[216:219], v[120:123], v[56:59]\n\t"
      "buffer_load_dwordx4 %[vWsrc], s[44:47], s43 offen offset:1024 lds\n\t"
      "s_add_u32 s41, s41, 131072\n\t"
      "v_mfma_f32_16x16x32_bf16 v[60:63], v[220:223], v[120:123], v[60:63]\n\t"
      "v_mfma_f32_16x16x32_bf16 v[76:79], v[220:223], v[124:127], v[76:79]\n\t"
      "s_add_u32 m0, s42, 73728\n\t"
      "s_add_u32 s43, s41, 15360\n\t"
      "buffer_load_dwordx4 %[vWsrc], s[44:47], s41 offen lds\n\t"
      "v_mfma_f32_16x16x32_bf16 v[72:75], v[216:219], v[124:127], v[72:75]\n\t"
      "v_mfma_f32_16x16x32_bf16 v[68:71], v[212:215], v[124:127], v[68:71]\n\t"
      "buffer_load_dwordx4 %[vWsrc], s[44:47], s43 offen offset:1024 lds\n\t"
      "s_add_u32 s41, s41, 131072\n\t"
      "v_mfma_f32_16x16x32_bf16 v[64:67], v[208:211], v[124:127], v[64:67]\n\t"
      "s_waitcnt vmcnt(28) lgkmcnt(0)\n\t"
      "v_mfma_f32_16x16x32_bf16 v[16:19], v[224:227], v[128:131], v[16:19]\n\t"
      "ds_read_b64_tr_b16 v[208:209], v8 offset:16384\n\t"
      "v_mfma_f32_16x16x32_bf16 v[20:23], v[228:231], v[128:131], v[20:23]\n\t"
      "ds_read_b64_tr_b16 v[210:211], v8 offset:18432\n\t"
      "v_mfma_f32_16x16x32_bf16 v[24:27], v[232:235], v[128:131], v[24:27]\n\t"
      "ds_read_b64_tr_b16 v[212:213], v9 offset:16384\n\t"
      "v_mfma_f32_16x16x32_bf16 v[28:31], v[236:239], v[128:131], v[28:31]\n\t"
      "ds_read_b64_tr_b16 v[214:215], v9 offset:18432\n\t"
      "v_mfma_f32_16x16x32_bf16 v[44:47], v[236:239], v[132:135], v[44:47]\n\t"
      "ds_read_b64_tr_b16 v[216:217], v10 offset:16384\n\t"
      "v_mfma_f32_16x16x32_bf16 v[40:43], v[232:235], v[132:135], v[40:43]\n\t"
      "ds_read_b64_tr_b16 v[218:219], v10 offset:18432\n\t"
      "v_mfma_f32_16x16x32_bf16 v[36:39], v[228:231], v[132:135], v[36:39]\n\t"
      "ds_read_b64_tr_b16 v[220:221], v11 offset:16384\n\t"
      "v_mfma_f32_16x16x32_bf16 v[32:35], v[224:227], v[132:135], v[32:35]\n\t"
      "ds_read_b64_tr_b16 v[222:223], v11 offset:18432\n\t"
      "v_mfma_f32_16x16x32_bf16 v[48:51], v[224:227], v[136:139], v[48:51]\n\t"
      "v_mfma_f32_16x16x32_bf16 v[52:55], v[228:231], v[136:139], v[52:55]\n\t"
      "v_mfma_f32_16x16x32_bf16 v[56:59], v[232:235], v[136:139], v[56:59]\n\t"
      "v_mfma_f32_16x16x32_bf16 v[60:63], v[236:239], v[136:139], v[60:63]\n\t"
      "v_mfma_f32_16x16x32_bf16 v[76:79], v[236:239], v[140:143], v[76:79]\n\t"
      "v_mfma_f32_16x16x32_bf16 v[72:75], v[232:235], v[140:143], v[72:75]\n\t"
      "v_mfma_f32_16x16x32_bf16 v[68:71], v[228:231], v[140:143], v[68:71]\n\t"
      "v_mfma_f32_16x16x32_bf16 v[64:67], v[224:227], v[140:143], v[64:67]\n\t"
      "s_waitcnt vmcnt(28) lgkmcnt(0)\n\t"
      "v_mfma_f32_16x16x32_bf16 v[16:19], v[208:211], v[144:147], v[16:19]\n\t"
      "ds_read_b64_tr_b16 v[224:225], v8 offset:24576\n\t"
      "v_mfma_f32_16x16x32_bf16 v[20:23], v[212:215], v[144:147], v[20:23]\n\t"
      "ds_read_b64_tr_b16 v[226:227], v8 offset:26624\n\t"
      "v_mfma_f32_16x16x32_bf16 v[24:27], v[216:219], v[144:147], v[24:27]\n\t"
      "ds_read_b64_tr_b16 v[228:229], v9 offset:24576\n\t"
      "v_mfma_f32_16x16x32_bf16 v[28:31], v[220:223], v[144:147], v[28:31]\n\t"
      "ds_read_b64_tr_b16 v[230:231], v9 offset:26624\n\t"
      "v_mfma_f32_16x16x32_bf16 v[44:47], v[220:223], v[148:151], v[44:47]\n\t"
      "ds_read_b64_tr_b16 v[232:233], v10 offset:24576\n\t"
      "v_mfma_f32_16x16x32_bf16 v[40:43], v[216:219], v[148:151], v[40:43]\n\t"
      "ds_read_b64_tr_b16 v[234:235], v10 offset:26624\n\t"
      "v_mfma_f32_16x16x32_bf16 v[36:39], v[212:215], v[148:151], v[36:39]\n\t"
      "ds_read_b64_tr_b16 v[236:237], v11 offset:24576\n\t"
      "v_mfma_f32_16x16x32_bf16 v[32:35], v[208:211], v[148:151], v[32:35]\n\t"
      "ds_read_b64_tr_b16 v[238:239], v11 offset:26624\n\t"
      "v_mfma_f32_16x16x32_bf16 v[48:51], v[208:211], v[152:155], v[48:51]\n\t"
      "v_mfma_f32_16x16x32_bf16 v[52:55], v[212:215], v[152:155], v[52:55]\n\t"
      "v_mfma_f32_16x16x32_bf16 v[56:59], v[216:219], v[152:155], v[56:59]\n\t"
      "v_mfma_f32_16x16x32_bf16 v[60:63], v[220:223], v[152:155], v[60:63]\n\t"
      "v_mfma_f32_16x16x32_bf16 v[76:79], v[220:223], v[156:159], v[76:79]\n\t"
      "v_mfma_f32_16x16x32_bf16 v[72:75], v[216:219], v[156:159], v[72:75]\n\t"
      "v_mfma_f32_16x16x32_bf16 v[68:71], v[212:215], v[156:159], v[68:71]\n\t"
      "v_mfma_f32_16x16x32_bf16 v[64:67], v[208:211], v[156:159], v[64:67]\n\t"
      "s_waitcnt vmcnt(28) lgkmcnt(0)\n\t"
      "v_mfma_f32_16x16x32_bf16 v[16:19], v[224:227], v[160:163], v[16:19]\n\t"
      "v_add_u32 v8, 65536, v8\n\t"
      "v_add_u32 v9, 65536, v9\n\t"
      "v_add_u32 v10, 65536, v10\n\t"
      "v_add_u32 v11, 65536, v11\n\t"
      "ds_read_b64_tr_b16 v[208:209], v8 offset:0\n\t"
      "v_mfma_f32_16x16x32_bf16 v[20:23], v[228:231], v[160:163], v[20:23]\n\t"
      "ds_read_b64_tr_b16 v[210:211], v8 offset:2048\n\t"
      "v_mfma_f32_16x16x32_bf16 v[24:27], v[232:235], v[160:163], v[24:27]\n\t"
      "ds_read_b64_tr_b16 v[212:213], v9 offset:0\n\t"
      "v_mfma_f32_16x16x32_bf16 v[28:31], v[236:239], v[160:163], v[28:31]\n\t"
      "ds_read_b64_tr_b16 v[214:215], v9 offset:2048\n\t"
      "v_mfma_f32_16x16x32_bf16 v[44:47], v[236:239], v[164:167], v[44:47]\n\t"
      "ds_read_b64_tr_b16 v[216:217], v10 offset:0\n\t"
      "v_mfma_f32_16x16x32_bf16 v[40:43], v[232:235], v[164:167], v[40:43]\n\t"
      "ds_read_b64_tr_b16 v[218:219], v10 offset:2048\n\t"
      "v_mfma_f32_16x16x32_bf16 v[36:39], v[228:231], v[164:167], v[36:39]\n\t"
      "ds_read_b64_tr_b16 v[220:221], v11 offset:0\n\t"
      "v_mfma_f32_16x16x32_bf16 v[32:35], v[224:227], v[164:167], v[32:35]\n\t"
      "ds_read_b64_tr_b16 v[222:223], v11 offset:2048\n\t"
      "v_mfma_f32_16x16x32_bf16 v[48:51], v[224:227], v[168:171], v[48:51]\n\t"
      "v_mfma_f32_16x16x32_bf16 v[52:55], v[228:231], v[168:171], v[52:55]\n\t"
      "v_mfma_f32_16x16x32_bf16 v[56:59], v[232:235], v[168:171], v[56:59]\n\t"
      "v_mfma_f32_16x16x32_bf16 v[60:63], v[236:239], v[168:171], v[60:63]\n\t"
      "v_mfma_f32_16x16x32_bf16 v[76:79], v[236:239], v[172:175], v[76:79]\n\t"
      "v_mfma_f32_16x16x32_bf16 v[72:75], v[232:235], v[172:175], v[72:75]\n\t"
      "v_mfma_f32_16x16x32_bf16 v[68:71], v[228:231], v[172:175], v[68:71]\n\t"
      "v_mfma_f32_16x16x32_bf16 v[64:67], v[224:227], v[172:175], v[64:67]\n\t"
      "s_waitcnt vmcnt(28) lgkmcnt(0)\n\t"
      "v_mfma_f32_16x16x32_bf16 v[16:19], v[208:211], v[176:179], v[16:19]\n\t"
      "v_add_u32 v8, 4294868992, v8\n\t"
      "v_add_u32 v9, 4294868992, v9\n\t"
      "v_add_u32 v10, 4294868992, v10\n\t"
      "v_add_u32 v11, 4294868992, v11\n\t"
      "ds_read_b64_tr_b16 v[224:225], v8 offset:0\n\t"
      "buffer_load_dwordx2 v[12:13], %[vPl], %[srdWS], %[sPO] offen sc1\n\t"
      "v_mfma_f32_16x16x32_bf16 v[20:23], v[212:215], v[176:179], v[20:23]\n\t"
      "ds_read_b64_tr_b16 v[226:227], v8 offset:2048\n\t"
      "v_mfma_f32_16x16x32_bf16 v[24:27], v[216:219], v[176:179], v[24:27]\n\t"
      "ds_read_b64_tr_b16 v[228:229], v9 offset:0\n\t"
      "v_mfma_f32_16x16x32_bf16 v[28:31], v[220:223], v[176:179], v[28:31]\n\t"
      "ds_read_b64_tr_b16 v[230:231], v9 offset:2048\n\t"
      "v_mfma_f32_16x16x32_bf16 v[44:47], v[220:223], v[180:183], v[44:47]\n\t"
      "ds_read_b64_tr_b16 v[232:233], v10 offset:0\n\t"
      "v_mfma_f32_16x16x32_bf16 v[40:43], v[216:219], v[180:183], v[40:43]\n\t"
      "ds_read_b64_tr_b16 v[234:235], v10 offset:2048\n\t"
      "v_mfma_f32_16x16x32_bf16 v[36:39], v[212:215], v[180:183], v[36:39]\n\t"
      "ds_read_b64_tr_b16 v[236:237], v11 offset:0\n\t"
      "v_mfma_f32_16x16x32_bf16 v[32:35], v[208:211], v[180:183], v[32:35]\n\t"
      "ds_read_b64_tr_b16 v[238:239], v11 offset:2048\n\t"
      "v_mfma_f32_16x16x32_bf16 v[48:51], v[208:211], v[184:187], v[48:51]\n\t"
      "v_mfma_f32_16x16x32_bf16 v[52:55], v[212:215], v[184:187], v[52:55]\n\t"
      "v_mfma_f32_16x16x32_bf16 v[56:59], v[216:219], v[184:187], v[56:59]\n\t"
      "v_mfma_f32_16x16x32_bf16 v[60:63], v[220:223], v[184:187], v[60:63]\n\t"
      "v_mfma_f32_16x16x32_bf16 v[76:79], v[220:223], v[188:191], v[76:79]\n\t"
      "v_mfma_f32_16x16x32_bf16 v[72:75], v[216:219], v[188:191], v[72:75]\n\t"
      "v_mfma_f32_16x16x32_bf16 v[68:71], v[212:215], v[188:191], v[68:71]\n\t"
      "v_mfma_f32_16x16x32_bf16 v[64:67], v[208:211], v[188:191], v[64:67]\n\t"
      "s_waitcnt vmcnt(23) lgkmcnt(0)\n\t"
      "v_mfma_f32_16x16x32_bf16 v[16:19], v[224:227], v[192:195], v[16:19]\n\t"
      "ds_read_b64_tr_b16 v[208:209], v8 offset:8192\n\t"
      "v_mfma_f32_16x16x32_bf16 v[20:23], v[228:231], v[192:195], v[20:23]\n\t"
      "ds_read_b64_tr_b16 v[210:211], v8 offset:10240\n\t"
      "v_mfma_f32_16x16x32_bf16 v[24:27], v[232:235], v[192:195], v[24:27]\n\t"
      "ds_read_b64_tr_b16 v[212:213], v9 offset:8192\n\t"
      "v_mfma_f32_16x16x32_bf16 v[28:31], v[236:239], v[192:195], v[28:31]\n\t"
      "ds_read_b64_tr_b16 v[214:215], v9 offset:10240\n\t"
      "v_mfma_f32_16x16x32_bf16 v[44:47], v[236:239], v[196:199], v[44:47]\n\t"
      "ds_read_b64_tr_b16 v[216:217], v10 offset:8192\n\t"
      "v_mfma_f32_16x16x32_bf16 v[40:43], v[232:235], v[196:199], v[40:43]\n\t"
      "ds_read_b64_tr_b16 v[218:219], v10 offset:10240\n\t"
      "v_mfma_f32_16x16x32_bf16 v[36:39], v[228:231], v[196:199], v[36:39]\n\t"
      "ds_read_b64_tr_b16 v[220:221], v11 offset:8192\n\t"
      "v_mfma_f32_16x16x32_bf16 v[32:35], v[224:227], v[196:199], v[32:35]\n\t"
      "ds_read_b64_tr_b16 v[222:223], v11 offset:10240\n\t"
      "v_mfma_f32_16x16x32_bf16 v[48:51], v[224:227], v[200:203], v[48:51]\n\t"
      "v_mfma_f32_16x16x32_bf16 v[52:55], v[228:231], v[200:203], v[52:55]\n\t"
      "v_mfma_f32_16x16x32_bf16 v[56:59], v[232:235], v[200:203], v[56:59]\n\t"
      "v_mfma_f32_16x16x32_bf16 v[60:63], v[236:239], v[200:203], v[60:63]\n\t"
      "v_mfma_f32_16x16x32_bf16 v[76:79], v[236:239], v[204:207], v[76:79]\n\t"
      "v_mfma_f32_16x16x32_bf16 v[72:75], v[232:235], v[204:207], v[72:75]\n\t"
      "v_mfma_f32_16x16x32_bf16 v[68:71], v[228:231], v[204:207], v[68:71]\n\t"
      "v_mfma_f32_16x16x32_bf16 v[64:67], v[224:227], v[204:207], v[64:67]\n\t"
      "s_mov_b32 s48, 0\n\t"
      "s_waitcnt vmcnt(0)\n\t"
      "s_branch PG_PO0_CHK_%=\n\t"
      "PG_PO0_%=:\n\t"
      "buffer_load_dwordx2 v[12:13], %[vPl], %[srdWS], %[sPO] offen sc1\n\t"
      "s_waitcnt vmcnt(0)\n\t"
      "PG_PO0_CHK_%=:\n\t"
      "v_cmp_eq_u64 vcc, %[tag], v[12:13]\n\t"
      "s_add_u32 s48, s48, 1\n\t"
      "s_bfe_u32 s43, vcc_lo, 524288\n\t"
      "s_cmp_eq_u32 s43, 0xff\n\t"
      "s_cbranch_scc1 PG_PO0_OK_%=\n\t"
      "s_cmp_lt_u32 s48, 0x40000\n\t"
      "s_cbranch_scc1 PG_PO0_%=\n\t"
      "PG_PO0_OK_%=:\n\t"
      "s_waitcnt vmcnt(0) lgkmcnt(0)\n\t"
      "v_mfma_f32_16x16x32_bf16 v[16:19], v[208:211], v[80:83], v[16:19]\n\t"
      "v_add_u32 v8, 106496, v8\n\t"
      "v_add_u32 v9, 106496, v9\n\t"
      "v_add_u32 v10, 106496, v10\n\t"
      "v_add_u32 v11, 106496, v11\n\t"
      "ds_read_b64_tr_b16 v[224:225], v8 offset:0\n\t"
      "buffer_load_dwordx4 v[112:115], %[vXsrc], %[srdWS], s40 offen offset:0 sc1\n\t"
      "v_mfma_f32_16x16x32_bf16 v[20:23], v[212:215], v[80:83], v[20:23]\n\t"
      "ds_read_b64_tr_b16 v[226:227], v8 offset:2048\n\t"
      "v_mfma_f32_16x16x32_bf16 v[24:27], v[216:219], v[80:83], v[24:27]\n\t"
      "ds_read_b64_tr_b16 v[228:229], v9 offset:0\n\t"
      "s_add_u32 s50, s40, %[sXo]\n\t"
      "buffer_load_dwordx4 v[116:119], %[vXsrc], %[srdWS], s40 offen offset:256 sc1\n\t"
      "v_mfma_f32_16x16x32_bf16 v[28:31], v[220:223], v[80:83], v[28:31]\n\t"
      "ds_read_b64_tr_b16 v[230:231], v9 offset:2048\n\t"
      "v_mfma_f32_16x16x32_bf16 v[44:47], v[220:223], v[84:87], v[44:47]\n\t"
      "ds_read_b64_tr_b16 v[232:233], v10 offset:0\n\t"
      "buffer_load_dwordx4 v[120:123], %[vXsrc], %[srdWS], s50 offen offset:0 sc1\n\t"
      "v_mfma_f32_16x16x32_bf16 v[40:43], v[216:219], v[84:87], v[40:43]\n\t"
      "ds_read_b64_tr_b16 v[234:235], v10 offset:2048\n\t"
      "v_mfma_f32_16x16x32_bf16 v[36:39], v[212:215], v[84:87], v[36:39]\n\t"
      "ds_read_b64_tr_b16 v[236:237], v11 offset:0\n\t"
      "buffer_load_dwordx4 v[124:127], %[vXsrc], %[srdWS], s50 offen offset:256 sc1\n\t"
      "s_add_u32 s40, s40, 4096\n\t"
      "v_mfma_f32_16x16x32_bf16 v[32:35], v[208:211], v[84:87], v[32:35]\n\t"
      "ds_read_b64_tr_b16 v[238:239], v11 offset:2048\n\t"
      "v_mfma_f32_16x16x32_bf16 v[48:51], v[208:211], v[88:91], v[48:51]\n\t"
      "buffer_load_dwordx4 v[128:131], %[vXsrc], %[srdWS], s40 offen offset:0 sc1\n\t"
      "v_mfma_f32_16x16x32_bf16 v[52:55], v[212:215], v[88:91], v[52:55]\n\t"
      "v_mfma_f32_16x16x32_bf16 v[56:59], v[216:219], v[88:91], v[56:59]\n\t"
      "s_add_u32 s50, s40, %[sXo]\n\t"
      "buffer_load_dwordx4 v[132:135], %[vXsrc], %[srdWS], s40 offen offset:256 sc1\n\t"
      "v_mfma_f32_16x16x32_bf16 v[60:63], v[220:223], v[88:91], v[60:63]\n\t"
      "v_mfma_f32_16x16x32_bf16 v[76:79], v[220:223], v[92:95], v[76:79]\n\t"
      "buffer_load_dwordx4 v[136:139], %[vXsrc], %[srdWS], s50 offen offset:0 sc1\n\t"
      "v_mfma_f32_16x16x32_bf16 v[72:75], v[216:219], v[92:95], v[72:75]\n\t"
      "v_mfma_f32_16x16x32_bf16 v[68:71], v[212:215], v[92:95], v[68:71]\n\t"
      "buffer_load_dwordx4 v[140:143], %[vXsrc], %[srdWS], s50 offen offset:256 sc1\n\t"
      "s_add_u32 s40, s40, 4096\n\t"
      "v_mfma_f32_16x16x32_bf16 v[64:67], v[208:211], v[92:95], v[64:67]\n\t"
      "s_waitcnt vmcnt(8) lgkmcnt(0)\n\t"
      "v_mfma_f32_16x16x32_bf16 v[16:19], v[224:227], v[96:99], v[16:19]\n\t"
      "ds_read_b64_tr_b16 v[208:209], v8 offset:8192\n\t"
      "buffer_load_dwordx4 v[144:147], %[vXsrc], %[srdWS], s40 offen offset:0 sc1\n\t"
      "v_mfma_f32_16x16x32_bf16 v[20:23], v[228:231], v[96:99], v[20:23]\n\t"
      "ds_read_b64_tr_b16 v[210:211], v8 offset:10240\n\t"
      "v_mfma_f32_16x16x32_bf16 v[24:27], v[232:235], v[96:99], v[24:27]\n\t"
      "ds_read_b64_tr_b16 v[212:213], v9 offset:8192\n\t"
      "s_add_u32 s50, s40, %[sXo]\n\t"
      "buffer_load_dwordx4 v[148:151], %[vXsrc], %[srdWS], s40 offen offset:256 sc1\n\t"
      "v_mfma_f32_16x16x32_bf16 v[28:31], v[236:239], v[96:99], v[28:31]\n\t"
      "ds_read_b64_tr_b16 v[214:215], v9 offset:10240\n\t"
      "v_mfma_f32_16x16x32_bf16 v[44:47], v[236:239], v[100:103], v[44:47]\n\t"
      "ds_read_b64_tr_b16 v[216:217], v10 offset:8192\n\t"
      "buffer_load_dwordx4 v[152:155], %[vXsrc], %[srdWS], s50 offen offset:0 sc1\n\t"
      "v_mfma_f32_16x16x32_bf16 v[40:43], v[232:235], v[100:103], v[40:43]\n\t"
      "ds_read_b64_tr_b16 v[218:219], v10 offset:10240\n\t"
      "v_mfma_f32_16x16x32_bf16 v[36:39], v[228:231], v[100:103], v[36:39]\n\t"
      "ds_read_b64_tr_b16 v[220:221], v11 offset:8192\n\t"
      "buffer_load_dwordx4 v[156:159], %[vXsrc], %[srdWS], s50 offen offset:256 sc1\n\t"
      "s_add_u32 s40, s40, 4096\n\t"
      "v_mfma_f32_16x16x32_bf16 v[32:35], v[224:227], v[100:103], v[32:35]\n\t"
      "ds_read_b64_tr_b16 v[222:223], v11 offset:10240\n\t"
      "v_mfma_f32_16x16x32_bf16 v[48:51], v[224:227], v[104:107], v[48:51]\n\t"
      "buffer_load_dwordx4 v[160:163], %[vXsrc], %[srdWS], s40 offen offset:0 sc1\n\t"
      "v_mfma_f32_16x16x32_bf16 v[52:55], v[228:231], v[104:107], v[52:55]\n\t"
      "v_mfma_f32_16x16x32_bf16 v[56:59], v[232:235], v[104:107], v[56:59]\n\t"
      "s_add_u32 s50, s40, %[sXo]\n\t"
      "buffer_load_dwordx4 v[164:167], %[vXsrc], %[srdWS], s40 offen offset:256 sc1\n\t"
      "v_mfma_f32_16x16x32_bf16 v[60:63], v[236:239], v[104:107], v[60:63]\n\t"
      "v_mfma_f32_16x16x32_bf16 v[76:79], v[236:239], v[108:111], v[76:79]\n\t"
      "buffer_load_dwordx4 v[168:171], %[vXsrc], %[srdWS], s50 offen offset:0 sc1\n\t"
      "v_mfma_f32_16x16x32_bf16 v[72:75], v[232:235], v[108:111], v[72:75]\n\t"
      "v_mfma_f32_16x16x32_bf16 v[68:71], v[228:231], v[108:111], v[68:71]\n\t"
      "buffer_load_dwordx4 v[172:175], %[vXsrc], %[srdWS], s50 offen offset:256 sc1\n\t"
      "s_add_u32 s40, s40, 4096\n\t"
      "v_mfma_f32_16x16x32_bf16 v[64:67], v[224:227], v[108:111], v[64:67]\n\t"
      "s_mov_b32 s48, 0\n\t"
      "s_branch PG_PO1_CHK_%=\n\t"
      "PG_PO1_%=:\n\t"
      "buffer_load_dwordx2 v[12:13], %[vPl], %[srdWS], %[sPO] offen sc1\n\t"
      "s_waitcnt vmcnt(0)\n\t"
      "PG_PO1_CHK_%=:\n\t"
      "v_cmp_eq_u64 vcc, %[tag], v[12:13]\n\t"
      "s_add_u32 s48, s48, 1\n\t"
      "s_bfe_u32 s43, vcc_lo, 524296\n\t"
      "s_cmp_eq_u32 s43, 0xff\n\t"
      "s_cbranch_scc1 PG_PO1_OK_%=\n\t"
      "s_cmp_lt_u32 s48, 0x40000\n\t"
      "s_cbranch_scc1 PG_PO1_%=\n\t"
      "PG_PO1_OK_%=:\n\t"
      "s_waitcnt vmcnt(12) lgkmcnt(0)\n\t"
      "v_mfma_f32_16x16x32_bf16 v[16:19], v[208:211], v[112:115], v[16:19]\n\t"
      "ds_read_b64_tr_b16 v[224:225], v8 offset:16384\n\t"
      "buffer_load_dwordx4 v[176:179], %[vXsrc], %[srdWS], s40 offen offset:0 sc1\n\t"
      "v_mfma_f32_16x16x32_bf16 v[20:23], v[212:215], v[112:115], v[20:23]\n\t"
      "ds_read_b64_tr_b16 v[226:227], v8 offset:18432\n\t"
      "v_mfma_f32_16x16x32_bf16 v[24:27], v[216:219], v[112:115], v[24:27]\n\t"
      "ds_read_b64_tr_b16 v[228:229], v9 offset:16384\n\t"
      "s_add_u32 s50, s40, %[sXo]\n\t"
      "buffer_load_dwordx4 v[180:183], %[vXsrc], %[srdWS], s40 offen offset:256 sc1\n\t"
      "v_mfma_f32_16x16x32_bf16 v[28:31], v[220:223], v[112:115], v[28:31]\n\t"
      "ds_read_b64_tr_b16 v[230:231], v9 offset:18432\n\t"
      "v_mfma_f32_16x16x32_bf16 v[44:47], v[220:223], v[116:119], v[44:47]\n\t"
      "ds_read_b64_tr_b16 v[232:233], v10 offset:16384\n\t"
      "buffer_load_dwordx4 v[184:187], %[vXsrc], %[srdWS], s50 offen offset:0 sc1\n\t"
      "v_mfma_f32_16x16x32_bf16 v[40:43], v[216:219], v[116:119], v[40:43]\n\t"
      "ds_read_b64_tr_b16 v[234:235], v10 offset:18432\n\t"
      "v_mfma_f32_16x16x32_bf16 v[36:39], v[212:215], v[116:119], v[36:39]\n\t"
      "ds_read_b64_tr_b16 v[236:237], v11 offset:16384\n\t"
      "buffer_load_dwordx4 v[188:191], %[vXsrc], %[srdWS], s50 offen offset:256 sc1\n\t"
      "s_add_u32 s40, s40, 4096\n\t"
      "v_mfma_f32_16x16x32_bf16 v[32:35], v[208:211], v[116:119], v[32:35]\n\t"
      "ds_read_b64_tr_b16 v[238:239], v11 offset:18432\n\t"
      "v_mfma_f32_16x16x32_bf16 v[48:51], v[208:211], v[120:123], v[48:51]\n\t"
      "v_mfma_f32_16x16x32_bf16 v[52:55], v[212:215], v[120:123], v[52:55]\n\t"
      "v_mfma_f32_16x16x32_bf16 v[56:59], v[216:219], v[120:123], v[56:59]\n\t"
      "v_mfma_f32_16x16x32_bf16 v[60:63], v[220:223], v[120:123], v[60:63]\n\t"
      "v_mfma_f32_16x16x32_bf16 v[76:79], v[220:223], v[124:127], v[76:79]\n\t"
      "v_mfma_f32_16x16x32_bf16 v[72:75], v[216:219], v[124:127], v[72:75]\n\t"
      "v_mfma_f32_16x16x32_bf16 v[68:71], v[212:215], v[124:127], v[68:71]\n\t"
      "v_mfma_f32_16x16x32_bf16 v[64:67], v[208:211], v[124:127], v[64:67]\n\t"
      "s_waitcnt vmcnt(16) lgkmcnt(0)\n\t"
      "s_barrier\n\t"
      "s_waitcnt vmcnt(12) lgkmcnt(0)\n\t"
      "v_mfma_f32_16x16x32_bf16 v[16:19], v[224:227], v[128:131], v[16:19]\n\t"
      "ds_read_b64_tr_b16 v[208:209], v8 offset:24576\n\t"
      "buffer_load_dwordx4 v[192:195], %[vXsrc], %[srdWS], s40 offen offset:0 sc1\n\t"
      "v_mfma_f32_16x16x32_bf16 v[20:23], v[228:231], v[128:131], v[20:23]\n\t"
      "ds_read_b64_tr_b16 v[210:211], v8 offset:26624\n\t"
      "v_mfma_f32_16x16x32_bf16 v[24:27], v[232:235], v[128:131], v[24:27]\n\t"
      "ds_read_b64_tr_b16 v[212:213], v9 offset:24576\n\t"
      "s_add_u32 s50, s40, %[sXo]\n\t"
      "buffer_load_dwordx4 v[196:199], %[vXsrc], %[srdWS], s40 offen offset:256 sc1\n\t"
      "v_mfma_f32_16x16x32_bf16 v[28:31], v[236:239], v[128:131], v[28:31]\n\t"
      "ds_read_b64_tr_b16 v[214:215], v9 offset:26624\n\t"
      "v_mfma_f32_16x16x32_bf16 v[44:47], v[236:239], v[132:135], v[44:47]\n\t"
      "ds_read_b64_tr_b16 v[216:217], v10 offset:24576\n\t"
      "buffer_load_dwordx4 v[200:203], %[vXsrc], %[srdWS], s50 offen offset:0 sc1\n\t"
      "v_mfma_f32_16x16x32_bf16 v[40:43], v[232:235], v[132:135], v[40:43]\n\t"
      "ds_read_b64_tr_b16 v[218:219], v10 offset:26624\n\t"
      "v_mfma_f32_16x16x32_bf16 v[36:39], v[228:231], v[132:135], v[36:39]\n\t"
      "ds_read_b64_tr_b16 v[220:221], v11 offset:24576\n\t"
      "buffer_load_dwordx4 v[204:207], %[vXsrc], %[srdWS], s50 offen offset:256 sc1\n\t"
      "s_add_u32 s40, s40, 4096\n\t"
      "v_mfma_f32_16x16x32_bf16 v[32:35], v[224:227], v[132:135], v[32:35]\n\t"
      "ds_read_b64_tr_b16 v[222:223], v11 offset:26624\n\t"
      "v_mfma_f32_16x16x32_bf16 v[48:51], v[224:227], v[136:139], v[48:51]\n\t"
      "s_add_u32 m0, s42, 32768\n\t"
      "s_add_u32 s43, s41, 15360\n\t"
      "buffer_load_dwordx4 %[vWsrc], s[44:47], s41 offen lds\n\t"
      "v_mfma_f32_16x16x32_bf16 v[52:55], v[228:231], v[136:139], v[52:55]\n\t"
      "v_mfma_f32_16x16x32_bf16 v[56:59], v[232:235], v[136:139], v[56:59]\n\t"
      "buffer_load_dwordx4 %[vWsrc], s[44:47], s43 offen offset:1024 lds\n\t"
      "s_add_u32 s41, s41, 131072\n\t"
      "v_mfma_f32_16x16x32_bf16 v[60:63], v[236:239], v[136:139], v[60:63]\n\t"
      "v_mfma_f32_16x16x32_bf16 v[76:79], v[236:239], v[140:143], v[76:79]\n\t"
      "s_add_u32 m0, s42, 40960\n\t"
      "s_add_u32 s43, s41, 15360\n\t"
      "buffer_load_dwordx4 %[vWsrc], s[44:47], s41 offen lds\n\t"
      "v_mfma_f32_16x16x32_bf16 v[72:75], v[232:235], v[140:143], v[72:75]\n\t"
      "v_mfma_f32_16x16x32_bf16 v[68:71], v[228:231], v[140:143], v[68:71]\n\t"
      "buffer_load_dwordx4 %[vWsrc], s[44:47], s43 offen offset:1024 lds\n\t"
      "s_add_u32 s41, s41, 131072\n\t"
      "v_mfma_f32_16x16x32_bf16 v[64:67], v[224:227], v[140:143], v[64:67]\n\t"
      "s_waitcnt vmcnt(16) lgkmcnt(0)\n\t"
      "v_mfma_f32_16x16x32_bf16 v[16:19], v[208:211], v[144:147], v[16:19]\n\t"
      "v_add_u32 v8, 4294860800, v8\n\t"
      "v_add_u32 v9, 4294860800, v9\n\t"
      "v_add_u32 v10, 4294860800, v10\n\t"
      "v_add_u32 v11, 4294860800, v11\n\t"
      "ds_read_b64_tr_b16 v[224:225], v8 offset:16384\n\t"
      "buffer_load_dwordx4 v[80:83], %[vXsrc], %[srdWS], s40 offen offset:0 sc1\n\t"
      "v_mfma_f32_16x16x32_bf16 v[20:23], v[212:215], v[144:147], v[20:23]\n\t"
      "ds_read_b64_tr_b16 v[226:227], v8 offset:18432\n\t"
      "v_mfma_f32_16x16x32_bf16 v[24:27], v[216:219], v[144:147], v[24:27]\n\t"
      "ds_read_b64_tr_b16 v[228:229], v9 offset:16384\n\t"
      "s_add_u32 s50, s40, %[sXo]\n\t"
      "buffer_load_dwordx4 v[84:87], %[vXsrc], %[srdWS], s40 offen offset:256 sc1\n\t"
      "v_mfma_f32_16x16x32_bf16 v[28:31], v[220:223], v[144:147], v[28:31]\n\t"
      "ds_read_b64_tr_b16 v[230:231], v9 offset:18432\n\t"
      "v_mfma_f32_16x16x32_bf16 v[44:47], v[220:223], v[148:151], v[44:47]\n\t"
      "ds_read_b64_tr_b16 v[232:233], v10 offset:16384\n\t"
      "buffer_load_dwordx4 v[88:91], %[vXsrc], %[srdWS], s50 offen offset:0 sc1\n\t"
      "v_mfma_f32_16x16x32_bf16 v[40:43], v[216:219], v[148:151], v[40:43]\n\t"
      "ds_read_b64_tr_b16 v[234:235], v10 offset:18432\n\t"
      "v_mfma_f32_16x16x32_bf16 v[36:39], v[212:215], v[148:151], v[36:39]\n\t"
      "ds_read_b64_tr_b16 v[236:237], v11 offset:16384\n\t"
      "buffer_load_dwordx4 v[92:95], %[vXsrc], %[srdWS], s50 offen offset:256 sc1\n\t"
      "s_add_u32 s40, s40, 4096\n\t"
      "v_mfma_f32_16x16x32_bf16 v[32:35], v[208:211], v[148:151], v[32:35]\n\t"
      "ds_read_b64_tr_b16 v[238:239], v11 offset:18432\n\t"
      "v_mfma_f32_16x16x32_bf16 v[48:51], v[208:211], v[152:155], v[48:51]\n\t"
      "s_add_u32 m0, s42, 49152\n\t"
      "s_add_u32 s43, s41, 15360\n\t"
      "buffer_load_dwordx4 %[vWsrc], s[44:47], s41 offen lds\n\t"
      "v_mfma_f32_16x16x32_bf16 v[52:55], v[212:215], v[152:155], v[52:55]\n\t"
      "v_mfma_f32_16x16x32_bf16 v[56:59], v[216:219], v[152:155], v[56:59]\n\t"
      "buffer_load_dwordx4 %[vWsrc], s[44:47], s43 offen offset:1024 lds\n\t"
      "s_add_u32 s41, s41, 131072\n\t"
      "v_mfma_f32_16x16x32_bf16 v[60:63], v[220:223], v[152:155], v[60:63]\n\t"
      "v_mfma_f32_16x16x32_bf16 v[76:79], v[220:223], v[156:159], v[76:79]\n\t"
      "s_add_u32 m0, s42, 57344\n\t"
      "s_add_u32 s43, s41, 15360\n\t"
      "buffer_load_dwordx4 %[vWsrc], s[44:47], s41 offen lds\n\t"
      "v_mfma_f32_16x16x32_bf16 v[72:75], v[216:219], v[156:159], v[72:75]\n\t"
      "v_mfma_f32_16x16x32_bf16 v[68:71], v[212:215], v[156:159], v[68:71]\n\t"
      "buffer_load_dwordx4 %[vWsrc], s[44:47], s43 offen offset:1024 lds\n\t"
      "s_add_u32 s41, s41, 131072\n\t"
      "v_mfma_f32_16x16x32_bf16 v[64:67], v[208:211], v[156:159], v[64:67]\n\t"
      "s_waitcnt vmcnt(20) lgkmcnt(0)\n\t"
      "v_mfma_f32_16x16x32_bf16 v[16:19], v[224:227], v[160:163], v[16:19]\n\t"
      "ds_read_b64_tr_b16 v[208:209], v8 offset:24576\n\t"
      "buffer_load_dwordx4 v[96:99], %[vXsrc], %[srdWS], s40 offen offset:0 sc1\n\t"
      "v_mfma_f32_16x16x32_bf16 v[20:23], v[228:231], v[160:163], v[20:23]\n\t"
      "ds_read_b64_tr_b16 v[210:211], v8 offset:26624\n\t"
      "v_mfma_f32_16x16x32_bf16 v[24:27], v[232:235], v[160:163], v[24:27]\n\t"
      "ds_read_b64_tr_b16 v[212:213], v9 offset:24576\n\t"
      "s_add_u32 s50, s40, %[sXo]\n\t"
      "buffer_load_dwordx4 v[100:103], %[vXsrc], %[srdWS], s40 offen offset:256 sc1\n\t"
      "v_mfma_f32_16x16x32_bf16 v[28:31], v[236:239], v[160:163], v[28:31]\n\t"
      "ds_read_b64_tr_b16 v[214:215], v9 offset:26624\n\t"
      "v_mfma_f32_16x16x32_bf16 v[44:47], v[236:239], v[164:167], v[44:47]\n\t"
      "ds_read_b64_tr_b16 v[216:217], v10 offset:24576\n\t"
      "buffer_load_dwordx4 v[104:107], %[vXsrc], %[srdWS], s50 offen offset:0 sc1\n\t"
      "v_mfma_f32_16x16x32_bf16 v[40:43], v[232:235], v[164:167], v[40:43]\n\t"
      "ds_read_b64_tr_b16 v[218:219], v10 offset:26624\n\t"
      "v_mfma_f32_16x16x32_bf16 v[36:39], v[228:231], v[164:167], v[36:39]\n\t"
      "ds_read_b64_tr_b16 v[220:221], v11 offset:24576\n\t"
      "buffer_load_dwordx4 v[108:111], %[vXsrc], %[srdWS], s50 offen offset:256 sc1\n\t"
      "s_add_u32 s40, s40, 4096\n\t"
      "v_mfma_f32_16x16x32_bf16 v[32:35], v[224:227], v[164:167], v[32:35]\n\t"
      "ds_read_b64_tr_b16 v[222:223], v11 offset:26624\n\t"
      "v_mfma_f32_16x16x32_bf16 v[48:51], v[224:227], v[168:171], v[48:51]\n\t"
      "s_add_u32 m0, s42, 98304\n\t"
      "s_add_u32 s43, s41, 15360\n\t"
      "buffer_load_dwordx4 %[vWsrc], s[44:47], s41 offen lds\n\t"
      "v_mfma_f32_16x16x32_bf16 v[52:55], v[228:231], v[168:171], v[52:55]\n\t"
      "v_mfma_f32_16x16x32_bf16 v[56:59], v[232:235], v[168:171], v[56:59]\n\t"
      "buffer_load_dwordx4 %[vWsrc], s[44:47], s43 offen offset:1024 lds\n\t"
      "s_add_u32 s41, s41, 131072\n\t"
      "v_mfma_f32_16x16x32_bf16 v[60:63], v[236:239], v[168:171], v[60:63]\n\t"
      "v_mfma_f32_16x16x32_bf16 v[76:79], v[236:239], v[172:175], v[76:79]\n\t"
      "s_add_u32 m0, s42, 0\n\t"
      "s_add_u32 s43, s41, 15360\n\t"
      "buffer_load_dwordx4 %[vWsrc], s[44:47], s41 offen lds\n\t"
      "v_mfma_f32_16x16x32_bf16 v[72:75], v[232:235], v[172:175], v[72:75]\n\t"
      "v_mfma_f32_16x16x32_bf16 v[68:71], v[228:231], v[172:175], v[68:71]\n\t"
      "buffer_load_dwordx4 %[vWsrc], s[44:47], s43 offen offset:1024 lds\n\t"
      "s_add_u32 s41, s41, 131072\n\t"
      "v_mfma_f32_16x16x32_bf16 v[64:67], v[224:227], v[172:175], v[64:67]\n\t"
      "s_mov_b32 s48, 0\n\t"
      "s_branch PG_PO2_CHK_%=\n\t"
      "PG_PO2_%=:\n\t"
      "buffer_load_dwordx2 v[12:13], %[vPl], %[srdWS], %[sPO] offen sc1\n\t"
      "s_waitcnt vmcnt(0)\n\t"
      "PG_PO2_CHK_%=:\n\t"
      "v_cmp_eq_u64 vcc, %[tag], v[12:13]\n\t"
      "s_add_u32 s48, s48, 1\n\t"
      "s_bfe_u32 s43, vcc_lo, 524304\n\t"
      "s_cmp_eq_u32 s43, 0xff\n\t"
      "s_cbranch_scc1 PG_PO2_OK_%=\n\t"
      "s_cmp_lt_u32 s48, 0x40000\n\t"
      "s_cbranch_scc1 PG_PO2_%=\n\t"
      "PG_PO2_OK_%=:\n\t"
      "s_waitcnt vmcnt(24) lgkmcnt(0)\n\t"
      "v_mfma_f32_16x16x32_bf16 v[16:19], v[208:211], v[176:179], v[16:19]\n\t"
      "v_add_u32 v8, 139264, v8\n\t"
      "v_add_u32 v9, 139264, v9\n\t"
      "v_add_u32 v10, 139264, v10\n\t"
      "v_add_u32 v11, 139264, v11\n\t"
      "ds_read_b64_tr_b16 v[224:225], v8 offset:0\n\t"
      "buffer_load_dwordx4 v[112:115], %[vXsrc], %[srdWS], s40 offen offset:0 sc1\n\t"
      "v_mfma_f32_16x16x32_bf16 v[20:23], v[212:215], v[176:179], v[20:23]\n\t"
      "ds_read_b64_tr_b16 v[226:227], v8 offset:2048\n\t"
      "v_mfma_f32_16x16x32_bf16 v[24:27], v[216:219], v[176:179], v[24:27]\n\t"
      "ds_read_b64_tr_b16 v[228:229], v9 offset:0\n\t"
      "s_add_u32 s50, s40, %[sXo]\n\t"
      "buffer_load_dwordx4 v[116:119], %[vXsrc], %[srdWS], s40 offen offset:256 sc1\n\t"
      "v_mfma_f32_16x16x32_bf16 v[28:31], v[220:223], v[176:179], v[28:31]\n\t"
      "ds_read_b64_tr_b16 v[230:231], v9 offset:2048\n\t"
      "v_mfma_f32_16x16x32_bf16 v[44:47], v[220:223], v[180:183], v[44:47]\n\t"
      "ds_read_b64_tr_b16 v[232:233], v10 offset:0\n\t"
      "buffer_load_dwordx4 v[120:123], %[vXsrc], %[srdWS], s50 offen offset:0 sc1\n\t"
      "v_mfma_f32_16x16x32_bf16 v[40:43], v[216:219], v[180:183], v[40:43]\n\t"
      "ds_read_b64_tr_b16 v[234:235], v10 offset:2048\n\t"
      "v_mfma_f32_16x16x32_bf16 v[36:39], v[212:215], v[180:183], v[36:39]\n\t"
      "ds_read_b64_tr_b16 v[236:237], v11 offset:0\n\t"
      "buffer_load_dwordx4 v[124:127], %[vXsrc], %[srdWS], s50 offen offset:256 sc1\n\t"
      "s_add_u32 s40, s40, 4096\n\t"
      "v_mfma_f32_16x16x32_bf16 v[32:35], v[208:211], v[180:183], v[32:35]\n\t"
      "ds_read_b64_tr_b16 v[238:239], v11 offset:2048\n\t"
      "v_mfma_f32_16x16x32_bf16 v[48:51], v[208:211], v[184:187], v[48:51]\n\t"
      "v_mfma_f32_16x16x32_bf16 v[52:55], v[212:215], v[184:187], v[52:55]\n\t"
      "v_mfma_f32_16x16x32_bf16 v[56:59], v[216:219], v[184:187], v[56:59]\n\t"
      "v_mfma_f32_16x16x32_bf16 v[60:63], v[220:223], v[184:187], v[60:63]\n\t"
      "v_mfma_f32_16x16x32_bf16 v[76:79], v[220:223], v[188:191], v[76:79]\n\t"
      "v_mfma_f32_16x16x32_bf16 v[72:75], v[216:219], v[188:191], v[72:75]\n\t"
      "v_mfma_f32_16x16x32_bf16 v[68:71], v[212:215], v[188:191], v[68:71]\n\t"
      "v_mfma_f32_16x16x32_bf16 v[64:67], v[208:211], v[188:191], v[64:67]\n\t"
      "s_waitcnt vmcnt(24) lgkmcnt(0)\n\t"
      "v_mfma_f32_16x16x32_bf16 v[16:19], v[224:227], v[192:195], v[16:19]\n\t"
      "ds_read_b64_tr_b16 v[208:209], v8 offset:8192\n\t"
      "buffer_load_dwordx4 v[128:131], %[vXsrc], %[srdWS], s40 offen offset:0 sc1\n\t"
      "v_mfma_f32_16x16x32_bf16 v[20:23], v[228:231], v[192:195], v[20:23]\n\t"
      "ds_read_b64_tr_b16 v[210:211], v8 offset:10240\n\t"
      "v_mfma_f32_16x16x32_bf16 v[24:27], v[232:235], v[192:195], v[24:27]\n\t"
      "ds_read_b64_tr_b16 v[212:213], v9 offset:8192\n\t"
      "s_add_u32 s50, s40, %[sXo]\n\t"
      "buffer_load_dwordx4 v[132:135], %[vXsrc], %[srdWS], s40 offen offset:256 sc1\n\t"
      "v_mfma_f32_16x16x32_bf16 v[28:31], v[236:239], v[192:195], v[28:31]\n\t"
      "ds_read_b64_tr_b16 v[214:215], v9 offset:10240\n\t"
      "v_mfma_f32_16x16x32_bf16 v[44:47], v[236:239], v[196:199], v[44:47]\n\t"
      "ds_read_b64_tr_b16 v[216:217], v10 offset:8192\n\t"
      "buffer_load_dwordx4 v[136:139], %[vXsrc], %[srdWS], s50 offen offset:0 sc1\n\t"
      "v_mfma_f32_16x16x32_bf16 v[40:43], v[232:235], v[196:199], v[40:43]\n\t"
      "ds_read_b64_tr_b16 v[218:219], v10 offset:10240\n\t"
      "v_mfma_f32_16x16x32_bf16 v[36:39], v[228:231], v[196:199], v[36:39]\n\t"
      "ds_read_b64_tr_b16 v[220:221], v11 offset:8192\n\t"
      "buffer_load_dwordx4 v[140:143], %[vXsrc], %[srdWS], s50 offen offset:256 sc1\n\t"
      "s_add_u32 s40, s40, 4096\n\t"
      "v_mfma_f32_16x16x32_bf16 v[32:35], v[224:227], v[196:199], v[32:35]\n\t"
      "ds_read_b64_tr_b16 v[222:223], v11 offset:10240\n\t"
      "v_mfma_f32_16x16x32_bf16 v[48:51], v[224:227], v[200:203], v[48:51]\n\t"
      "v_mfma_f32_16x16x32_bf16 v[52:55], v[228:231], v[200:203], v[52:55]\n\t"
      "v_mfma_f32_16x16x32_bf16 v[56:59], v[232:235], v[200:203], v[56:59]\n\t"
      "v_mfma_f32_16x16x32_bf16 v[60:63], v[236:239], v[200:203], v[60:63]\n\t"
      "v_mfma_f32_16x16x32_bf16 v[76:79], v[236:239], v[204:207], v[76:79]\n\t"
      "v_mfma_f32_16x16x32_bf16 v[72:75], v[232:235], v[204:207], v[72:75]\n\t"
      "v_mfma_f32_16x16x32_bf16 v[68:71], v[228:231], v[204:207], v[68:71]\n\t"
      "v_mfma_f32_16x16x32_bf16 v[64:67], v[224:227], v[204:207], v[64:67]\n\t"
      "s_waitcnt vmcnt(8) lgkmcnt(0)\n\t"
      "s_barrier\n\t"
      "s_waitcnt vmcnt(8) lgkmcnt(0)\n\t"
      "v_mfma_f32_16x16x32_bf16 v[16:19], v[208:211], v[80:83], v[16:19]\n\t"
      "ds_read_b64_tr_b16 v[224:225], v8 offset:16384\n\t"
      "buffer_load_dwordx4 v[144:147], %[vXsrc], %[srdWS], s40 offen offset:0 sc1\n\t"
      "v_mfma_f32_16x16x32_bf16 v[20:23], v[212:215], v[80:83], v[20:23]\n\t"
      "ds_read_b64_tr_b16 v[226:227], v8 offset:18432\n\t"
      "v_mfma_f32_16x16x32_bf16 v[24:27], v[216:219], v[80:83], v[24:27]\n\t"
      "ds_read_b64_tr_b16 v[228:229], v9 offset:16384\n\t"
      "s_add_u32 s50, s40, %[sXo]\n\t"
      "buffer_load_dwordx4 v[148:151], %[vXsrc], %[srdWS], s40 offen offset:256 sc1\n\t"
      "v_mfma_f32_16x16x32_bf16 v[28:31], v[220:223], v[80:83], v[28:31]\n\t"
      "ds_read_b64_tr_b16 v[230:231], v9 offset:18432\n\t"
      "v_mfma_f32_16x16x32_bf16 v[44:47], v[220:223], v[84:87], v[44:47]\n\t"
      "ds_read_b64_tr_b16 v[232:233], v10 offset:16384\n\t"
      "buffer_load_dwordx4 v[152:155], %[vXsrc], %[srdWS], s50 offen offset:0 sc1\n\t"
      "v_mfma_f32_16x16x32_bf16 v[40:43], v[216:219], v[84:87], v[40:43]\n\t"
      "ds_read_b64_tr_b16 v[234:235], v10 offset:18432\n\t"
      "v_mfma_f32_16x16x32_bf16 v[36:39], v[212:215], v[84:87], v[36:39]\n\t"
      "ds_read_b64_tr_b16 v[236:237], v11 offset:16384\n\t"
      "buffer_load_dwordx4 v[156:159], %[vXsrc], %[srdWS], s50 offen offset:256 sc1\n\t"
      "s_add_u32 s40, s40, 4096\n\t"
      "v_mfma_f32_16x16x32_bf16 v[32:35], v[208:211], v[84:87], v[32:35]\n\t"
      "ds_read_b64_tr_b16 v[238:239], v11 offset:18432\n\t"
      "v_mfma_f32_16x16x32_bf16 v[48:51], v[208:211], v[88:91], v[48:51]\n\t"
      "v_mfma_f32_16x16x32_bf16 v[52:55], v[212:215], v[88:91], v[52:55]\n\t"
      "v_mfma_f32_16x16x32_bf16 v[56:59], v[216:219], v[88:91], v[56:59]\n\t"
      "v_mfma_f32_16x16x32_bf16 v[60:63], v[220:223], v[88:91], v[60:63]\n\t"
      "v_mfma_f32_16x16x32_bf16 v[76:79], v[220:223], v[92:95], v[76:79]\n\t"
      "v_mfma_f32_16x16x32_bf16 v[72:75], v[216:219], v[92:95], v[72:75]\n\t"
      "v_mfma_f32_16x16x32_bf16 v[68:71], v[212:215], v[92:95], v[68:71]\n\t"
      "v_mfma_f32_16x16x32_bf16 v[64:67], v[208:211], v[92:95], v[64:67]\n\t"
      "s_waitcnt vmcnt(12) lgkmcnt(0)\n\t"
      "v_mfma_f32_16x16x32_bf16 v[16:19], v[224:227], v[96:99], v[16:19]\n\t"
      "v_add_u32 v8, 4294893568, v8\n\t"
      "v_add_u32 v9, 4294893568, v9\n\t"
      "v_add_u32 v10, 4294893568, v10\n\t"
      "v_add_u32 v11, 4294893568, v11\n\t"
      "ds_read_b64_tr_b16 v[208:209], v8 offset:0\n\t"
      "buffer_load_dwordx4 v[160:163], %[vXsrc], %[srdWS], s40 offen offset:0 sc1\n\t"
      "v_mfma_f32_16x16x32_bf16 v[20:23], v[228:231], v[96:99], v[20:23]\n\t"
      "ds_read_b64_tr_b16 v[210:211], v8 offset:2048\n\t"
      "v_mfma_f32_16x16x32_bf16 v[24:27], v[232:235], v[96:99], v[24:27]\n\t"
      "ds_read_b64_tr_b16 v[212:213], v9 offset:0\n\t"
      "s_add_u32 s50, s40, %[sXo]\n\t"
      "buffer_load_dwordx4 v[164:167], %[vXsrc], %[srdWS], s40 offen offset:256 sc1\n\t"
      "v_mfma_f32_16x16x32_bf16 v[28:31], v[236:239], v[96:99], v[28:31]\n\t"
      "ds_read_b64_tr_b16 v[214:215], v9 offset:2048\n\t"
      "v_mfma_f32_16x16x32_bf16 v[44:47], v[236:239], v[100:103], v[44:47]\n\t"
      "ds_read_b64_tr_b16 v[216:217], v10 offset:0\n\t"
      "buffer_load_dwordx4 v[168:171], %[vXsrc], %[srdWS], s50 offen offset:0 sc1\n\t"
      "v_mfma_f32_16x16x32_bf16 v[40:43], v[232:235], v[100:103], v[40:43]\n\t"
      "ds_read_b64_tr_b16 v[218:219], v10 offset:2048\n\t"
      "v_mfma_f32_16x16x32_bf16 v[36:39], v[228:231], v[100:103], v[36:39]\n\t"
      "ds_read_b64_tr_b16 v[220:221], v11 offset:0\n\t"
      "buffer_load_dwordx4 v[172:175], %[vXsrc], %[srdWS], s50 offen offset:256 sc1\n\t"
      "s_add_u32 s40, s40, 4096\n\t"
      "v_mfma_f32_16x16x32_bf16 v[32:35], v[224:227], v[100:103], v[32:35]\n\t"
      "ds_read_b64_tr_b16 v[222:223], v11 offset:2048\n\t"
      "v_mfma_f32_16x16x32_bf16 v[48:51], v[224:227], v[104:107], v[48:51]\n\t"
      "v_mfma_f32_16x16x32_bf16 v[52:55], v[228:231], v[104:107], v[52:55]\n\t"
      "v_mfma_f32_16x16x32_bf16 v[56:59], v[232:235], v[104:107], v[56:59]\n\t"
      "v_mfma_f32_16x16x32_bf16 v[60:63], v[236:239], v[104:107], v[60:63]\n\t"
      "v_mfma_f32_16x16x32_bf16 v[76:79], v[236:239], v[108:111], v[76:79]\n\t"
      "v_mfma_f32_16x16x32_bf16 v[72:75], v[232:235], v[108:111], v[72:75]\n\t"
      "v_mfma_f32_16x16x32_bf16 v[68:71], v[228:231], v[108:111], v[68:71]\n\t"
      "v_mfma_f32_16x16x32_bf16 v[64:67], v[224:227], v[108:111], v[64:67]\n\t"
      "s_mov_b32 s48, 0\n\t"
      "s_branch PG_PO3_CHK_%=\n\t"
      "PG_PO3_%=:\n\t"
      "buffer_load_dwordx2 v[12:13], %[vPl], %[srdWS], %[sPO] offen sc1\n\t"
      "s_waitcnt vmcnt(0)\n\t"
      "PG_PO3_CHK_%=:\n\t"
      "v_cmp_eq_u64 vcc, %[tag], v[12:13]\n\t"
      "s_add_u32 s48, s48, 1\n\t"
      "s_bfe_u32 s43, vcc_lo, 524312\n\t"
      "s_cmp_eq_u32 s43, 0xff\n\t"
      "s_cbranch_scc1 PG_PO3_OK_%=\n\t"
      "s_cmp_lt_u32 s48, 0x40000\n\t"
      "s_cbranch_scc1 PG_PO3_%=\n\t"
      "PG_PO3_OK_%=:\n\t"
      "s_waitcnt vmcnt(12) lgkmcnt(0)\n\t"
      "v_mfma_f32_16x16x32_bf16 v[16:19], v[208:211], v[112:115], v[16:19]\n\t"
      "ds_read_b64_tr_b16 v[224:225], v8 offset:8192\n\t"
      "buffer_load_dwordx4 v[176:179], %[vXsrc], %[srdWS], s40 offen offset:0 sc1\n\t"
      "v_mfma_f32_16x16x32_bf16 v[20:23], v[212:215], v[112:115], v[20:23]\n\t"
      "ds_read_b64_tr_b16 v[226:227], v8 offset:10240\n\t"
      "v_mfma_f32_16x16x32_bf16 v[24:27], v[216:219], v[112:115], v[24:27]\n\t"
      "ds_read_b64_tr_b16 v[228:229], v9 offset:8192\n\t"
      "s_add_u32 s50, s40, %[sXo]\n\t"
      "buffer_load_dwordx4 v[180:183], %[vXsrc], %[srdWS], s40 offen offset:256 sc1\n\t"
      "v_mfma_f32_16x16x32_bf16 v[28:31], v[220:223], v[112:115], v[28:31]\n\t"
      "ds_read_b64_tr_b16 v[230:231], v9 offset:10240\n\t"
      "v_mfma_f32_16x16x32_bf16 v[44:47], v[220:223], v[116:119], v[44:47]\n\t"
      "ds_read_b64_tr_b16 v[232:233], v10 offset:8192\n\t"
      "buffer_load_dwordx4 v[184:187], %[vXsrc], %[srdWS], s50 offen offset:0 sc1\n\t"
      "v_mfma_f32_16x16x32_bf16 v[40:43], v[216:219], v[116:119], v[40:43]\n\t"
      "ds_read_b64_tr_b16 v[234:235], v10 offset:10240\n\t"
      "v_mfma_f32_16x16x32_bf16 v[36:39], v[212:215], v[116:119], v[36:39]\n\t"
      "ds_read_b64_tr_b16 v[236:237], v11 offset:8192\n\t"
      "buffer_load_dwordx4 v[188:191], %[vXsrc], %[srdWS], s50 offen offset:256 sc1\n\t"
      "s_add_u32 s40, s40, 4096\n\t"
      "v_mfma_f32_16x16x32_bf16 v[32:35], v[208:211], v[116:119], v[32:35]\n\t"
      "ds_read_b64_tr_b16 v[238:239], v11 offset:10240\n\t"
      "v_mfma_f32_16x16x32_bf16 v[48:51], v[208:211], v[120:123], v[48:51]\n\t"
      "v_mfma_f32_16x16x32_bf16 v[52:55], v[212:215], v[120:123], v[52:55]\n\t"
      "v_mfma_f32_16x16x32_bf16 v[56:59], v[216:219], v[120:123], v[56:59]\n\t"
      "v_mfma_f32_16x16x32_bf16 v[60:63], v[220:223], v[120:123], v[60:63]\n\t"
      "v_mfma_f32_16x16x32_bf16 v[76:79], v[220:223], v[124:127], v[76:79]\n\t"
      "v_mfma_f32_16x16x32_bf16 v[72:75], v[216:219], v[124:127], v[72:75]\n\t"
      "v_mfma_f32_16x16x32_bf16 v[68:71], v[212:215], v[124:127], v[68:71]\n\t"
      "v_mfma_f32_16x16x32_bf16 v[64:67], v[208:211], v[124:127], v[64:67]\n\t"
      "s_waitcnt vmcnt(12) lgkmcnt(0)\n\t"
      "v_mfma_f32_16x16x32_bf16 v[16:19], v[224:227], v[128:131], v[16:19]\n\t"
      "v_add_u32 v8, 4294934528, v8\n\t"
      "v_add_u32 v9, 4294934528, v9\n\t"
      "v_add_u32 v10, 4294934528, v10\n\t"
      "v_add_u32 v11, 4294934528, v11\n\t"
      "ds_read_b64_tr_b16 v[208:209], v8 offset:0\n\t"
      "buffer_load_dwordx4 v[192:195], %[vXsrc], %[srdWS], s40 offen offset:0 sc1\n\t"
      "v_mfma_f32_16x16x32_bf16 v[20:23], v[228:231], v[128:131], v[20:23]\n\t"
      "ds_read_b64_tr_b16 v[210:211], v8 offset:2048\n\t"
      "v_mfma_f32_16x16x32_bf16 v[24:27], v[232:235], v[128:131], v[24:27]\n\t"
      "ds_read_b64_tr_b16 v[212:213], v9 offset:0\n\t"
      "s_add_u32 s50, s40, %[sXo]\n\t"
      "buffer_load_dwordx4 v[196:199], %[vXsrc], %[srdWS], s40 offen offset:256 sc1\n\t"
      "v_mfma_f32_16x16x32_bf16 v[28:31], v[236:239], v[128:131], v[28:31]\n\t"
      "ds_read_b64_tr_b16 v[214:215], v9 offset:2048\n\t"
      "v_mfma_f32_16x16x32_bf16 v[44:47], v[236:239], v[132:135], v[44:47]\n\t"
      "ds_read_b64_tr_b16 v[216:217], v10 offset:0\n\t"
      "buffer_load_dwordx4 v[200:203], %[vXsrc], %[srdWS], s50 offen offset:0 sc1\n\t"
      "v_mfma_f32_16x16x32_bf16 v[40:43], v[232:235], v[132:135], v[40:43]\n\t"
      "ds_read_b64_tr_b16 v[218:219], v10 offset:2048\n\t"
      "v_mfma_f32_16x16x32_bf16 v[36:39], v[228:231], v[132:135], v[36:39]\n\t"
      "ds_read_b64_tr_b16 v[220:221], v11 offset:0\n\t"
      "buffer_load_dwordx4 v[204:207], %[vXsrc], %[srdWS], s50 offen offset:256 sc1\n\t"
      "s_add_u32 s40, s40, 4096\n\t"
      "v_mfma_f32_16x16x32_bf16 v[32:35], v[224:227], v[132:135], v[32:35]\n\t"
      "ds_read_b64_tr_b16 v[222:223], v11 offset:2048\n\t"
      "v_mfma_f32_16x16x32_bf16 v[48:51], v[224:227], v[136:139], v[48:51]\n\t"
      "v_mfma_f32_16x16x32_bf16 v[52:55], v[228:231], v[136:139], v[52:55]\n\t"
      "v_mfma_f32_16x16x32_bf16 v[56:59], v[232:235], v[136:139], v[56:59]\n\t"
      "v_mfma_f32_16x16x32_bf16 v[60:63], v[236:239], v[136:139], v[60:63]\n\t"
      "v_mfma_f32_16x16x32_bf16 v[76:79], v[236:239], v[140:143], v[76:79]\n\t"
      "v_mfma_f32_16x16x32_bf16 v[72:75], v[232:235], v[140:143], v[72:75]\n\t"
      "v_mfma_f32_16x16x32_bf16 v[68:71], v[228:231], v[140:143], v[68:71]\n\t"
      "v_mfma_f32_16x16x32_bf16 v[64:67], v[224:227], v[140:143], v[64:67]\n\t"
      "s_waitcnt vmcnt(12) lgkmcnt(0)\n\t"
      "v_mfma_f32_16x16x32_bf16 v[16:19], v[208:211], v[144:147], v[16:19]\n\t"
      "ds_read_b64_tr_b16 v[224:225], v8 offset:8192\n\t"
      "buffer_load_dwordx4 v[80:83], %[vXsrc], %[srdWS], s40 offen offset:0 sc1\n\t"
      "v_mfma_f32_16x16x32_bf16 v[20:23], v[212:215], v[144:147], v[20:23]\n\t"
      "ds_read_b64_tr_b16 v[226:227], v8 offset:10240\n\t"
      "v_mfma_f32_16x16x32_bf16 v[24:27], v[216:219], v[144:147], v[24:27]\n\t"
      "ds_read_b64_tr_b16 v[228:229], v9 offset:8192\n\t"
      "s_add_u32 s50, s40, %[sXo]\n\t"
      "buffer_load_dwordx4 v[84:87], %[vXsrc], %[srdWS], s40 offen offset:256 sc1\n\t"
      "v_mfma_f32_16x16x32_bf16 v[28:31], v[220:223], v[144:147], v[28:31]\n\t"
      "ds_read_b64_tr_b16 v[230:231], v9 offset:10240\n\t"
      "v_mfma_f32_16x16x32_bf16 v[44:47], v[220:223], v[148:151], v[44:47]\n\t"
      "ds_read_b64_tr_b16 v[232:233], v10 offset:8192\n\t"
      "buffer_load_dwordx4 v[88:91], %[vXsrc], %[srdWS], s50 offen offset:0 sc1\n\t"
      "v_mfma_f32_16x16x32_bf16 v[40:43], v[216:219], v[148:151], v[40:43]\n\t"
      "ds_read_b64_tr_b16 v[234:235], v10 offset:10240\n\t"
      "v_mfma_f32_16x16x32_bf16 v[36:39], v[212:215], v[148:151], v[36:39]\n\t"
      "ds_read_b64_tr_b16 v[236:237], v11 offset:8192\n\t"
      "buffer_load_dwordx4 v[92:95], %[vXsrc], %[srdWS], s50 offen offset:256 sc1\n\t"
      "s_add_u32 s40, s40, 4096\n\t"
      "v_mfma_f32_16x16x32_bf16 v[32:35], v[208:211], v[148:151], v[32:35]\n\t"
      "ds_read_b64_tr_b16 v[238:239], v11 offset:10240\n\t"
      "v_mfma_f32_16x16x32_bf16 v[48:51], v[208:211], v[152:155], v[48:51]\n\t"
      "v_mfma_f32_16x16x32_bf16 v[52:55], v[212:215], v[152:155], v[52:55]\n\t"
      "v_mfma_f32_16x16x32_bf16 v[56:59], v[216:219], v[152:155], v[56:59]\n\t"
      "v_mfma_f32_16x16x32_bf16 v[60:63], v[220:223], v[152:155], v[60:63]\n\t"
      "v_mfma_f32_16x16x32_bf16 v[76:79], v[220:223], v[156:159], v[76:79]\n\t"
      "v_mfma_f32_16x16x32_bf16 v[72:75], v[216:219], v[156:159], v[72:75]\n\t"
      "v_mfma_f32_16x16x32_bf16 v[68:71], v[212:215], v[156:159], v[68:71]\n\t"
      "v_mfma_f32_16x16x32_bf16 v[64:67], v[208:211], v[156:159], v[64:67]\n\t"
      "s_waitcnt vmcnt(12) lgkmcnt(0)\n\t"
      "v_mfma_f32_16x16x32_bf16 v[16:19], v[224:227], v[160:163], v[16:19]\n\t"
      "ds_read_b64_tr_b16 v[208:209], v8 offset:16384\n\t"
      "buffer_load_dwordx4 v[96:99], %[vXsrc], %[srdWS], s40 offen offset:0 sc1\n\t"
      "v_mfma_f32_16x16x32_bf16 v[20:23], v[228:231], v[160:163], v[20:23]\n\t"
      "ds_read_b64_tr_b16 v[210:211], v8 offset:18432\n\t"
      "v_mfma_f32_16x16x32_bf16 v[24:27], v[232:235], v[160:163], v[24:27]\n\t"
      "ds_read_b64_tr_b16 v[212:213], v9 offset:16384\n\t"
      "s_add_u32 s50, s40, %[sXo]\n\t"
      "buffer_load_dwordx4 v[100:103], %[vXsrc], %[srdWS], s40 offen offset:256 sc1\n\t"
      "v_mfma_f32_16x16x32_bf16 v[28:31], v[236:239], v[160:163], v[28:31]\n\t"
      "ds_read_b64_tr_b16 v[214:215], v9 offset:18432\n\t"
      "v_mfma_f32_16x16x32_bf16 v[44:47], v[236:239], v[164:167], v[44:47]\n\t"
      "ds_read_b64_tr_b16 v[216:217], v10 offset:16384\n\t"
      "buffer_load_dwordx4 v[104:107], %[vXsrc], %[srdWS], s50 offen offset:0 sc1\n\t"
      "v_mfma_f32_16x16x32_bf16 v[40:43], v[232:235], v[164:167], v[40:43]\n\t"
      "ds_read_b64_tr_b16 v[218:219], v10 offset:18432\n\t"
      "v_mfma_f32_16x16x32_bf16 v[36:39], v[228:231], v[164:167], v[36:39]\n\t"
      "ds_read_b64_tr_b16 v[220:221], v11 offset:16384\n\t"
      "buffer_load_dwordx4 v[108:111], %[vXsrc], %[srdWS], s50 offen offset:256 sc1\n\t"
      "s_add_u32 s40, s40, 4096\n\t"
      "v_mfma_f32_16x16x32_bf16 v[32:35], v[224:227], v[164:167], v[32:35]\n\t"
      "ds_read_b64_tr_b16 v[222:223], v11 offset:18432\n\t"
      "v_mfma_f32_16x16x32_bf16 v[48:51], v[224:227], v[168:171], v[48:51]\n\t"
      "v_mfma_f32_16x16x32_bf16 v[52:55], v[228:231], v[168:171], v[52:55]\n\t"
      "v_mfma_f32_16x16x32_bf16 v[56:59], v[232:235], v[168:171], v[56:59]\n\t"
      "v_mfma_f32_16x16x32_bf16 v[60:63], v[236:239], v[168:171], v[60:63]\n\t"
      "v_mfma_f32_16x16x32_bf16 v[76:79], v[236:239], v[172:175], v[76:79]\n\t"
      "v_mfma_f32_16x16x32_bf16 v[72:75], v[232:235], v[172:175], v[72:75]\n\t"
      "v_mfma_f32_16x16x32_bf16 v[68:71], v[228:231], v[172:175], v[68:71]\n\t"
      "v_mfma_f32_16x16x32_bf16 v[64:67], v[224:227], v[172:175], v[64:67]\n\t"
      "s_waitcnt vmcnt(12) lgkmcnt(0)\n\t"
      "v_mfma_f32_16x16x32_bf16 v[16:19], v[208:211], v[176:179], v[16:19]\n\t"
      "ds_read_b64_tr_b16 v[224:225], v8 offset:24576\n\t"
      "v_mfma_f32_16x16x32_bf16 v[20:23], v[212:215], v[176:179], v[20:23]\n\t"
      "ds_read_b64_tr_b16 v[226:227], v8 offset:26624\n\t"
      "v_mfma_f32_16x16x32_bf16 v[24:27], v[216:219], v[176:179], v[24:27]\n\t"
      "ds_read_b64_tr_b16 v[228:229], v9 offset:24576\n\t"
      "v_mfma_f32_16x16x32_bf16 v[28:31], v[220:223], v[176:179], v[28:31]\n\t"
      "ds_read_b64_tr_b16 v[230:231], v9 offset:26624\n\t"
      "v_mfma_f32_16x16x32_bf16 v[44:47], v[220:223], v[180:183], v[44:47]\n\t"
      "ds_read_b64_tr_b16 v[232:233], v10 offset:24576\n\t"
      "v_mfma_f32_16x16x32_bf16 v[40:43], v[216:219], v[180:183], v[40:43]\n\t"
      "ds_read_b64_tr_b16 v[234:235], v10 offset:26624\n\t"
      "v_mfma_f32_16x16x32_bf16 v[36:39], v[212:215], v[180:183], v[36:39]\n\t"
      "ds_read_b64_tr_b16 v[236:237], v11 offset:24576\n\t"
      "v_mfma_f32_16x16x32_bf16 v[32:35], v[208:211], v[180:183], v[32:35]\n\t"
      "ds_read_b64_tr_b16 v[238:239], v11 offset:26624\n\t"
      "v_mfma_f32_16x16x32_bf16 v[48:51], v[208:211], v[184:187], v[48:51]\n\t"
      "v_mfma_f32_16x16x32_bf16 v[52:55], v[212:215], v[184:187], v[52:55]\n\t"
      "v_mfma_f32_16x16x32_bf16 v[56:59], v[216:219], v[184:187], v[56:59]\n\t"
      "v_mfma_f32_16x16x32_bf16 v[60:63], v[220:223], v[184:187], v[60:63]\n\t"
      "v_mfma_f32_16x16x32_bf16 v[76:79], v[220:223], v[188:191], v[76:79]\n\t"
      "v_mfma_f32_16x16x32_bf16 v[72:75], v[216:219], v[188:191], v[72:75]\n\t"
      "v_mfma_f32_16x16x32_bf16 v[68:71], v[212:215], v[188:191], v[68:71]\n\t"
      "v_mfma_f32_16x16x32_bf16 v[64:67], v[208:211], v[188:191], v[64:67]\n\t"
      "s_waitcnt vmcnt(8) lgkmcnt(0)\n\t"
      "v_mfma_f32_16x16x32_bf16 v[16:19], v[224:227], v[192:195], v[16:19]\n\t"
      "v_add_u32 v8, 65536, v8\n\t"
      "v_add_u32 v9, 65536, v9\n\t"
      "v_add_u32 v10, 65536, v10\n\t"
      "v_add_u32 v11, 65536, v11\n\t"
      "ds_read_b64_tr_b16 v[208:209], v8 offset:0\n\t"
      "v_mfma_f32_16x16x32_bf16 v[20:23], v[228:231], v[192:195], v[20:23]\n\t"
      "ds_read_b64_tr_b16 v[210:211], v8 offset:2048\n\t"
      "v_mfma_f32_16x16x32_bf16 v[24:27], v[232:235], v[192:195], v[24:27]\n\t"
      "ds_read_b64_tr_b16 v[212:213], v9 offset:0\n\t"
      "v_mfma_f32_16x16x32_bf16 v[28:31], v[236:239], v[192:195], v[28:31]\n\t"
      "ds_read_b64_tr_b16 v[214:215], v9 offset:2048\n\t"
      "v_mfma_f32_16x16x32_bf16 v[44:47], v[236:239], v[196:199], v[44:47]\n\t"
      "ds_read_b64_tr_b16 v[216:217], v10 offset:0\n\t"
      "v_mfma_f32_16x16x32_bf16 v[40:43], v[232:235], v[196:199], v[40:43]\n\t"
      "ds_read_b64_tr_b16 v[218:219], v10 offset:2048\n\t"
      "v_mfma_f32_16x16x32_bf16 v[36:39], v[228:231], v[196:199], v[36:39]\n\t"
      "ds_read_b64_tr_b16 v[220:221], v11 offset:0\n\t"
      "v_mfma_f32_16x16x32_bf16 v[32:35], v[224:227], v[196:199], v[32:35]\n\t"
      "ds_read_b64_tr_b16 v[222:223], v11 offset:2048\n\t"
      "v_mfma_f32_16x16x32_bf16 v[48:51], v[224:227], v[200:203], v[48:51]\n\t"
      "v_mfma_f32_16x16x32_bf16 v[52:55], v[228:231], v[200:203], v[52:55]\n\t"
      "v_mfma_f32_16x16x32_bf16 v[56:59], v[232:235], v[200:203], v[56:59]\n\t"
      "v_mfma_f32_16x16x32_bf16 v[60:63], v[236:239], v[200:203], v[60:63]\n\t"
      "v_mfma_f32_16x16x32_bf16 v[76:79], v[236:239], v[204:207], v[76:79]\n\t"
      "v_mfma_f32_16x16x32_bf16 v[72:75], v[232:235], v[204:207], v[72:75]\n\t"
      "v_mfma_f32_16x16x32_bf16 v[68:71], v[228:231], v[204:207], v[68:71]\n\t"
      "v_mfma_f32_16x16x32_bf16 v[64:67], v[224:227], v[204:207], v[64:67]\n\t"
      "s_waitcnt vmcnt(4) lgkmcnt(0)\n\t"
      "v_mfma_f32_16x16x32_bf16 v[16:19], v[208:211], v[80:83], v[16:19]\n\t"
      "v_add_u32 v8, 4294868992, v8\n\t"
      "v_add_u32 v9, 4294868992, v9\n\t"
      "v_add_u32 v10, 4294868992, v10\n\t"
      "v_add_u32 v11, 4294868992, v11\n\t"
      "ds_read_b64_tr_b16 v[224:225], v8 offset:0\n\t"
      "v_mfma_f32_16x16x32_bf16 v[20:23], v[212:215], v[80:83], v[20:23]\n\t"
      "ds_read_b64_tr_b16 v[226:227], v8 offset:2048\n\t"
      "v_mfma_f32_16x16x32_bf16 v[24:27], v[216:219], v[80:83], v[24:27]\n\t"
      "ds_read_b64_tr_b16 v[228:229], v9 offset:0\n\t"
      "v_mfma_f32_16x16x32_bf16 v[28:31], v[220:223], v[80:83], v[28:31]\n\t"
      "ds_read_b64_tr_b16 v[230:231], v9 offset:2048\n\t"
      "v_mfma_f32_16x16x32_bf16 v[44:47], v[220:223], v[84:87], v[44:47]\n\t"
      "ds_read_b64_tr_b16 v[232:233], v10 offset:0\n\t"
      "v_mfma_f32_16x16x32_bf16 v[40:43], v[216:219], v[84:87], v[40:43]\n\t"
      "ds_read_b64_tr_b16 v[234:235], v10 offset:2048\n\t"
      "v_mfma_f32_16x16x32_bf16 v[36:39], v[212:215], v[84:87], v[36:39]\n\t"
      "ds_read_b64_tr_b16 v[236:237], v11 offset:0\n\t"
      "v_mfma_f32_16x16x32_bf16 v[32:35], v[208:211], v[84:87], v[32:35]\n\t"
      "ds_read_b64_tr_b16 v[238:239], v11 offset:2048\n\t"
      "v_mfma_f32_16x16x32_bf16 v[48:51], v[208:211], v[88:91], v[48:51]\n\t"
      "v_lshlrev_b32 v144, 16, v240\n\t"
      "v_and_b32 v145, 0xffff0000, v240\n\t"
      "v_mfma_f32_16x16x32_bf16 v[52:55], v[212:215], v[88:91], v[52:55]\n\t"
      "v_lshlrev_b32 v146, 16, v241\n\t"
      "v_and_b32 v147, 0xffff0000, v241\n\t"
      "v_mfma_f32_16x16x32_bf16 v[56:59], v[216:219], v[88:91], v[56:59]\n\t"
      "v_lshlrev_b32 v148, 16, v242\n\t"
      "v_and_b32 v149, 0xffff0000, v242\n\t"
      "v_mfma_f32_16x16x32_bf16 v[60:63], v[220:223], v[88:91], v[60:63]\n\t"
      "v_lshlrev_b32 v150, 16, v243\n\t"
      "v_and_b32 v151, 0xffff0000, v243\n\t"
      "v_mfma_f32_16x16x32_bf16 v[76:79], v[220:223], v[92:95], v[76:79]\n\t"
      "v_lshlrev_b32 v152, 16, v244\n\t"
      "v_and_b32 v153, 0xffff0000, v244\n\t"
      "v_mfma_f32_16x16x32_bf16 v[72:75], v[216:219], v[92:95], v[72:75]\n\t"
      "v_lshlrev_b32 v154, 16, v245\n\t"
      "v_and_b32 v155, 0xffff0000, v245\n\t"
      "v_mfma_f32_16x16x32_bf16 v[68:71], v[212:215], v[92:95], v[68:71]\n\t"
      "v_lshlrev_b32 v156, 16, v246\n\t"
      "v_and_b32 v157, 0xffff0000, v246\n\t"
      "v_mfma_f32_16x16x32_bf16 v[64:67], v[208:211], v[92:95], v[64:67]\n\t"
      "v_lshlrev_b32 v158, 16, v247\n\t"
      "v_and_b32 v159, 0xffff0000, v247\n\t"
      "v_lshlrev_b32 v160, 16, v248\n\t"
      "v_and_b32 v161, 0xffff0000, v248\n\t"
      "v_lshlrev_b32 v162, 16, v249\n\t"
      "v_and_b32 v163, 0xffff0000, v249\n\t"
      "v_lshlrev_b32 v164, 16, v250\n\t"
      "v_and_b32 v165, 0xffff0000, v250\n\t"
      "v_lshlrev_b32 v166, 16, v251\n\t"
      "v_and_b32 v167, 0xffff0000, v251\n\t"
      "v_lshlrev_b32 v168, 16, v252\n\t"
      "v_and_b32 v169, 0xffff0000, v252\n\t"
      "v_lshlrev_b32 v170, 16, v253\n\t"
      "v_and_b32 v171, 0xffff0000, v253\n\t"
      "v_lshlrev_b32 v172, 16, v254\n\t"
      "v_and_b32 v173, 0xffff0000, v254\n\t"
      "v_lshlrev_b32 v174, 16, v255\n\t"
      "v_and_b32 v175, 0xffff0000, v255\n\t"
      "s_waitcnt vmcnt(0) lgkmcnt(0)\n\t"
      "v_mfma_f32_16x16x32_bf16 v[48:51], v[224:227], v[104:107], v[48:51]\n\t"
      "s_lshl_b32 s43, s42, 2\n\t"
      "v_mfma_f32_16x16x32_bf16 v[52:55], v[228:231], v[104:107], v[52:55]\n\t"
      "s_add_u32 s43, s43, 106496\n\t"
      "v_mfma_f32_16x16x32_bf16 v[56:59], v[232:235], v[104:107], v[56:59]\n\t"
      "v_add_u32 v8, s43, %[vL16]\n\t"
      "v_mfma_f32_16x16x32_bf16 v[60:63], v[236:239], v[104:107], v[60:63]\n\t"
      "s_xor_b32 s43, s42, 2048\n\t"
      "v_mfma_f32_16x16x32_bf16 v[76:79], v[236:239], v[108:111], v[76:79]\n\t"
      "s_lshl_b32 s43, s43, 2\n\t"
      "v_mfma_f32_16x16x32_bf16 v[72:75], v[232:235], v[108:111], v[72:75]\n\t"
      "s_add_u32 s43, s43, 106496\n\t"
      "v_mfma_f32_16x16x32_bf16 v[68:71], v[228:231], v[108:111], v[68:71]\n\t"
      "v_add_u32 v9, s43, %[vL16]\n\t"
      "v_mfma_f32_16x16x32_bf16 v[64:67], v[224:227], v[108:111], v[64:67]\n\t"
      "s_nop 0\n\t"
      "v_mfma_f32_16x16x32_bf16 v[16:19], v[224:227], v[96:99], v[16:19]\n\t"
      "ds_write_b128 v8, v[48:51] offset:0\n\t"
      "v_mfma_f32_16x16x32_bf16 v[20:23], v[228:231], v[96:99], v[20:23]\n\t"
      "ds_write_b128 v8, v[52:55] offset:1024\n\t"
      "v_mfma_f32_16x16x32_bf16 v[24:27], v[232:235], v[96:99], v[24:27]\n\t"
      "ds_write_b128 v8, v[56:59] offset:2048\n\t"
      "v_mfma_f32_16x16x32_bf16 v[28:31], v[236:239], v[96:99], v[28:31]\n\t"
      "ds_write_b128 v8, v[60:63] offset:3072\n\t"
      "v_mfma_f32_16x16x32_bf16 v[44:47], v[236:239], v[100:103], v[44:47]\n\t"
      "ds_write_b128 v8, v[76:79] offset:7168\n\t"
      "v_mfma_f32_16x16x32_bf16 v[40:43], v[232:235], v[100:103], v[40:43]\n\t"
      "ds_write_b128 v8, v[72:75] offset:6144\n\t"
      "v_mfma_f32_16x16x32_bf16 v[36:39], v[228:231], v[100:103], v[36:39]\n\t"
      "ds_write_b128 v8, v[68:71] offset:5120\n\t"
      "v_mfma_f32_16x16x32_bf16 v[32:35], v[224:227], v[100:103], v[32:35]\n\t"
      "ds_write_b128 v8, v[64:67] offset:4096\n\t"
      "s_waitcnt lgkmcnt(0)\n\t"
      "s_barrier\n\t"
      "ds_read_b128 v[48:51], v9 offset:0\n\t"
      "ds_read_b128 v[52:55], v9 offset:1024\n\t"
      "ds_read_b128 v[56:59], v9 offset:2048\n\t"
      "ds_read_b128 v[60:63], v9 offset:3072\n\t"
      "ds_read_b128 v[64:67], v9 offset:4096\n\t"
      "ds_read_b128 v[68:71], v9 offset:5120\n\t"
      "ds_read_b128 v[72:75], v9 offset:6144\n\t"
      "ds_read_b128 v[76:79], v9 offset:7168\n\t"
      "v_bfe_u32 v12, %[vL16], 7, 1\n\t"
      "v_mul_u32_u24 v12, 16320, v12\n\t"
      "v_sub_u32 v12, %[vO], v12\n\t"
      "s_waitcnt lgkmcnt(6)\n\t"
      "v_pk_add_f32 v[16:17], v[16:17], v[48:49]\n\t"
      "v_pk_add_f32 v[18:19], v[18:19], v[50:51]\n\t"
      "v_pk_add_f32 v[20:21], v[20:21], v[52:53]\n\t"
      "v_pk_add_f32 v[22:23], v[22:23], v[54:55]\n\t"
      "s_nop 0\n\t"
      "v_permlane16_swap_b32 v16, v20\n\t"
      "v_permlane16_swap_b32 v17, v21\n\t"
      "v_permlane16_swap_b32 v18, v22\n\t"
      "v_permlane16_swap_b32 v19, v23\n\t"
      "s_nop 0\n\t"
      "v_pk_add_f32 v[16:17], v[16:17], v[144:145]\n\t"
      "v_pk_add_f32 v[18:19], v[18:19], v[146:147]\n\t"
      "v_pk_add_f32 v[20:21], v[20:21], v[148:149]\n\t"
      "v_pk_add_f32 v[22:23], v[22:23], v[150:151]\n\t"
      "v_cvt_pk_bf16_f32 v176, v16, v17\n\t"
      "v_cvt_pk_bf16_f32 v177, v18, v19\n\t"
      "v_cvt_pk_bf16_f32 v178, v20, v21\n\t"
      "v_cvt_pk_bf16_f32 v179, v22, v23\n\t"
      "s_waitcnt lgkmcnt(4)\n\t"
      "v_pk_add_f32 v[24:25], v[24:25], v[56:57]\n\t"
      "v_pk_add_f32 v[26:27], v[26:27], v[58:59]\n\t"
      "v_pk_add_f32 v[28:29], v[28:29], v[60:61]\n\t"
      "v_pk_add_f32 v[30:31], v[30:31], v[62:63]\n\t"
      "s_nop 0\n\t"
      "v_permlane16_swap_b32 v24, v28\n\t"
      "v_permlane16_swap_b32 v25, v29\n\t"
      "v_permlane16_swap_b32 v26, v30\n\t"
      "v_permlane16_swap_b32 v27, v31\n\t"
      "s_nop 0\n\t"
      "v_pk_add_f32 v[24:25], v[24:25], v[152:153]\n\t"
      "v_pk_add_f32 v[26:27], v[26:27], v[154:155]\n\t"
      "v_pk_add_f32 v[28:29], v[28:29], v[156:157]\n\t"
      "v_pk_add_f32 v[30:31], v[30:31], v[158:159]\n\t"
      "v_cvt_pk_bf16_f32 v180, v24, v25\n\t"
      "v_cvt_pk_bf16_f32 v181, v26, v27\n\t"
      "v_cvt_pk_bf16_f32 v182, v28, v29\n\t"
      "v_cvt_pk_bf16_f32 v183, v30, v31\n\t"
      "v_mov_b32 v208, v176\n\t"
      "v_mov_b32 v209, v177\n\t"
      "v_mov_b32 v210, v178\n\t"
      "v_mov_b32 v211, v179\n\t"
      "v_mov_b32_dpp v176, v180 row_ror:8 row_mask:0xf bank_mask:0xc\n\t"
      "v_mov_b32_dpp v177, v181 row_ror:8 row_mask:0xf bank_mask:0xc\n\t"
      "v_mov_b32_dpp v178, v182 row_ror:8 row_mask:0xf bank_mask:0xc\n\t"
      "v_mov_b32_dpp v179, v183 row_ror:8 row_mask:0xf bank_mask:0xc\n\t"
      "v_mov_b32_dpp v180, v208 row_ror:8 row_mask:0xf bank_mask:0x3\n\t"
      "v_mov_b32_dpp v181, v209 row_ror:8 row_mask:0xf bank_mask:0x3\n\t"
      "v_mov_b32_dpp v182, v210 row_ror:8 row_mask:0xf bank_mask:0x3\n\t"
      "v_mov_b32_dpp v183, v211 row_ror:8 row_mask:0xf bank_mask:0x3\n\t"
      "s_mov_b32 s43, 16384\n\t"
      "buffer_store_dwordx4 v[176:179], v12, %[srdO], 0 offen sc1\n\t"
      "buffer_store_dwordx4 v[180:183], v12, %[srdO], s43 offen sc1\n\t"
      "s_waitcnt lgkmcnt(2)\n\t"
      "v_pk_add_f32 v[32:33], v[32:33], v[64:65]\n\t"
      "v_pk_add_f32 v[34:35], v[34:35], v[66:67]\n\t"
      "v_pk_add_f32 v[36:37], v[36:37], v[68:69]\n\t"
      "v_pk_add_f32 v[38:39], v[38:39], v[70:71]\n\t"
      "s_nop 0\n\t"
      "v_permlane16_swap_b32 v32, v36\n\t"
      "v_permlane16_swap_b32 v33, v37\n\t"
      "v_permlane16_swap_b32 v34, v38\n\t"
      "v_permlane16_swap_b32 v35, v39\n\t"
      "s_nop 0\n\t"
      "v_pk_add_f32 v[32:33], v[32:33], v[160:161]\n\t"
      "v_pk_add_f32 v[34:35], v[34:35], v[162:163]\n\t"
      "v_pk_add_f32 v[36:37], v[36:37], v[164:165]\n\t"
      "v_pk_add_f32 v[38:39], v[38:39], v[166:167]\n\t"
      "v_cvt_pk_bf16_f32 v184, v32, v33\n\t"
      "v_cvt_pk_bf16_f32 v185, v34, v35\n\t"
      "v_cvt_pk_bf16_f32 v186, v36, v37\n\t"
      "v_cvt_pk_bf16_f32 v187, v38, v39\n\t"
      "s_waitcnt lgkmcnt(0)\n\t"
      "v_pk_add_f32 v[40:41], v[40:41], v[72:73]\n\t"
      "v_pk_add_f32 v[42:43], v[42:43], v[74:75]\n\t"
      "v_pk_add_f32 v[44:45], v[44:45], v[76:77]\n\t"
      "v_pk_add_f32 v[46:47], v[46:47], v[78:79]\n\t"
      "s_nop 0\n\t"
      "v_permlane16_swap_b32 v40, v44\n\t"
      "v_permlane16_swap_b32 v41, v45\n\t"
      "v_permlane16_swap_b32 v42, v46\n\t"
      "v_permlane16_swap_b32 v43, v47\n\t"
      "s_nop 0\n\t"
      "v_pk_add_f32 v[40:41], v[40:41], v[168:169]\n\t"
      "v_pk_add_f32 v[42:43], v[42:43], v[170:171]\n\t"
      "v_pk_add_f32 v[44:45], v[44:45], v[172:173]\n\t"
      "v_pk_add_f32 v[46:47], v[46:47], v[174:175]\n\t"
      "v_cvt_pk_bf16_f32 v188, v40, v41\n\t"
      "v_cvt_pk_bf16_f32 v189, v42, v43\n\t"
      "v_cvt_pk_bf16_f32 v190, v44, v45\n\t"
      "v_cvt_pk_bf16_f32 v191, v46, v47\n\t"
      "v_mov_b32 v208, v184\n\t"
      "v_mov_b32 v209, v185\n\t"
      "v_mov_b32 v210, v186\n\t"
      "v_mov_b32 v211, v187\n\t"
      "v_mov_b32_dpp v184, v188 row_ror:8 row_mask:0xf bank_mask:0xc\n\t"
      "v_mov_b32_dpp v185, v189 row_ror:8 row_mask:0xf bank_mask:0xc\n\t"
      "v_mov_b32_dpp v186, v190 row_ror:8 row_mask:0xf bank_mask:0xc\n\t"
      "v_mov_b32_dpp v187, v191 row_ror:8 row_mask:0xf bank_mask:0xc\n\t"
      "v_mov_b32_dpp v188, v208 row_ror:8 row_mask:0xf bank_mask:0x3\n\t"
      "v_mov_b32_dpp v189, v209 row_ror:8 row_mask:0xf bank_mask:0x3\n\t"
      "v_mov_b32_dpp v190, v210 row_ror:8 row_mask:0xf bank_mask:0x3\n\t"
      "v_mov_b32_dpp v191, v211 row_ror:8 row_mask:0xf bank_mask:0x3\n\t"
      "s_mov_b32 s43, 49152\n\t"
      "buffer_store_dwordx4 v[184:187], v12, %[srdO], %[s32k] offen sc1\n\t"
      "buffer_store_dwordx4 v[188:191], v12, %[srdO], s43 offen sc1\n\t"
      ".Lfb_end:\n\t"
      :
      : [srdWS] "s"(srdWS), [srdR] "s"(srdR), [srdO] "s"(srdO), [sX0] "s"(gX0), [sXo] "s"(gXo), [sW0] "s"(gW0), [sM] "s"(gM), [kr] "s"(krot), [sFH] "s"(sFH),
        [wdlo] "s"(wdlo), [wdhi] "s"(wdhi), [wolo] "s"(srdWO[0]), [wohi] "s"(srdWO[1]), [tag] "s"(tag), [taglo] "s"(taglo), [taghi] "s"(taghi), [s32k] "s"(32768u),
        [sPH] "s"(sPH), [sPO] "s"(sPO), [vXsrc] "v"(vXsrc), [vWsrc] "v"(vWsrc), [vWr0] "v"(vWr), [vO] "v"(vO), [vPl] "v"(vPl), [vL16] "v"(vL16)
      : "v8","v9","v10","v11","v12","v13","v14","v15","v16","v17","v18","v19","v20","v21","v22","v23","v24","v25","v26","v27","v28","v29","v30","v31","v32","v33","v34","v35","v36","v37","v38","v39","v40","v41","v42","v43","v44","v45","v46","v47","v48","v49","v50","v51","v52","v53","v54","v55","v56","v57","v58","v59","v60","v61","v62","v63","v64","v65","v66","v67","v68","v69","v70","v71","v72","v73","v74","v75","v76","v77","v78","v79","v80","v81","v82","v83","v84","v85","v86","v87","v88","v89","v90","v91","v92","v93","v94","v95","v96","v97","v98","v99","v100","v101","v102","v103","v104","v105","v106","v107","v108","v109","v110","v111","v112","v113","v114","v115","v116","v117","v118","v119","v120","v121","v122","v123","v124","v125","v126","v127","v128","v129","v130","v131","v132","v133","v134","v135","v136","v137","v138","v139","v140","v141","v142","v143","v144","v145","v146","v147","v148","v149","v150","v151","v152","v153","v154","v155","v156","v157","v158","v159","v160","v161","v162","v163","v164","v165","v166","v167","v168","v169","v170","v171","v172","v173","v174","v175","v176","v177","v178","v179","v180","v181","v182","v183","v184","v185","v186","v187","v188","v189","v190","v191","v192","v193","v194","v195","v196","v197","v198","v199","v200","v201","v202","v203","v204","v205","v206","v207","v208","v209","v210","v211","v212","v213","v214","v215","v216","v217","v218","v219","v220","v221","v222","v223","v224","v225","v226","v227","v228","v229","v230","v231","v232","v233","v234","v235","v236","v237","v238","v239","v240","v241","v242","v243","v244","v245","v246","v247","v248","v249","v250","v251","v252","v253","v254","v255","s40","s41","s42","s43","s44","s45","s46","s47","s48","s49","s50","s51","s52","s53","s54","s55","s56","s57","s58","s59","vcc","scc","memory");

  }

}

extern "C" void kernel_launch(void* const* d_in, const int* in_sizes, int n_in, void* d_out, int out_size, void* d_ws, size_t ws_size,
                              hipStream_t stream) {
  fused_block<<<256, 512, 0, stream>>>((const char*)d_in[0], (char*)d_ws, (const char*)d_in[1], (const char*)d_in[2], (const char*)d_in[3], (const char*)d_in[4],
                                       (const char*)d_in[6], (const char*)d_in[7], (const char*)d_in[8], (char*)d_out);
}
```
